```python
import torch

def _softmax(x, axis=-1):
    return torch.softmax(x, dim=axis)
def _gelu(x):
    return torch.nn.functional.gelu(x, approximate="tanh")
def _silu(x):
    return torch.nn.functional.silu(x)
def _sigmoid(x):
    return torch.sigmoid(x)
def _layernorm(x, axis=-1, eps=1e-5):
    m = x.mean(dim=axis, keepdim=True)
    v = x.var(dim=axis, keepdim=True, unbiased=False)
    return (x - m) / torch.sqrt(v + eps)
def _scatter_add(tgt, idx, src, axis=0):
    return tgt.clone().index_add_(axis, idx, src)
def _segment_sum(src, seg, n):
    out = torch.zeros((n,) + src.shape[1:], dtype=src.dtype)
    return out.index_add_(0, seg, src)
def _pad(x, pw):
    flat = tuple(v for pair in reversed(pw) for v in pair)
    return torch.nn.functional.pad(x, flat)

kernel_name = 'sequential_preLN__linear_kernel_hedgehog__geglu_ffn__prompt'

def setup_inputs():

    _w = (lambda a: (lambda t: t.to(torch.bfloat16) if t.is_floating_point() else t)(torch.as_tensor(a)))
    def _A_setup():
        B, S, D, H = 1, 2048, 640, 32
        Dh = D // H
        return {
            "x": _w((lambda *s: torch.randn(*s, dtype=torch.bfloat16) * 0.05)(B, S, D)),
            "wqkv": _w((lambda *s: torch.randn(*s, dtype=torch.bfloat16) * 0.2)(D, 3 * D)),
            "w_phi": _w((lambda *s: torch.randn(*s, dtype=torch.bfloat16) * 0.2)(H, Dh, Dh) * (Dh ** -0.5)),
            "wo": _w((lambda *s: torch.randn(*s, dtype=torch.bfloat16) * 0.05)(D, D)),
        }
    _A_in = _A_setup()

    def _B_setup():
        B, S, D, F = 1, 2048, 640, 1728
        return {
            "x": _w((lambda *s: torch.randn(*s, dtype=torch.bfloat16) * 0.05)(B, S, D)),
            "w_gate": _w((lambda *s: torch.randn(*s, dtype=torch.bfloat16) * 0.05)(D, F)),
            "w_up": _w((lambda *s: torch.randn(*s, dtype=torch.bfloat16) * 0.05)(D, F)),
            "w_down": _w((lambda *s: torch.randn(*s, dtype=torch.bfloat16) * 0.05)(F, D)),
        }
    _B_in = _B_setup()

    return {
        "A_x": _A_in["x"],
        "A_wqkv": _A_in["wqkv"],
        "A_w_phi": _A_in["w_phi"],
        "A_wo": _A_in["wo"],
        "B_x": _B_in["x"],
        "B_w_gate": _B_in["w_gate"],
        "B_w_up": _B_in["w_up"],
        "B_w_down": _B_in["w_down"],
    }

def reference(A_x, A_wqkv, A_w_phi, A_wo, B_x, B_w_gate, B_w_up, B_w_down):
    def _A_ref(x, wqkv, w_phi, wo):
        B, S, D = x.shape
        H = 32; Dh = D // H
        qkv = (lambda x, *d: x.permute(*d))((x @ wqkv).reshape(B, S, 3, H, Dh), 2, 0, 3, 1, 4)
        q, k, v = qkv[0], qkv[1], qkv[2]
        pq = torch.einsum("bhsd,hdf->bhsf", q, w_phi)
        pk = torch.einsum("bhsd,hdf->bhsf", k, w_phi)
        phi_q = torch.concatenate([_softmax(pq, axis=-1), _softmax(-pq, axis=-1)], axis=-1)
        phi_k = torch.concatenate([_softmax(pk, axis=-1), _softmax(-pk, axis=-1)], axis=-1)
        kv = torch.einsum("bhsf,bhsd->bhfd", phi_k, v)
        z  = torch.einsum("bhsf->bhf", phi_k)
        num = torch.einsum("bhqf,bhfd->bhqd", phi_q, kv)
        den = torch.einsum("bhqf,bhf->bhq", phi_q, z)[..., None] + 1e-6
        o = num / den
        return (lambda x, *d: x.permute(*d))(o, 0, 2, 1, 3).reshape(B, S, D) @ wo

    def _B_ref(x, w_gate, w_up, w_down):
        return (_gelu(x @ w_gate) * (x @ w_up)) @ w_down

    x = A_x
    h = x + _A_ref(_layernorm(x, axis=-1), A_wqkv, A_w_phi, A_wo)
    h2 = h + _B_ref(_layernorm(h, axis=-1), B_w_gate, B_w_up, B_w_down)
    return h2
```

```cpp
#include <hip/hip_runtime.h>
#include <hip/hip_bf16.h>
typedef unsigned short u16;
__device__ __forceinline__ float b2f(u16 b){ return __uint_as_float(((unsigned)b)<<16); }
__device__ __forceinline__ u16 f2b(float f){ unsigned u=__float_as_uint(f); u += 0x7FFF + ((u>>16)&1); return (u16)(u>>16); }
constexpr int S=2048,D=640,H=32,DH=20,F=1728;
__global__ void k_ln(const u16* x, u16* y){
  int r=blockIdx.x; __shared__ float red[256];
  float s=0; for(int i=threadIdx.x;i<D;i+=256) s+=b2f(x[r*D+i]);
  red[threadIdx.x]=s; __syncthreads();
  for(int o=128;o>0;o>>=1){ if(threadIdx.x<o) red[threadIdx.x]+=red[threadIdx.x+o]; __syncthreads(); }
  float m=red[0]/D; __syncthreads();
  s=0; for(int i=threadIdx.x;i<D;i+=256){ float d=b2f(x[r*D+i])-m; s+=d*d; }
  red[threadIdx.x]=s; __syncthreads();
  for(int o=128;o>0;o>>=1){ if(threadIdx.x<o) red[threadIdx.x]+=red[threadIdx.x+o]; __syncthreads(); }
  float rs=rsqrtf(red[0]/D+1e-5f);
  for(int i=threadIdx.x;i<D;i+=256) y[r*D+i]=f2b((b2f(x[r*D+i])-m)*rs);
}
__global__ void k_gemm(const u16* A,const u16* B,u16* C,const u16* res,int M,int N,int K){
  __shared__ float As[16][17], Bs[16][17];
  int tx=threadIdx.x&15, ty=threadIdx.x>>4; int r=blockIdx.y*16+ty, c=blockIdx.x*16+tx; float acc=0;
  for(int k0=0;k0<K;k0+=16){ As[ty][tx]=b2f(A[r*K+k0+tx]); Bs[ty][tx]=b2f(B[(k0+ty)*N+c]); __syncthreads();
    for(int k=0;k<16;k++) acc+=As[ty][k]*Bs[k][tx]; __syncthreads(); }
  if(res) acc = b2f(f2b(acc)) + b2f(res[r*N+c]);
  C[r*N+c]=f2b(acc);
}
__global__ void k_phi(const u16* qkv,const u16* wphi,float* phi){
  int idx=blockIdx.x*blockDim.x+threadIdx.x; if(idx>=2*S*H) return;
  int h=idx%H, s=(idx/H)%S, w=idx/(H*S);
  float* o=phi+(size_t)idx*40;
  float mx=-1e30f,mn=1e30f;
  for(int f=0;f<DH;f++){ float a=0; for(int d=0;d<DH;d++) a+=b2f(qkv[s*3*D+w*D+h*DH+d])*b2f(wphi[h*400+d*20+f]); a=b2f(f2b(a)); o[f]=a; mx=fmaxf(mx,a); mn=fminf(mn,a);}
  float s1=0,s2=0; for(int f=0;f<DH;f++){ float p=o[f]; float e1=__expf(p-mx), e2=__expf(mn-p); o[f]=e1; o[20+f]=e2; s1+=e1; s2+=e2; }
  for(int f=0;f<DH;f++){ o[f]=b2f(f2b(o[f]/s1)); o[20+f]=b2f(f2b(o[20+f]/s2)); }
}
__global__ void k_kv(const u16* qkv,const float* phi,float* kv){
  int h=blockIdx.x; int t=threadIdx.x; if(t>=840) return; int f=t/21,d=t%21; float a=0;
  const float* pk=phi+(size_t)S*H*40;
  for(int s=0;s<S;s++){ float v= d<20? b2f(qkv[s*3*D+2*D+h*DH+d]):1.f; a+=pk[((size_t)s*H+h)*40+f]*v; }
  kv[h*840+t]=b2f(f2b(a));
}
__global__ void k_o(const float* phi,const float* kv,u16* o){
  int idx=blockIdx.x*blockDim.x+threadIdx.x; if(idx>=S*D) return; int s=idx/D, c=idx%D, h=c/20,d=c%20;
  float n=0,de=0; for(int f=0;f<40;f++){ float p=phi[((size_t)s*H+h)*40+f]; n+=p*kv[h*840+f*21+d]; de+=p*kv[h*840+f*21+20]; }
  o[idx]=f2b(n/(de+1e-6f));
}
__global__ void k_glu(const u16* g,const u16* u,u16* o){ int i=blockIdx.x*blockDim.x+threadIdx.x; if(i>=S*F) return;
  float x=b2f(g[i]); float t=tanhf(0.7978845608f*(x+0.044715f*x*x*x)); float ge=b2f(f2b(0.5f*x*(1+t))); o[i]=f2b(ge*b2f(u[i])); }
extern "C" void kernel_launch(void* const* d_in, const int* in_sizes, int n_in, void* d_out, int out_size, void* d_ws, size_t ws_size, hipStream_t stream){
  const u16* x=(const u16*)d_in[0]; const u16* wqkv=(const u16*)d_in[1]; const u16* wphi=(const u16*)d_in[2]; const u16* wo=(const u16*)d_in[3];
  const u16* wg=(const u16*)d_in[5]; const u16* wu=(const u16*)d_in[6]; const u16* wd=(const u16*)d_in[7];
  char* ws=(char*)d_ws; size_t off=0; auto al=[&](size_t b){ void* p=ws+off; off+=(b+255)&~255ull; return p; };
  u16* xn=(u16*)al(S*D*2); u16* qkv=(u16*)al(S*3*D*2); float* phi=(float*)al((size_t)2*S*H*40*4); float* kv=(float*)al(H*840*4);
  u16* o=(u16*)al(S*D*2); u16* h=(u16*)al(S*D*2); u16* hn=(u16*)al(S*D*2); u16* g=(u16*)al(S*F*2); u16* u=(u16*)al(S*F*2); u16* gu=(u16*)al(S*F*2);
  k_ln<<<S,256,0,stream>>>(x,xn);
  k_gemm<<<dim3(3*D/16,S/16),256,0,stream>>>(xn,wqkv,qkv,nullptr,S,3*D,D);
  k_phi<<<(2*S*H+255)/256,256,0,stream>>>(qkv,wphi,phi);
  k_kv<<<H,1024,0,stream>>>(qkv,phi,kv);
  k_o<<<(S*D+255)/256,256,0,stream>>>(phi,kv,o);
  k_gemm<<<dim3(D/16,S/16),256,0,stream>>>(o,wo,h,x,S,D,D);
  k_ln<<<S,256,0,stream>>>(h,hn);
  k_gemm<<<dim3(F/16,S/16),256,0,stream>>>(hn,wg,g,nullptr,S,F,D);
  k_gemm<<<dim3(F/16,S/16),256,0,stream>>>(hn,wu,u,nullptr,S,F,D);
  k_glu<<<(S*F+255)/256,256,0,stream>>>(g,u,gu);
  k_gemm<<<dim3(D/16,S/16),256,0,stream>>>(gu,wd,(u16*)d_out,h,S,D,F);
}
```

```cpp
#include <hip/hip_runtime.h>
#include <hip/hip_bf16.h>

typedef unsigned short u16;
typedef unsigned int u32;
typedef unsigned long long u64;
typedef u32 v4u __attribute__((ext_vector_type(4)));
typedef u32 v2u __attribute__((ext_vector_type(2)));
typedef float f4 __attribute__((ext_vector_type(4)));
typedef float f2 __attribute__((ext_vector_type(2)));
typedef float f16v __attribute__((ext_vector_type(16)));
typedef short s4 __attribute__((ext_vector_type(4)));
typedef short s8 __attribute__((ext_vector_type(8)));
typedef __bf16 bf2 __attribute__((ext_vector_type(2)));
#define LAS __attribute__((address_space(3)))
#define CAS __attribute__((address_space(4)))
#if defined(__HIP_DEVICE_COMPILE__)
#define NOPK __attribute__((target("no-packed-fp32-ops")))
#else
#define NOPK
#endif
#define DEV __device__ __forceinline__ NOPK
#define SB() __builtin_amdgcn_sched_barrier(0)
#define OPQ(x) asm volatile("" : "+v"(x))
typedef __amdgpu_buffer_rsrc_t rsrc_t;

#define DBG 0
#define ABL 0
#define ROT ((rank + 5 * (team & 1)) % 10)
#define XMAP 8

constexpr int SEQ = 2048, DM = 640, FF = 1728;
constexpr u32 W_EPOCH = 0;
constexpr u32 W_FS0 = 4096, W_FS2 = W_FS0 + 32768, W_FS3 = W_FS2 + 32768, W_FS4 = W_FS3 + 32768;
constexpr u32 W_FRT = W_FS4 + 131072;
constexpr u32 W_RED = W_FRT + 4096;
constexpr u32 W_STAT = W_RED + 65536;
constexpr u32 W_CSP = W_STAT + 131072;
constexpr u32 W_PART = W_CSP + 20 * 108 * 128;
constexpr u32 W_O = W_PART + 16 * 16 * 8192;
constexpr u32 W_H = W_O + 16 * 8 * 20 * 1024;
constexpr u32 W_G = W_H + 16 * 4 * 40 * 1024;
constexpr u32 W_WGU = W_G + 16 * 8 * 54 * 1024;
constexpr u32 W_WD = W_WGU + 108 * 40 * 1024;
constexpr u32 W_DBG = W_WD + 8 * 54 * 5 * 1024;
constexpr u32 W_END = W_DBG + 256 * 8 * 32 * 4;
constexpr u32 L_WPHI = 0;
constexpr u32 L_CS1 = 2048;
constexpr u32 L_CS3 = 2560;
constexpr u32 L_SYNC = 3584;
constexpr u32 L_RING = 4096, SLOT = 26624;
constexpr u32 L_WQ = L_RING + 4 * SLOT;
constexpr u32 L_RT = L_WQ;
constexpr u32 L_KV = 4096;
constexpr u32 L_IMG = L_RING + 2 * SLOT;
constexpr u32 L_WO = 8192;
constexpr u32 L_HST = 151552;
constexpr u32 L_HS = 8192, HSLOT = 20480;
constexpr u32 L_BYTES = 163840;
constexpr u32 SENT = 0xFFFFFFFFu;
constexpr u32 SPIN_CAP = 1u << 18;

DEV v4u mk_srd(const void* p, u32 bytes) { return v4u{(u32)(size_t)p, (u32)((size_t)p >> 32), bytes, 0x00020000u}; }
DEV rsrc_t mk_rsrc(const void* p, u32 bytes) { return __builtin_amdgcn_make_buffer_rsrc((void*)p, 0, bytes, 0x00020000); }
DEV void dma16(u32 voff, v4u srd, u32 soff, u32 lds) {
  asm volatile("s_mov_b32 m0, %2\n\ts_nop 0\n\tbuffer_load_dwordx4 %0, %1, %3 offen lds" ::"v"(voff), "s"(srd), "s"(lds), "s"(soff) : "memory");
}
DEV void dma16c(u32 voff, v4u srd, u32 soff, u32 lds) {
  asm volatile("s_mov_b32 m0, %2\n\ts_nop 0\n\tbuffer_load_dwordx4 %0, %1, %3 offen sc1 lds" ::"v"(voff), "s"(srd), "s"(lds), "s"(soff) : "memory");
}
#define WAITV(n) asm volatile("s_waitcnt vmcnt(" #n ")" ::: "memory")
DEV void bar() {
  asm volatile("" ::: "memory");
  __builtin_amdgcn_s_waitcnt(0xC07F);
  __builtin_amdgcn_s_barrier();
  asm volatile("" ::: "memory");
  SB();
}
DEV void bar_nw() {
  asm volatile("" ::: "memory");
  __builtin_amdgcn_s_barrier();
  asm volatile("" ::: "memory");
  SB();
}
DEV s4 trd(u32 a) { return __builtin_amdgcn_ds_read_tr16_b64_v4i16((LAS s4*)a); }
DEV s8 cat(s4 a, s4 b) { return __builtin_shufflevector(a, b, 0, 1, 2, 3, 4, 5, 6, 7); }
DEV s8 as8(v4u a) { return __builtin_bit_cast(s8, a); }
DEV s4 as4(v2u a) { return __builtin_bit_cast(s4, a); }
DEV f4 mfma16(s8 a, s8 b, f4 c) { return __builtin_amdgcn_mfma_f32_16x16x32_bf16(a, b, c, 0, 0, 0); }
DEV f4 mfma16h(s4 a, s4 b, f4 c) { return __builtin_amdgcn_mfma_f32_16x16x16bf16_1k(a, b, c, 0, 0, 0); }
DEV f16v mfma32(s8 a, s8 b, f16v c) { return __builtin_amdgcn_mfma_f32_32x32x16_bf16(a, b, c, 0, 0, 0); }
DEV u32 pk(float a, float b) { return __builtin_bit_cast(u32, __builtin_convertvector(f2{a, b}, bf2)); }
DEV float blo(u32 x) { return __builtin_bit_cast(float, x << 16); }
DEV float bhi(u32 x) { return __builtin_bit_cast(float, x & 0xFFFF0000u); }
DEV float dot2(u32 a, u32 b, float c) { return __builtin_amdgcn_fdot2_f32_bf16(__builtin_bit_cast(bf2, a), __builtin_bit_cast(bf2, b), c, false); }
DEV u32 fu(float x) { return __builtin_bit_cast(u32, x); }
DEV float uf(u32 x) { return __builtin_bit_cast(float, x); }
template <class T> DEV T lld(u32 a) { return *(LAS T*)a; }
template <class T> DEV void lst(u32 a, T v) { *(LAS T*)a = v; }
DEV v4u ld16c(rsrc_t r, u32 vo, u32 so) { return __builtin_amdgcn_raw_buffer_load_b128(r, vo, so, 16); }
DEV v2u ld8c(rsrc_t r, u32 vo, u32 so) { return __builtin_amdgcn_raw_buffer_load_b64(r, vo, so, 16); }
DEV u32 ld4c(rsrc_t r, u32 vo, u32 so) { return __builtin_amdgcn_raw_buffer_load_b32(r, vo, so, 16); }
DEV void st16c(v4u v, rsrc_t r, u32 vo, u32 so) {
  __builtin_amdgcn_raw_buffer_store_b128(v, r, vo, so, 16);
  asm volatile("s_nop 1" ::"v"(v) : "memory");
}
DEV void st16p(v4u v, rsrc_t r, u32 vo, u32 so) {
  __builtin_amdgcn_raw_buffer_store_b128(v, r, vo, so, 0);
  asm volatile("s_nop 1" ::"v"(v) : "memory");
}
DEV void st8c(v2u v, rsrc_t r, u32 vo, u32 so) { __builtin_amdgcn_raw_buffer_store_b64(v, r, vo, so, 16); }
DEV void st4c(u32 v, rsrc_t r, u32 vo, u32 so) { __builtin_amdgcn_raw_buffer_store_b32(v, r, vo, so, 16); }
DEV bool wall(bool c) { return __builtin_amdgcn_ballot_w64(!c) == 0ull; }
DEV u32 san(u32 x) { return x < 0xFFFFFFFEu ? x : 0xFFFFFFFEu; }
DEV u32 swap23(u32 k) { return (k & ~12u) | ((k & 4u) << 1) | ((k & 8u) >> 1); }

template <bool MAXOP, bool RCP> DEV void red4(float& a, float& b, float& c, float& d) {
  auto r1 = __builtin_amdgcn_permlane16_swap(fu(a), fu(b), false, false);
  auto r2 = __builtin_amdgcn_permlane16_swap(fu(c), fu(d), false, false);
  float ab = MAXOP ? __builtin_fmaxf(uf(r1[0]), uf(r1[1])) : uf(r1[0]) + uf(r1[1]);
  float cd = MAXOP ? __builtin_fmaxf(uf(r2[0]), uf(r2[1])) : uf(r2[0]) + uf(r2[1]);
  auto r3 = __builtin_amdgcn_permlane32_swap(fu(ab), fu(cd), false, false);
  float z = MAXOP ? __builtin_fmaxf(uf(r3[0]), uf(r3[1])) : uf(r3[0]) + uf(r3[1]);
  if (RCP) z = __builtin_amdgcn_rcpf(z);
  auto g1 = __builtin_amdgcn_permlane32_swap(fu(z), fu(z), false, false);
  auto g2 = __builtin_amdgcn_permlane16_swap(g1[0], g1[0], false, false);
  auto g3 = __builtin_amdgcn_permlane16_swap(g1[1], g1[1], false, false);
  a = uf(g2[0]); b = uf(g2[1]); c = uf(g3[0]); d = uf(g3[1]);
}
DEV void red2(float& a, float& b) {
  auto r1 = __builtin_amdgcn_permlane16_swap(fu(a), fu(b), false, false);
  float ab = uf(r1[0]) + uf(r1[1]);
  auto r3 = __builtin_amdgcn_permlane32_swap(fu(ab), fu(ab), false, false);
  float z = uf(r3[0]) + uf(r3[1]);
  auto g2 = __builtin_amdgcn_permlane16_swap(fu(z), fu(z), false, false);
  a = uf(g2[0]); b = uf(g2[1]);
}

struct Phi { v2u pl[2], mi[2], ex; };
DEV Phi feature_map(f4 ta, f4 tb, f4 tc, const s8 (&A0)[2], const s8 (&A1)[2]) {
  const float L2E = 1.4426950408889634f;
  const u32 c0 = pk(tc[0], tc[1]), c1 = pk(tc[2], tc[3]);
  const s8 b0 = as8(v4u{pk(ta[0], ta[1]), pk(ta[2], ta[3]), c0, c1});
  const s8 b1 = as8(v4u{pk(tb[0], tb[1]), pk(tb[2], tb[3]), c0, c1});
  const f4 z = {0.f, 0.f, 0.f, 0.f};
  f4 p0 = mfma16(A0[0], b0, z), p1 = mfma16(A0[1], b1, z);
  f4 xx = mfma16(A1[0], b0, z);
  xx = mfma16(A1[1], b1, xx);
  float p[2][5] = {{p0[0], p0[1], p0[2], p0[3], xx[0]}, {p1[0], p1[1], p1[2], p1[3], xx[1]}};
  float mx[2], nm[2];
#pragma unroll
  for (int h = 0; h < 2; ++h) {
    mx[h] = __builtin_fmaxf(__builtin_fmaxf(__builtin_fmaxf(p[h][0], p[h][1]), __builtin_fmaxf(p[h][2], p[h][3])), p[h][4]);
    nm[h] = -__builtin_fminf(__builtin_fminf(__builtin_fminf(p[h][0], p[h][1]), __builtin_fminf(p[h][2], p[h][3])), p[h][4]);
  }
  red4<true, false>(mx[0], nm[0], mx[1], nm[1]);
  float ep[2][5], em[2][5], sp[2], sm[2];
#pragma unroll
  for (int h = 0; h < 2; ++h) {
    const float a = -mx[h] * L2E, b = -nm[h] * L2E;
    sp[h] = 0.f; sm[h] = 0.f;
#pragma unroll
    for (int j = 0; j < 5; ++j) {
      ep[h][j] = __builtin_amdgcn_exp2f(__builtin_fmaf(p[h][j], L2E, a));
      em[h][j] = __builtin_amdgcn_exp2f(__builtin_fmaf(p[h][j], -L2E, b));
      sp[h] += ep[h][j]; sm[h] += em[h][j];
    }
  }
  red4<false, true>(sp[0], sm[0], sp[1], sm[1]);
  Phi o;
#pragma unroll
  for (int h = 0; h < 2; ++h) {
    o.pl[h] = v2u{pk(ep[h][0] * sp[h], ep[h][1] * sp[h]), pk(ep[h][2] * sp[h], ep[h][3] * sp[h])};
    o.mi[h] = v2u{pk(em[h][0] * sm[h], em[h][1] * sm[h]), pk(em[h][2] * sm[h], em[h][3] * sm[h])};
    o.ex[h] = pk(ep[h][4] * sp[h], em[h][4] * sm[h]);
  }
  return o;
}
DEV f4 lnfix(f4 a, f4 cs, float nmean, float rstd) {
  f4 o;
#pragma unroll
  for (int j = 0; j < 4; ++j) o[j] = __builtin_fmaf(nmean, cs[j], a[j]) * rstd;
  return o;
}
DEV void poll_flags(rsrc_t ws, u32 voff, bool active, v2u tag) {
#pragma unroll 1
  for (u32 s = 0; s < SPIN_CAP; ++s) {
    v2u v = tag;
    if (active) v = ld8c(ws, voff, 0);
    if (wall(v.x == tag.x && v.y == tag.y)) break;
    asm volatile("" ::: "memory");
  }
  asm volatile("" ::: "memory");
}
DEV void lds_wait(u32 a) {
#pragma unroll 1
  for (u32 s = 0; s < (SPIN_CAP << 4); ++s) {
    if (__builtin_amdgcn_readfirstlane(*(volatile LAS u32*)a) != 0u) break;
    __builtin_amdgcn_s_sleep(1);
  }
  asm volatile("" ::: "memory");
}
DEV void lds_set(u32 a) { *(volatile LAS u32*)a = 1u; }

#define STAMP(i) do {} while (0)

#define LAM __attribute__((always_inline)) NOPK
template <int V> struct IC { static constexpr int v = V; };
template <int A, int B, class F> DEV void sfor(F&& f) {
  if constexpr (A < B) { f(IC<A>{}); sfor<A + 1, B>(f); }
}
DEV void poll_level(rsrc_t ws, u32 voff, bool active, v2u tag, u32 level) {
#pragma unroll 1
  for (u32 s = 0; s < SPIN_CAP; ++s) {
    v2u v = {tag.x | 15u, tag.y};
    if (active) v = ld8c(ws, voff, 0);
    if (wall((v.x & ~15u) == tag.x && v.y == tag.y && (v.x & 15u) >= level)) break;
    asm volatile("" ::: "memory");
  }
  asm volatile("" ::: "memory");
}

__global__ __launch_bounds__(512, 2) NOPK void fused_block(const u16* __restrict__ x, const u16* __restrict__ wqkv, const u16* __restrict__ wphi,
                                                           const u16* __restrict__ wo, const u16* __restrict__ wg, const u16* __restrict__ wu,
                                                           const u16* __restrict__ wd, u16* __restrict__ out, char* __restrict__ wsp) {
  STAMP(0);
  asm volatile("" ::"s"(x), "s"(wqkv), "s"(wphi), "s"(wo), "s"(wg), "s"(wu), "s"(wd), "s"(out), "s"(wsp));
  const u32 tid = threadIdx.x, lane = tid & 63, w = __builtin_amdgcn_readfirstlane(tid >> 6);
  const u32 q = lane >> 4, i16 = lane & 15, tq = (lane >> 2) & 3, tp = lane & 3;
  const u32 b = blockIdx.x, xcd = b & 7, idx = b >> 3;
  const u32 team = xcd * 2 + (idx >> 4), rank = idx & 15;
  const u32 bid = team * 16 + rank;
  const rsrc_t ws = mk_rsrc(wsp, W_END);
  const v4u sx = mk_srd(x, SEQ * DM * 2), sqkv = mk_srd(wqkv, DM * 3 * DM * 2), sws = mk_srd(wsp, W_END);
  u64 ep;
  asm volatile("s_load_dwordx2 %0, %1, 0x0" : "=s"(ep) : "s"(wsp));

  st16c(v4u{SENT, SENT, SENT, SENT}, ws, lane * 16, W_PART + (rank * 16 + team) * 8192 + w * 1024);
  if (lane < 4) st8c(v2u{SENT, SENT}, ws, lane * 8, W_RED + rank * 4096 + team * 256 + w * 32);
  if (w < 2) dma16(lane * 16, mk_srd(wphi, 32 * 400 * 2), rank * 1600 + w * 1024, L_WPHI + w * 1024);
  const bool w5 = w < 5;
  u32 vx[2], vw[2];
#pragma unroll
  for (int j = 0; j < 2; ++j) {
    const u32 row = 8 * j + (lane >> 3);
    vx[j] = row * 1280 + (((lane & 7) ^ ((row >> 1) & 7)) << 4);
    const u32 pi = 128 * w + 64 * j + lane, srow = pi / 10, pc = pi % 10;
    vw[j] = swap23(srow) * 3840 + (pc < 5 ? 1280 + pc * 16 : 2560 + (pc - 5) * 16);
  }
  const u32 xs0 = (team * 128 + w * 16) * 1280;
  const u32 rot = ROT;
  auto rc = [&](int c) LAM -> u32 { const u32 v = c + rot; return v >= 10 ? v - 10 : v; };
  auto stage1 = [&](int c, int n) LAM {
    const u32 sl = L_RING + (c & 3) * SLOT, cc = rc(c);
    if (n < 2) dma16(vx[n], sx, xs0 + cc * 128, sl + w * 2048 + n * 1024);
    else if (w5) dma16(vw[n - 2], sqkv, rank * 80 + cc * (64 * 3840), sl + 16384 + w * 2048 + (n - 2) * 1024);
  };
#pragma unroll
  for (int c = 0; c < 3; ++c)
#pragma unroll
    for (int n = 0; n < 4; ++n) stage1(c, n);
  if (tid < 16) lst<u32>(L_SYNC + tid * 4, 0u);

  const u32 piece = w * 256 + b;
  const bool has_piece = piece < 1620;
  const u32 pmat = piece / 540, prem = piece % 540;
  const u32 pk_ = pmat < 2 ? prem / 27 : prem / 10, pcb = pmat < 2 ? prem % 27 : prem % 10;
  v4u rt[4];

  const f4 z4 = {0.f, 0.f, 0.f, 0.f};
  v4u onesv = {0x3F803F80u, 0x3F803F80u, 0x3F803F80u, 0x3F803F80u};
  OPQ(onesv);
  const s8 ones = as8(onesv);
  v4u xa[20];
  s8 wf1[2][12];
  f4 acc[6] = {z4, z4, z4, z4, z4, z4};
  float s1[2] = {0.f, 0.f}, s2[2] = {0.f, 0.f};
  s8 A0[2], A1[2];
  v2u tag;
  const u32 ax0 = w * 2048 + i16 * 128 + ((q ^ ((i16 >> 1) & 7)) << 4);
  const u32 rowb = 16 * (q >> 1) + 4 * (q & 1) + tq;
  const u32 aw1 = 16384 + rowb * 160 + 8 * tp, aw2 = 16384 + rowb * 160 + 32 + 40 * tp;
  const u32 awc = w == 2 ? aw2 : aw1 + (w == 0 ? 0 : w == 1 ? 40 : w == 3 ? 80 : 120);
  u32 vq;
  {
    const u32 pi = 64 * w + lane, srow = pi / 5, pc = pi % 5;
    vq = ((srow & ~7u) + 4 * (srow & 1) + ((srow >> 1) & 3)) * 3840 + pc * 16;
  }
#pragma unroll
  for (int c = 0; c <= 10; ++c) {
    u32 bx0, bx1, b1, b2, bc;
    if (c < 10) {
      if (c <= 7) { if (w5) WAITV(8); else WAITV(4); }
      else if (c == 8) { if (w5) WAITV(9); else WAITV(2); }
      else { if (w5) WAITV(10); else WAITV(0); }
      bar();
      if (c == 0) {
        STAMP(1);
        asm volatile("" : "+s"(ep));
        ep = (ep & ~15ull) + 16ull;
        tag = v2u{(u32)ep, (u32)(ep >> 32)};
      }
      const u32 sl = L_RING + (c & 3) * SLOT;
      bx0 = sl + ax0; bx1 = sl + (ax0 ^ 64); b1 = sl + aw1; b2 = sl + aw2; bc = sl + awc;
      OPQ(b1); OPQ(b2); OPQ(bc);
      if (c == 1 && tid == 448) st8c(tag, ws, 0, W_FS0 + bid * 128);
    }
#pragma unroll
    for (int m = 0; m < 12; ++m) {
      const int k2 = m / 6, t = m % 6;
      if (c < 10) {
        if (m == 0) xa[2 * c] = lld<v4u>(bx0);
        if (m == 1) xa[2 * c + 1] = lld<v4u>(bx1);
        const u32 ba = (t == 2 ? b2 : t == 5 ? bc : b1) + k2 * 5120 + (t == 1 ? 40 : t == 3 ? 80 : t == 4 ? 120 : 0);
        if (!(ABL & 4) || c == 0) wf1[c & 1][m] = cat(trd(ba), trd(ba + 1280)); else wf1[c & 1][m] = wf1[0][m];
      }
      if (c >= 1 && (!(ABL & 8) || c == 10)) {
        const v4u xv = xa[2 * (c - 1) + k2];
        acc[t] = mfma16(wf1[(c - 1) & 1][m], t < 5 ? as8(xv) : ones, acc[t]);
        if (t < 4 && !(ABL & 1)) { s1[t & 1] = dot2(xv[t], 0x3F803F80u, s1[t & 1]); s2[t & 1] = dot2(xv[t], xv[t], s2[t & 1]); OPQ(s1[t & 1]); OPQ(s2[t & 1]); }
      }
      SB();
      if (c <= 6 && m % 3 == 1 && !(ABL & 2)) stage1(c + 3, m / 3);
      if ((c == 7 || c == 8) && m % 2 == 1 && m < 10 && w5) {
        const int cq = (c - 7) * 5 + m / 2;
        dma16(vq, sqkv, rank * 80 + rc(cq) * (64 * 3840), L_WQ + cq * 5120 + w * 1024);
      }
      if (c == 9 && m == 2 && has_piece) {
        const v4u sp = mk_srd(pmat == 0 ? wg : pmat == 1 ? wu : wd, DM * FF * 2);
        const u32 rs = pmat < 2 ? 3456 : 1280, vo = (lane >> 3) * rs + (lane & 7) * 16;
#pragma unroll
        for (int n = 0; n < 4; ++n)
          asm volatile("buffer_load_dwordx4 %0, %1, %2, %3 offen" : "=v"(rt[n]) : "v"(vo), "s"(sp), "s"((pk_ * 32 + n * 8) * rs + pcb * 128) : "memory");
      }
      SB();
    }
    if (c == 0) {
#pragma unroll
      for (int h = 0; h < 2; ++h) {
        s8 a0, a1;
#pragma unroll
        for (int j = 0; j < 8; ++j) {
          const u32 d = j < 4 ? 4 * q + j : 12 + j;
          const bool ok = j < 4 || q == (u32)h;
          const short v0 = lld<short>(L_WPHI + h * 800 + d * 40 + i16 * 2);
          const short v1 = lld<short>(L_WPHI + h * 800 + d * 40 + 32 + (i16 >> 2) * 2);
          a0[j] = ok ? v0 : (short)0;
          a1[j] = (ok && (i16 & 3) == (u32)h) ? v1 : (short)0;
        }
        OPQ(a0); OPQ(a1);
        A0[h] = a0; A1[h] = a1;
      }
      SB();
    }
  }
  STAMP(2);
  if (w < 5 && i16 == 0) lst<f4>(L_CS1 + w * 64 + q * 16, acc[5]);
  float sa = s1[0] + s1[1], sb = s2[0] + s2[1];
  red2(sa, sb);
  float nmean1 = -sa * (1.0f / DM);
  float rstd1 = __builtin_amdgcn_rsqf(__builtin_fmaxf(__builtin_fmaf(-nmean1, nmean1, sb * (1.0f / DM)), 0.f) + 1e-5f);
  OPQ(nmean1); OPQ(rstd1);
  bar();
  {
    f4 t[5];
#pragma unroll
    for (int n = 0; n < 5; ++n) t[n] = lnfix(acc[n], lld<f4>(L_CS1 + n * 64 + q * 16), nmean1, rstd1);
    const Phi ph = feature_map(t[0], t[1], t[2], A0, A1);
    const u32 ib = L_IMG + (w * 16 + i16) * 264 + q * 8;
    lst<v2u>(ib + 0, ph.pl[0]); lst<v2u>(ib + 32, ph.mi[0]); lst<v2u>(ib + 64, ph.pl[1]); lst<v2u>(ib + 96, ph.mi[1]);
    lst<v2u>(ib + 128, ph.ex);
    lst<v2u>(ib + 160, v2u{pk(t[3][0], t[3][1]), pk(t[3][2], t[3][3])});
    lst<v2u>(ib + 192, v2u{pk(t[4][0], t[4][1]), pk(t[4][2], t[4][3])});
    const u32 e0 = q < 2 ? 0x3F803F80u : pk(t[2][0], t[2][1]), e1 = q < 2 ? 0x3F803F80u : pk(t[2][2], t[2][3]);
    const u32 eb = L_IMG + (w * 16 + i16) * 264 + 224 + (q ^ 2) * 2;
    lst<u16>(eb, (u16)e0); lst<u16>(eb + 8, (u16)(e0 >> 16)); lst<u16>(eb + 16, (u16)e1); lst<u16>(eb + 24, (u16)(e1 >> 16));
  }
  bar();
  STAMP(3);
  {
    const u32 a1c = w < 4 ? w * 32 : w < 6 ? (w - 4) * 32 : 128;
    const u32 b1c = w < 4 ? 160 + (w >> 1) * 32 : w == 6 ? 160 : 224;
    const u32 a2c = (w == 4 || w == 5) ? a1c + 64 : a1c;
    const u32 b2c = w == 6 ? 192 : b1c;
    const u32 rb = L_IMG + (2 * (q >> 1) + 16 * (q & 1) + 4 * tq) * 264 + 8 * tp;
    u32 ra[4] = {rb + a1c, rb + b1c, rb + a2c, rb + b2c};
    s8 fr[4][4];
#pragma unroll
    for (int n = 0; n < 4; ++n) OPQ(ra[n]);
#pragma unroll
    for (int ks = 0; ks < 4; ++ks)
#pragma unroll
      for (int n = 0; n < 4; ++n) fr[ks][n] = cat(trd(ra[n] + ks * 32 * 264), trd(ra[n] + ks * 32 * 264 + 264));
    SB();
    f4 d1 = z4, d2 = z4;
#pragma unroll
    for (int ks = 0; ks < 4; ++ks) {
      d1 = mfma16(fr[ks][0], fr[ks][1], d1);
      d2 = mfma16(fr[ks][2], fr[ks][3], d2);
    }
    const bool mlo = w != 6 && (lane & 1), mhi = w == 6 || (lane & 1);
    const v4u pv = {san(fu(mlo ? d2[0] : d1[0])), san(fu(mlo ? d2[1] : d1[1])), san(fu(mhi ? d2[2] : d1[2])), san(fu(mhi ? d2[3] : d1[3]))};
    st16c(pv, ws, lane * 16, W_PART + (rank * 16 + team) * 8192 + w * 1024);
  }
  WAITV(5);
  bar();
  STAMP(4);
  const u32 cblk = rank & 7, rhalf = rank >> 3;
  f4 qa[4] = {z4, z4, z4, z4};
  {
    const v4u swo = mk_srd(wo, DM * DM * 2);
    u32 vom, voe;
    {
      const u32 P = w * 64 + lane, srow = P / 10, pc = P % 10;
      const u32 g = 2 * ((srow >> 4) & 1) + ((srow >> 2) & 1), t = (srow >> 3) & 1, i = srow & 3;
      vom = (20 * t + 4 * g + i) * 1280 + pc * 16;
      voe = (40 * g + 20 * (i & 1) + 16 + 2 * t + (i >> 1)) * 1280 + pc * 16;
    }
    const u32 r1 = L_WQ + (8 * q + 2 * tq) * 80;
    u32 bq1 = r1 + 8 * tp, bq2 = r1 + 32 + 40 * (tp & 1);
    u32 bqc = w == 0 ? bq1 : w == 1 ? bq1 + 40 : bq2;
    OPQ(bq1); OPQ(bq2); OPQ(bqc);
    constexpr int QD = 7;
    s8 qf[QD + 1];
    auto ldq = [&](int f) LAM {
      const int t = f & 3;
      const u32 ba = (t == 2 ? bq2 : t == 3 ? bqc : bq1) + (f >> 2) * 2560 + (t == 1 ? 40 : 0);
      qf[f % (QD + 1)] = cat(trd(ba), trd(ba + 80));
    };
#pragma unroll
    for (int f = 0; f < QD; ++f) ldq(f);
    SB();
#pragma unroll
    for (int f = 0; f < 80; ++f) {
      if (f + QD < 80) ldq(f + QD);
      qa[f & 3] = mfma16(qf[f % (QD + 1)], (f & 3) < 3 ? as8(xa[f >> 2]) : ones, qa[f & 3]);
      SB();
      if ((f & 3) == 1 && w5) {
        const int ks = f >> 2;
        dma16(ks < 16 ? vom : voe, swo, cblk * 160 + (ks < 16 ? ks * 40 * 1280 : (ks - 16) * 160 * 1280), L_WO + ks * 5120 + w * 1024);
      }
      SB();
    }
    if (w < 3 && i16 == 0) lst<f4>(L_CS1 + (5 + w) * 64 + q * 16, qa[3]);
  }
  bar();
  STAMP(5);
  if (w == 7) {
    poll_flags(ws, W_FS0 + (lane * 16 + rank) * 128, lane < 16, tag);
    const u32 vo = (lane >> 5) * 8 * 8192 + (lane & 31) * 16, so = W_PART + rank * 16 * 8192 + team * 512;
    v4u pv[8];
#pragma unroll 1
    for (u32 s = 0; s < SPIN_CAP; ++s) {
      bool ok = true;
#pragma unroll
      for (int n = 0; n < 8; ++n) pv[n] = ld16c(ws, vo + n * 8192, so);
      SB();
#pragma unroll
      for (int n = 0; n < 8; ++n) ok = ok && pv[n].x != SENT && pv[n].y != SENT && pv[n].z != SENT && pv[n].w != SENT;
      if (wall(ok)) break;
      asm volatile("" ::: "memory");
    }
    float sm[4];
#pragma unroll
    for (int j = 0; j < 4; ++j) {
      float a = 0.f;
#pragma unroll
      for (int n = 0; n < 8; ++n) a += uf(pv[n][j]);
      auto r = __builtin_amdgcn_permlane32_swap(fu(a), fu(a), false, false);
      sm[j] = uf(r[0]) + uf(r[1]);
    }
    if (lane < 32) st8c(v2u{san(pk(sm[0], sm[1])), san(pk(sm[2], sm[3]))}, ws, lane * 8, W_RED + rank * 4096 + team * 256);
    STAMP(6);
  }
  Phi pq;
  {
    f4 t[3];
#pragma unroll
    for (int n = 0; n < 3; ++n) t[n] = lnfix(qa[n], lld<f4>(L_CS1 + (5 + n) * 64 + q * 16), nmean1, rstd1);
    pq = feature_map(t[0], t[1], t[2], A0, A1);
    OPQ(pq.pl[0]); OPQ(pq.pl[1]); OPQ(pq.mi[0]); OPQ(pq.mi[1]); OPQ(pq.ex);
  }
  STAMP(7);
  if (has_piece) {
    if (w5) WAITV(21); else WAITV(1);
    SB();
    asm volatile("" : "+v"(rt[0]), "+v"(rt[1]), "+v"(rt[2]), "+v"(rt[3])::"memory");
#pragma unroll
    for (int n = 0; n < 4; ++n) lst<v4u>(L_RT + w * 4096 + n * 1024 + lane * 16, rt[n]);
    asm volatile("" ::: "memory");
    const u32 rb = L_RT + w * 4096 + (8 * q + tq) * 128 + (((tp & 1) << 1) | (tp >> 1)) * 8;
    v4u f[4];
#pragma unroll
    for (int sg = 0; sg < 4; ++sg) f[sg] = __builtin_bit_cast(v4u, cat(trd(rb + sg * 32), trd(rb + sg * 32 + 512)));
    if (pmat < 2) {
      float c[4];
#pragma unroll
      for (int sg = 0; sg < 4; ++sg) {
        st16c(f[sg], ws, (q >> 1) * 1024 + (i16 + 32 * (q & 1)) * 16, W_WGU + ((4 * pcb + sg) * 40 + 2 * pk_) * 1024 + pmat * 256);
        c[sg] = 0.f;
#pragma unroll
        for (int j = 0; j < 4; ++j) c[sg] = dot2(f[sg][j], 0x3F803F80u, c[sg]);
      }
      red4<false, false>(c[0], c[1], c[2], c[3]);
      const float cm = q == 0 ? c[0] : q == 1 ? c[1] : q == 2 ? c[2] : c[3];
      st4c(fu(cm), ws, q * 128 + swap23(i16) * 4, W_CSP + (pk_ * 108 + 4 * pcb) * 128 + pmat * 64);
    } else {
#pragma unroll
      for (int sg = 0; sg < 4; ++sg) {
        const u32 sgi = 4 * pcb + sg, cc = sgi / 5, pos = sgi % 5;
        const u32 tile = pos < 4 ? (pos & 2) + (i16 >> 3) : 4, row = pos < 4 ? 8 * (pos & 1) + (i16 & 7) : swap23(i16);
        st16c(f[sg], ws, tile * 1024 + (row + 16 * q) * 16, W_WD + (cc * 54 + pk_) * 5120);
      }
    }
  }
  if (w == 7) {
    v4u kv[4];
#pragma unroll 1
    for (u32 s = 0; s < SPIN_CAP; ++s) {
      bool ok = true;
#pragma unroll
      for (int n = 0; n < 4; ++n) kv[n] = ld16c(ws, lane * 16 + n * 1024, W_RED + rank * 4096);
      SB();
#pragma unroll
      for (int n = 0; n < 4; ++n) ok = ok && kv[n].x != SENT && kv[n].y != SENT && kv[n].z != SENT && kv[n].w != SENT;
      if (wall(ok)) break;
      asm volatile("" ::: "memory");
    }
#pragma unroll
    for (int n = 0; n < 4; ++n) lst<v4u>(L_KV + n * 1024 + lane * 16, kv[n]);
  }
  bar();
  STAMP(8);
  {
    v2u it[8];
#pragma unroll
    for (int n = 0; n < 8; ++n) it[n] = lld<v2u>(L_KV + n * 512 + lane * 8);
    const s8 b0 = as8(v4u{pq.pl[0].x, pq.pl[0].y, pq.mi[0].x, pq.mi[0].y}), b1 = as8(v4u{pq.pl[1].x, pq.pl[1].y, pq.mi[1].x, pq.mi[1].y});
    const s4 bx = as4(pq.ex);
    const bool odd = lane & 1;
    f4 n0 = mfma16(as8(v4u{it[0].x, it[0].y, it[1].x, it[1].y}), b0, z4);
    f4 n1 = mfma16(as8(v4u{it[2].x, it[2].y, it[3].x, it[3].y}), b1, z4);
    const v4u am = {it[4].x, it[4].y, it[5].x, it[5].y}, zz = {0u, 0u, 0u, 0u};
    f4 nx = mfma16(as8(odd ? zz : am), b0, z4);
    n0 = mfma16h(as4(v2u{it[6].x, 0u}), bx, n0);
    n1 = mfma16h(as4(v2u{0u, it[6].y}), bx, n1);
    nx = mfma16(as8(odd ? am : zz), b1, nx);
    nx = mfma16h(as4(odd ? v2u{0u, it[7].y} : v2u{it[7].x, 0u}), bx, nx);
    const float r0 = __builtin_amdgcn_rcpf(nx[2] + 1e-6f), r1 = __builtin_amdgcn_rcpf(nx[3] + 1e-6f);
    const u32 ob = W_O + (team * 8 + w) * 20480;
    st16c(v4u{pk(n0[0] * r0, n0[1] * r0), pk(n0[2] * r0, n0[3] * r0), pk(n1[0] * r1, n1[1] * r1), pk(n1[2] * r1, n1[3] * r1)}, ws, lane * 16, ob + rank * 1024);
    st4c(pk(nx[0] * r0, nx[1] * r1), ws, i16 * 16 + q * 4, ob + (16 + (rank >> 2)) * 1024 + (rank & 3) * 256);
  }
  WAITV(0);
  bar();
  STAMP(9);
  const bool u7 = rank < 12;
  if (w < 4) {
    u32 lane = __builtin_amdgcn_mbcnt_hi(~0u, __builtin_amdgcn_mbcnt_lo(~0u, 0u));
    OPQ(lane);
    const u32 q = lane >> 4, i16 = lane & 15, tq = (lane >> 2) & 3, tp = lane & 3, rowb = 16 * (q >> 1) + 4 * (q & 1) + tq;
    if (w == 0) st8c(tag, ws, lane << 26, W_FS2 + bid * 128);
    const u32 tk = rhalf * 64 + w * 16 + i16;
    const u32 xo = tk * 1280 + q * 16, xso = team * 128 * 1280 + cblk * 160;
    {
      const rsrc_t rx = mk_rsrc(x, SEQ * DM * 2);
      const v4u xr0 = __builtin_amdgcn_raw_buffer_load_b128(rx, xo, xso, 0), xr1 = __builtin_amdgcn_raw_buffer_load_b128(rx, xo + 64, xso, 0);
      const v2u xr2 = __builtin_amdgcn_raw_buffer_load_b64(rx, tk * 1280 + 128 + q * 8, xso, 0);
      constexpr int OD = 6;
      s8 wf[OD][5];
      const u32 r1 = L_WO + rowb * 160;
      u32 bA[2], bB[2], bC[2];
      bA[0] = r1 + 16 * tp + 8 * (tp >> 1); bB[0] = r1 + 16 * tp + 8 * (1 - (tp >> 1)); bC[0] = r1 + 128 + 8 * tp;
      bA[1] = bA[0] + 51200; bB[1] = bB[0] + 51200; bC[1] = bC[0] + 51200;
      OPQ(bA[0]); OPQ(bB[0]); OPQ(bC[0]); OPQ(bA[1]); OPQ(bB[1]); OPQ(bC[1]);
      auto ldo1 = [&](int ks, int t) LAM {
        const int hb = ks / 10;
        const u32 ba = (t == 4 ? bC[hb] : (t & 1) ? bB[hb] : bA[hb]) + (ks % 10) * 5120 + ((t == 2 || t == 3) ? 64 : 0);
        wf[ks % OD][t] = cat(trd(ba), trd(ba + 1280));
      };
#pragma unroll
      for (int ks = 0; ks < OD; ++ks)
#pragma unroll
        for (int t = 0; t < 5; ++t) ldo1(ks, t);
      __builtin_amdgcn_s_waitcnt(0xC07F);
      SB();
      if (w == 0) { poll_flags(ws, W_FS2 + (team * 16 + lane) * 128, lane < 16, tag); lds_set(L_SYNC + 0); }
      else lds_wait(L_SYNC + 0);
      STAMP(10);
      v4u of[20];
      const u32 oso = W_O + (team * 8 + rhalf * 4 + w) * 20480;
      constexpr int PD = 12;
#pragma unroll
      for (int ks = 0; ks < PD; ++ks) of[ks] = ld16c(ws, lane * 16, oso + ks * 1024);
      SB();
      f4 ha[5] = {z4, z4, z4, z4, z4};
#pragma unroll
      for (int ks = 0; ks < 20; ++ks) {
#pragma unroll
        for (int t = 0; t < 5; ++t) {
          ha[t] = mfma16(wf[ks % OD][t], as8(of[ks]), ha[t]);
          if (t == 0 && ks + PD < 20) of[ks + PD] = ld16c(ws, lane * 16, oso + (ks + PD) * 1024);
          if (ks + OD < 20) ldo1(ks + OD, t);
          SB();
        }
      }
      STAMP(11);
      v4u hc[2];
      const bool sw = q >= 2;
#pragma unroll
      for (int m = 0; m < 2; ++m) {
        const f4 lo = sw ? ha[2 * m + 1] : ha[2 * m], hi = sw ? ha[2 * m] : ha[2 * m + 1];
        const v4u xr = m ? xr1 : xr0;
        hc[m] = v4u{pk(lo[0] + blo(xr.x), lo[1] + bhi(xr.x)), pk(lo[2] + blo(xr.y), lo[3] + bhi(xr.y)), pk(hi[0] + blo(xr.z), hi[1] + bhi(xr.z)), pk(hi[2] + blo(xr.w), hi[3] + bhi(xr.w))};
      }
      const v2u ht = {pk(ha[4][0] + blo(xr2.x), ha[4][1] + bhi(xr2.x)), pk(ha[4][2] + blo(xr2.y), ha[4][3] + bhi(xr2.y))};
      const u32 hb = W_H + (team * 4 + rhalf * 2 + (w >> 1)) * 40960 + cblk * 5120;
      const u32 t32 = (w & 1) * 16 + i16;
      st16c(hc[0], ws, (q >> 1) * 1024 + (t32 + 32 * (q & 1)) * 16, hb);
      st16c(hc[1], ws, (q >> 1) * 1024 + (t32 + 32 * (q & 1)) * 16, hb + 2048);
      st8c(ht, ws, (t32 + 32 * (q >> 1)) * 16 + 8 * (q & 1), hb + 4096);
      float a = 0.f, c = 0.f;
#pragma unroll
      for (int m = 0; m < 2; ++m)
#pragma unroll
        for (int j = 0; j < 4; ++j) { a = dot2(hc[m][j], 0x3F803F80u, a); c = dot2(hc[m][j], hc[m][j], c); }
#pragma unroll
      for (int j = 0; j < 2; ++j) { a = dot2(ht[j], 0x3F803F80u, a); c = dot2(ht[j], ht[j], c); }
      red2(a, c);
      if (q == 0) st8c(v2u{fu(a), fu(c)}, ws, tk * 8, W_STAT + (team * 8 + cblk) * 1024);
      lst<v4u>(L_HST + w * 2560 + lane * 16, hc[0]);
      lst<v4u>(L_HST + w * 2560 + 1024 + lane * 16, hc[1]);
      lst<v2u>(L_HST + w * 2560 + 2048 + lane * 8, ht);
    }
    WAITV(0);
    bar();
    STAMP(12);
    if (w == 0) {
      if (lane == 0) st8c(tag, ws, 0, W_FS3 + bid * 128);
      poll_flags(ws, W_FS3 + (team * 16 + lane) * 128, lane < 16, tag);
    }
    bar();
    STAMP(13);
    {
      const u32 hh = lane >> 5, t32 = lane & 31;
      v2u sv[4];
#pragma unroll
      for (int n = 0; n < 4; ++n) sv[n] = ld8c(ws, (hh * 4 + n) * 1024 + t32 * 8, W_STAT + team * 8192 + w * 256);
      v4u hf[40];
      constexpr int HD = 12;
      const u32 hso = W_H + (team * 4 + w) * 40960;
#pragma unroll
      for (int s = 0; s < HD; ++s) hf[s] = ld16c(ws, lane * 16, hso + s * 1024);
      SB();
      u32 lb[3] = {L_HS + lane * 16, L_HS + 3 * HSLOT + lane * 16, L_HS + 6 * HSLOT + lane * 16};
      OPQ(lb[0]); OPQ(lb[1]); OPQ(lb[2]);
      auto ldw = [&](int n, int s) LAM -> s8 {
        const int hs = (2 * n + s / 20 + 5) % 7;
        return lld<s8>(lb[hs / 3] + (hs % 3) * HSLOT + (s % 20) * 1024);
      };
      const u32 csb = L_CS3 + hh * 32;
      auto ldcs = [&](int n) LAM -> f16v {
        const f4 g0 = lld<f4>(csb + n * 128), g1 = lld<f4>(csb + n * 128 + 16), u0 = lld<f4>(csb + n * 128 + 64), u1 = lld<f4>(csb + n * 128 + 80);
        return __builtin_shufflevector(__builtin_shufflevector(g0, g1, 0, 1, 2, 3, 4, 5, 6, 7), __builtin_shufflevector(u0, u1, 0, 1, 2, 3, 4, 5, 6, 7), 0, 1, 2, 3, 4,
                                       5, 6, 7, 8, 9, 10, 11, 12, 13, 14, 15);
      };
      f16v ac[3];
      ac[0] = ldcs(0); ac[1] = ldcs(1); ac[2] = ldcs(2);
      constexpr int RD = 4;
      s8 rg[RD];
#pragma unroll
      for (int f = 0; f < RD; ++f) rg[f] = ldw(f & 1, f >> 1);
      SB();
      float nmean, rstd;
      {
        const float a = (uf(sv[0].x) + uf(sv[1].x)) + (uf(sv[2].x) + uf(sv[3].x)), c = (uf(sv[0].y) + uf(sv[1].y)) + (uf(sv[2].y) + uf(sv[3].y));
        auto r = __builtin_amdgcn_permlane32_swap(fu(a), fu(a), false, false);
        auto r2 = __builtin_amdgcn_permlane32_swap(fu(c), fu(c), false, false);
        nmean = -(uf(r[0]) + uf(r[1])) * (1.0f / DM);
        rstd = __builtin_amdgcn_rsqf(__builtin_fmaxf(__builtin_fmaf(-nmean, nmean, (uf(r2[0]) + uf(r2[1])) * (1.0f / DM)), 0.f) + 1e-5f);
      }
#pragma unroll
      for (int j = 0; j < 16; ++j) { ac[0][j] *= nmean; ac[1][j] *= nmean; }
      SB();
#pragma unroll
      for (int f = 0; f < 80; ++f) {
        ac[f & 1] = mfma32(rg[f % RD], as8(hf[f >> 1]), ac[f & 1]);
        const int fn = f + RD;
        rg[f % RD] = fn < 80 ? ldw(fn & 1, fn >> 1) : ldw(2, fn - 80);
        if (f >= 8 && f < 24) ac[2][f - 8] *= nmean;
        if ((f & 1) == 0 && (f >> 1) + HD < 40) hf[(f >> 1) + HD] = ld16c(ws, lane * 16, hso + ((f >> 1) + HD) * 1024);
        SB();
      }
      STAMP(14);
      bar_nw();
      const u32 gvo = (t32 >> 4) * 54 * 1024 + ((t32 & 15) + 16 * hh) * 16 + (rank & 1) * 512;
      const u32 gso = W_G + (team * 8 + 2 * w) * 54 * 1024 + (rank >> 1) * 1024;
      const u32 fso = W_FS4 + (bid * 4 + w) * 128, fvo = lane << 26;
      const float k1 = -2.0f * 0.7978845608028654f * 1.4426950408889634f, k2 = k1 * 0.044715f;
      float gg, uu, tt, ov[2];
      v4u gp;
      auto gstep = [&](const f16v& a, int j, int m) LAM {
        if (m == 0) { gg = a[j] * rstd; tt = gg * gg; tt = __builtin_fmaf(tt, k2, k1); }
        else if (m == 1) { tt = tt * gg; uu = a[8 + j] * rstd; tt = __builtin_amdgcn_exp2f(tt); }
        else if (m == 2) { uu = uu * gg; tt = tt + 1.0f; tt = __builtin_amdgcn_rcpf(tt); }
        else { ov[j & 1] = uu * tt; if (j & 1) gp[j >> 1] = pk(ov[0], ov[1]); }
      };
      auto pass = [&](int n, bool last) LAM {
#pragma unroll
        for (int s = 0; s < 40; ++s) {
          ac[n % 3] = mfma32(rg[s % RD], as8(hf[s]), ac[n % 3]);
          const int sn = s + RD;
          if (sn < 40) rg[s % RD] = ldw(n, sn);
          else if (!last) rg[s % RD] = ldw(n + 1, sn - 40);
          if (n == 2) {
            if (s < 32) { const int un = s >> 4, j = (s & 15) >> 1; gstep(ac[un], j, 2 * (s & 1)); gstep(ac[un], j, 2 * (s & 1) + 1); }
            if (s == 15) st16c(gp, ws, gvo, gso);
            if (s == 17) ac[0] = ldcs(3);
            if (s == 31) st16c(gp, ws, gvo, gso + 8192);
            if (s >= 32) { ac[0][2 * (s - 32)] *= nmean; ac[0][2 * (s - 32) + 1] *= nmean; }
          } else {
            if (s < 32) gstep(ac[(n - 1) % 3], s >> 2, s & 3);
            if (s == 31) st16c(gp, ws, gvo, gso + (n - 1) * 8192);
            if (!last) {
              if (s == 2) ac[(n + 1) % 3] = ldcs(n + 1);
              if (s >= 32) { ac[(n + 1) % 3][2 * (s - 32)] *= nmean; ac[(n + 1) % 3][2 * (s - 32) + 1] *= nmean; }
            }
            if (s == 20) {
              WAITV(0);
              st8c(v2u{tag.x | (u32)(n - 1), tag.y}, ws, fvo, fso);
            }
          }
          SB();
        }
        bar_nw();
      };
      auto tail = [&](int n) LAM {
#pragma unroll
        for (int s = 0; s < 32; ++s) gstep(ac[n % 3], s >> 2, s & 3);
        WAITV(0);
        st8c(v2u{tag.x | (u32)n, tag.y}, ws, fvo, fso);
        st16c(gp, ws, gvo, gso + n * 8192);
        WAITV(0);
        st8c(v2u{tag.x | (u32)(n + 1), tag.y}, ws, fvo, fso);
      };

      pass(2, false); STAMP(22); pass(3, false); STAMP(23); pass(4, false); STAMP(24); pass(5, false); STAMP(25);
      if (u7) { pass(6, true); STAMP(15); tail(6); }
      else { STAMP(15); tail(5); }
    }
    STAMP(16);
    bar();
    STAMP(17);
    {
      u32 ln = __builtin_amdgcn_mbcnt_hi(~0u, __builtin_amdgcn_mbcnt_lo(~0u, 0u));
      OPQ(ln);
      f4 r[5][4];
#pragma unroll
      for (int n = 0; n < 5; ++n)
#pragma unroll
        for (int j = 0; j < 4; ++j) r[n][j] = lld<f4>(L_HS + w * HSLOT + j * 5120 + n * 1024 + ln * 16);
      v4u hc[2];
      hc[0] = lld<v4u>(L_HST + w * 2560 + ln * 16); hc[1] = lld<v4u>(L_HST + w * 2560 + 1024 + ln * 16);
      const v2u ht = lld<v2u>(L_HST + w * 2560 + 2048 + ln * 8);
      SB();
#pragma unroll
      for (int n = 0; n < 5; ++n) r[n][0] = (r[n][0] + r[n][1]) + (r[n][2] + r[n][3]);
      const rsrc_t ro = mk_rsrc(out, SEQ * DM * 2);
      const u32 oso = (team * 128 + rhalf * 64 + w * 16) * 1280 + cblk * 160, ovo = (ln & 15) * 1280;
#pragma unroll
      for (int m = 0; m < 2; ++m) {
        const f4 lo = r[2 * m][0], hi = r[2 * m + 1][0];
        const v4u o = {pk(lo[0] + blo(hc[m].x), lo[1] + bhi(hc[m].x)), pk(lo[2] + blo(hc[m].y), lo[3] + bhi(hc[m].y)),
                       pk(hi[0] + blo(hc[m].z), hi[1] + bhi(hc[m].z)), pk(hi[2] + blo(hc[m].w), hi[3] + bhi(hc[m].w))};
        st16p(o, ro, ovo + (ln >> 4) * 16 + 64 * m, oso);
      }
      const v2u o = {pk(r[4][0][0] + blo(ht.x), r[4][0][1] + bhi(ht.x)), pk(r[4][0][2] + blo(ht.y), r[4][0][3] + bhi(ht.y))};
      __builtin_amdgcn_raw_buffer_store_b64(o, ro, ovo + 128 + (ln >> 4) * 8, oso, 0);
      if (b == 0 && w == 0) st8c(tag, ws, ln << 26, W_EPOCH);
    }
    STAMP(18);
  } else {
    const u32 j = w - 4;
    u32 lane = __builtin_amdgcn_mbcnt_hi(~0u, __builtin_amdgcn_mbcnt_lo(~0u, 0u));
    OPQ(lane);
    {
      bool done = false;
#pragma unroll 1
      for (u32 s = 0; s < SPIN_CAP; ++s) {
        if (!done) { const v2u v = ld8c(ws, (j * 64 + lane) * 128, W_FS2); done = v.x == tag.x && v.y == tag.y; }
        if (wall(done)) break;
        asm volatile("" ::: "memory");
      }
      if (lane == 0) __hip_atomic_fetch_add((LAS u32*)(L_SYNC + 4), 1u, __ATOMIC_RELAXED, __HIP_MEMORY_SCOPE_WORKGROUP);
#pragma unroll 1
      for (u32 s = 0; s < (SPIN_CAP << 4); ++s) {
        if (__builtin_amdgcn_readfirstlane(*(volatile LAS u32*)(L_SYNC + 4)) >= 4u) break;
        __builtin_amdgcn_s_sleep(1);
      }
      asm volatile("" ::: "memory");
    }
    STAMP(10);
    const u32 vl = lane * 16;
    auto stage_u = [&](u32 hs) LAM {
      const u32 so = W_WGU + ((16 * (hs >> 1) + rank) * 40 + (hs & 1) * 20 + j * 5) * 1024, dst = L_HS + ((hs + 5) % 7) * HSLOT + j * 5120;
#pragma unroll
      for (int f = 0; f < 5; ++f) dma16c(vl, sws, so + f * 1024, dst + f * 1024);
    };
    stage_u(0); stage_u(1);
    {
      const u32 t = j * 64 + lane, u = 16 * (t >> 5) + rank;
      if (u < 108) {
        u32 cv[20];
#pragma unroll
        for (int k = 0; k < 20; ++k) cv[k] = ld4c(ws, u * 128 + (t & 31) * 4, W_CSP + k * 108 * 128);
        SB();
        float c = 0.f;
#pragma unroll
        for (int k = 0; k < 20; ++k) c += uf(cv[k]);
        lst<float>(L_CS3 + t * 4, c);
      }
    }
    v4u wf[14][5];
    const u32 wso = W_WD + (cblk * 54 + 2 * j) * 5120;
    const u32 slast = j < 3 ? 0u : 0x80000000u;
    auto ldw = [&](int i) LAM {
#pragma unroll
      for (int t = 0; t < 5; ++t) wf[i][t] = ld16c(ws, (i >= 12 ? vl | slast : vl) + t * 1024, wso + ((i >> 1) * 8 + (i & 1)) * 5120);
    };
    auto stage_d = [&](int i) LAM {
#pragma unroll
      for (int t = 0; t < 5; ++t) dma16c(i >= 12 ? vl | slast : vl, sws, wso + ((i >> 1) * 8 + (i & 1)) * 5120 + t * 1024, L_HS + ((i - 1) % 7) * HSLOT + j * 5120 + t * 1024);
    };
#pragma unroll
    for (int i = 0; i < 6; ++i) ldw(i);
    bar();
    stage_u(2); stage_u(3); stage_u(4); stage_u(5); stage_u(6);
    WAITV(10);
    bar_nw();
    WAITV(0);
    bar_nw();
    stage_u(7); stage_u(8); stage_u(9); stage_u(10);
    WAITV(10);
    bar_nw();
    stage_u(11);
    if (u7) { stage_u(12); WAITV(10); } else WAITV(5);
    bar_nw();
    if (u7) stage_u(13);
    stage_d(6);
    if (u7) WAITV(10); else WAITV(5);
    bar_nw();
    stage_d(7); stage_d(8);
    WAITV(15);
    bar_nw();
    stage_d(9); stage_d(10);
    v4u gf[14][4];
    const u32 gso = W_G + (team * 8 + rhalf * 4) * 54 * 1024 + 2 * j * 1024;
    auto ldg = [&](int i) LAM {
#pragma unroll
      for (int r = 0; r < 4; ++r) gf[i][r] = ld16c(ws, i >= 12 ? vl | slast : vl, gso + r * 54 * 1024 + ((i >> 1) * 8 + (i & 1)) * 1024);
    };
    const u32 fvo = W_FS4 + ((team * 16 + 4 * j + ((lane >> 4) & 3)) * 4 + 2 * rhalf + ((lane >> 3) & 1)) * 128;
    poll_level(ws, fvo, true, tag, 3);
    ldg(0); ldg(1);
    OPQ(gf[1][3]);
    SB();
    if (u7) bar_nw();
    STAMP(16);
    stage_d(11); stage_d(12);
    f4 da[4][5];
#pragma unroll
    for (int r = 0; r < 4; ++r)
#pragma unroll
      for (int t = 0; t < 5; ++t) da[r][t] = z4;
    u32 ldb[3];
    auto ldg1 = [&](int i, int r) LAM {
      gf[i][r] = ld16c(ws, i >= 12 ? vl | slast : vl, gso + r * 54 * 1024 + ((i >> 1) * 8 + (i & 1)) * 1024);
    };
    sfor<0, 14>([&](auto ic) LAM {
      constexpr int i = decltype(ic)::v;
      constexpr int lvl = i == 2 ? 4 : i == 3 ? 5 : i == 6 ? 6 : i == 9 ? 7 : 0;
      constexpr int gl = i == 1 ? 2 : i == 2 ? 4 : i == 3 ? 6 : i == 4 ? 8 : i == 7 ? 10 : i == 10 ? 12 : -1;
      v2u fl;
      if (i == 5) {
        u32 v2 = vl;
        OPQ(v2);
        ldb[0] = L_HS + j * 5120 + v2; ldb[1] = L_HS + 3 * HSLOT + j * 5120 + v2; ldb[2] = L_HS + 6 * HSLOT + j * 5120 + v2;
        OPQ(ldb[0]); OPQ(ldb[1]); OPQ(ldb[2]);
      }
      SB();
#pragma unroll
      for (int m = 0; m < 20; ++m) {
        const int r = m / 5, t = m % 5;
        da[r][t] = mfma16(as8(wf[i][t]), as8(gf[i][r]), da[r][t]);
        if constexpr (gl >= 0) { if (m < 8) ldg1(gl + (m >> 2), m & 3); }
        if (gl == 12 && m >= 8 && m < 13) wf[13][m - 8] = ld16c(ws, (vl | slast) + (m - 8) * 1024, wso + 49 * 5120);
        if (lvl && m == 8) fl = ld8c(ws, fvo, 0);
        if constexpr (i + 1 >= 6 && i + 1 <= 12) { if (m >= 10 && m < 15) wf[i + 1][m - 10] = lld<v4u>(ldb[(i % 7) / 3] + ((i % 7) % 3) * HSLOT + (m - 10) * 1024); }
        SB();
      }
      if (lvl) {
        const u32 need = (lvl < 7 || j < 3) ? lvl : 6;
        if (!wall((fl.x & ~15u) == tag.x && fl.y == tag.y && (fl.x & 15u) >= need)) poll_level(ws, fvo, true, tag, need);
        asm volatile("" ::: "memory");
        if (lvl == 6) STAMP(17);
      }
      SB();
    });
    STAMP(18);
#pragma unroll
    for (int r = 0; r < 4; ++r)
#pragma unroll
      for (int t = 0; t < 5; ++t) lst<f4>(ldb[r / 3] + (r % 3) * HSLOT + t * 1024, da[r][t]);
    bar();
  }
}

extern "C" void kernel_launch(void* const* d_in, const int* in_sizes, int n_in, void* d_out, int out_size, void* d_ws, size_t ws_size,
                              hipStream_t stream) {
  (void)hipFuncSetAttribute((const void*)fused_block, hipFuncAttributeMaxDynamicSharedMemorySize, L_BYTES);
  fused_block<<<256, 512, L_BYTES, stream>>>((const u16*)d_in[0], (const u16*)d_in[1], (const u16*)d_in[2], (const u16*)d_in[3],
                                             (const u16*)d_in[5], (const u16*)d_in[6], (const u16*)d_in[7], (u16*)d_out, (char*)d_ws);
}
```

```cpp
#include <hip/hip_runtime.h>
#include <hip/hip_bf16.h>

typedef unsigned short u16;
typedef unsigned int u32;
typedef unsigned long long u64;
typedef u32 v4u __attribute__((ext_vector_type(4)));
typedef u32 v2u __attribute__((ext_vector_type(2)));
typedef float f4 __attribute__((ext_vector_type(4)));
typedef float f2 __attribute__((ext_vector_type(2)));
typedef float f16v __attribute__((ext_vector_type(16)));
typedef short s4 __attribute__((ext_vector_type(4)));
typedef short s8 __attribute__((ext_vector_type(8)));
typedef __bf16 bf2 __attribute__((ext_vector_type(2)));
#define LAS __attribute__((address_space(3)))
#define CAS __attribute__((address_space(4)))
#if defined(__HIP_DEVICE_COMPILE__)
#define NOPK __attribute__((target("no-packed-fp32-ops")))
#else
#define NOPK
#endif
#define DEV __device__ __forceinline__ NOPK
#define SB() __builtin_amdgcn_sched_barrier(0)
#define OPQ(x) asm volatile("" : "+v"(x))
typedef __amdgpu_buffer_rsrc_t rsrc_t;

#define DBG 1
#define ABL 0
#define ROT ((rank + 5 * (team & 1)) % 10)
#define XMAP 8

constexpr int SEQ = 2048, DM = 640, FF = 1728;
constexpr u32 W_EPOCH = 0;
constexpr u32 W_FS0 = 4096, W_FS2 = W_FS0 + 32768, W_FS3 = W_FS2 + 32768, W_FS4 = W_FS3 + 32768;
constexpr u32 W_FRT = W_FS4 + 131072;
constexpr u32 W_RED = W_FRT + 4096;
constexpr u32 W_STAT = W_RED + 65536;
constexpr u32 W_CSP = W_STAT + 131072;
constexpr u32 W_PART = W_CSP + 20 * 108 * 128;
constexpr u32 W_O = W_PART + 16 * 16 * 8192;
constexpr u32 W_H = W_O + 16 * 8 * 20 * 1024;
constexpr u32 W_G = W_H + 16 * 4 * 40 * 1024;
constexpr u32 W_WGU = W_G + 16 * 8 * 54 * 1024;
constexpr u32 W_WD = W_WGU + 108 * 40 * 1024;
constexpr u32 W_DBG = W_WD + 8 * 54 * 5 * 1024;
constexpr u32 W_END = W_DBG + 256 * 8 * 32 * 4;
constexpr u32 L_WPHI = 0;
constexpr u32 L_CS1 = 2048;
constexpr u32 L_CS3 = 2560;
constexpr u32 L_SYNC = 3584;
constexpr u32 L_RING = 4096, SLOT = 26624;
constexpr u32 L_WQ = L_RING + 4 * SLOT;
constexpr u32 L_RT = L_WQ;
constexpr u32 L_KV = 4096;
constexpr u32 L_IMG = L_RING + 2 * SLOT;
constexpr u32 L_WO = 8192;
constexpr u32 L_HST = 151552;
constexpr u32 L_HS = 8192, HSLOT = 20480;
constexpr u32 L_BYTES = 163840;
constexpr u32 SENT = 0xFFFFFFFFu;
constexpr u32 SPIN_CAP = 1u << 18;

DEV v4u mk_srd(const void* p, u32 bytes) { return v4u{(u32)(size_t)p, (u32)((size_t)p >> 32), bytes, 0x00020000u}; }
DEV rsrc_t mk_rsrc(const void* p, u32 bytes) { return __builtin_amdgcn_make_buffer_rsrc((void*)p, 0, bytes, 0x00020000); }
DEV void dma16(u32 voff, v4u srd, u32 soff, u32 lds) {
  asm volatile("s_mov_b32 m0, %2\n\ts_nop 0\n\tbuffer_load_dwordx4 %0, %1, %3 offen lds" ::"v"(voff), "s"(srd), "s"(lds), "s"(soff) : "memory");
}
DEV void dma16c(u32 voff, v4u srd, u32 soff, u32 lds) {
  asm volatile("s_mov_b32 m0, %2\n\ts_nop 0\n\tbuffer_load_dwordx4 %0, %1, %3 offen sc1 lds" ::"v"(voff), "s"(srd), "s"(lds), "s"(soff) : "memory");
}
#define WAITV(n) asm volatile("s_waitcnt vmcnt(" #n ")" ::: "memory")
DEV void bar() {
  asm volatile("" ::: "memory");
  __builtin_amdgcn_s_waitcnt(0xC07F);
  __builtin_amdgcn_s_barrier();
  asm volatile("" ::: "memory");
  SB();
}
DEV void bar_nw() {
  asm volatile("" ::: "memory");
  __builtin_amdgcn_s_barrier();
  asm volatile("" ::: "memory");
  SB();
}
DEV s4 trd(u32 a) { return __builtin_amdgcn_ds_read_tr16_b64_v4i16((LAS s4*)a); }
DEV s8 cat(s4 a, s4 b) { return __builtin_shufflevector(a, b, 0, 1, 2, 3, 4, 5, 6, 7); }
DEV s8 as8(v4u a) { return __builtin_bit_cast(s8, a); }
DEV s4 as4(v2u a) { return __builtin_bit_cast(s4, a); }
DEV f4 mfma16(s8 a, s8 b, f4 c) { return __builtin_amdgcn_mfma_f32_16x16x32_bf16(a, b, c, 0, 0, 0); }
DEV f4 mfma16h(s4 a, s4 b, f4 c) { return __builtin_amdgcn_mfma_f32_16x16x16bf16_1k(a, b, c, 0, 0, 0); }
DEV f16v mfma32(s8 a, s8 b, f16v c) { return __builtin_amdgcn_mfma_f32_32x32x16_bf16(a, b, c, 0, 0, 0); }
DEV u32 pk(float a, float b) { return __builtin_bit_cast(u32, __builtin_convertvector(f2{a, b}, bf2)); }
DEV float blo(u32 x) { return __builtin_bit_cast(float, x << 16); }
DEV float bhi(u32 x) { return __builtin_bit_cast(float, x & 0xFFFF0000u); }
DEV float dot2(u32 a, u32 b, float c) { return __builtin_amdgcn_fdot2_f32_bf16(__builtin_bit_cast(bf2, a), __builtin_bit_cast(bf2, b), c, false); }
DEV u32 fu(float x) { return __builtin_bit_cast(u32, x); }
DEV float uf(u32 x) { return __builtin_bit_cast(float, x); }
template <class T> DEV T lld(u32 a) { return *(LAS T*)a; }
template <class T> DEV void lst(u32 a, T v) { *(LAS T*)a = v; }
DEV v4u ld16c(rsrc_t r, u32 vo, u32 so) { return __builtin_amdgcn_raw_buffer_load_b128(r, vo, so, 16); }
DEV v2u ld8c(rsrc_t r, u32 vo, u32 so) { return __builtin_amdgcn_raw_buffer_load_b64(r, vo, so, 16); }
DEV u32 ld4c(rsrc_t r, u32 vo, u32 so) { return __builtin_amdgcn_raw_buffer_load_b32(r, vo, so, 16); }
DEV void st16c(v4u v, rsrc_t r, u32 vo, u32 so) {
  __builtin_amdgcn_raw_buffer_store_b128(v, r, vo, so, 16);
  asm volatile("s_nop 1" ::"v"(v) : "memory");
}
DEV void st16p(v4u v, rsrc_t r, u32 vo, u32 so) {
  __builtin_amdgcn_raw_buffer_store_b128(v, r, vo, so, 0);
  asm volatile("s_nop 1" ::"v"(v) : "memory");
}
DEV void st8c(v2u v, rsrc_t r, u32 vo, u32 so) { __builtin_amdgcn_raw_buffer_store_b64(v, r, vo, so, 16); }
DEV void st4c(u32 v, rsrc_t r, u32 vo, u32 so) { __builtin_amdgcn_raw_buffer_store_b32(v, r, vo, so, 16); }
DEV bool wall(bool c) { return __builtin_amdgcn_ballot_w64(!c) == 0ull; }
DEV u32 san(u32 x) { return x < 0xFFFFFFFEu ? x : 0xFFFFFFFEu; }
DEV u32 swap23(u32 k) { return (k & ~12u) | ((k & 4u) << 1) | ((k & 8u) >> 1); }

template <bool MAXOP, bool RCP> DEV void red4(float& a, float& b, float& c, float& d) {
  auto r1 = __builtin_amdgcn_permlane16_swap(fu(a), fu(b), false, false);
  auto r2 = __builtin_amdgcn_permlane16_swap(fu(c), fu(d), false, false);
  float ab = MAXOP ? __builtin_fmaxf(uf(r1[0]), uf(r1[1])) : uf(r1[0]) + uf(r1[1]);
  float cd = MAXOP ? __builtin_fmaxf(uf(r2[0]), uf(r2[1])) : uf(r2[0]) + uf(r2[1]);
  auto r3 = __builtin_amdgcn_permlane32_swap(fu(ab), fu(cd), false, false);
  float z = MAXOP ? __builtin_fmaxf(uf(r3[0]), uf(r3[1])) : uf(r3[0]) + uf(r3[1]);
  if (RCP) z = __builtin_amdgcn_rcpf(z);
  auto g1 = __builtin_amdgcn_permlane32_swap(fu(z), fu(z), false, false);
  auto g2 = __builtin_amdgcn_permlane16_swap(g1[0], g1[0], false, false);
  auto g3 = __builtin_amdgcn_permlane16_swap(g1[1], g1[1], false, false);
  a = uf(g2[0]); b = uf(g2[1]); c = uf(g3[0]); d = uf(g3[1]);
}
DEV void red2(float& a, float& b) {
  auto r1 = __builtin_amdgcn_permlane16_swap(fu(a), fu(b), false, false);
  float ab = uf(r1[0]) + uf(r1[1]);
  auto r3 = __builtin_amdgcn_permlane32_swap(fu(ab), fu(ab), false, false);
  float z = uf(r3[0]) + uf(r3[1]);
  auto g2 = __builtin_amdgcn_permlane16_swap(fu(z), fu(z), false, false);
  a = uf(g2[0]); b = uf(g2[1]);
}

struct Phi { v2u pl[2], mi[2], ex; };
DEV Phi feature_map(f4 ta, f4 tb, f4 tc, const s8 (&A0)[2], const s8 (&A1)[2]) {
  const float L2E = 1.4426950408889634f;
  const u32 c0 = pk(tc[0], tc[1]), c1 = pk(tc[2], tc[3]);
  const s8 b0 = as8(v4u{pk(ta[0], ta[1]), pk(ta[2], ta[3]), c0, c1});
  const s8 b1 = as8(v4u{pk(tb[0], tb[1]), pk(tb[2], tb[3]), c0, c1});
  const f4 z = {0.f, 0.f, 0.f, 0.f};
  f4 p0 = mfma16(A0[0], b0, z), p1 = mfma16(A0[1], b1, z);
  f4 xx = mfma16(A1[0], b0, z);
  xx = mfma16(A1[1], b1, xx);
  float p[2][5] = {{p0[0], p0[1], p0[2], p0[3], xx[0]}, {p1[0], p1[1], p1[2], p1[3], xx[1]}};
  float mx[2], nm[2];
#pragma unroll
  for (int h = 0; h < 2; ++h) {
    mx[h] = __builtin_fmaxf(__builtin_fmaxf(__builtin_fmaxf(p[h][0], p[h][1]), __builtin_fmaxf(p[h][2], p[h][3])), p[h][4]);
    nm[h] = -__builtin_fminf(__builtin_fminf(__builtin_fminf(p[h][0], p[h][1]), __builtin_fminf(p[h][2], p[h][3])), p[h][4]);
  }
  red4<true, false>(mx[0], nm[0], mx[1], nm[1]);
  float ep[2][5], em[2][5], sp[2], sm[2];
#pragma unroll
  for (int h = 0; h < 2; ++h) {
    const float a = -mx[h] * L2E, b = -nm[h] * L2E;
    sp[h] = 0.f; sm[h] = 0.f;
#pragma unroll
    for (int j = 0; j < 5; ++j) {
      ep[h][j] = __builtin_amdgcn_exp2f(__builtin_fmaf(p[h][j], L2E, a));
      em[h][j] = __builtin_amdgcn_exp2f(__builtin_fmaf(p[h][j], -L2E, b));
      sp[h] += ep[h][j]; sm[h] += em[h][j];
    }
  }
  red4<false, true>(sp[0], sm[0], sp[1], sm[1]);
  Phi o;
#pragma unroll
  for (int h = 0; h < 2; ++h) {
    o.pl[h] = v2u{pk(ep[h][0] * sp[h], ep[h][1] * sp[h]), pk(ep[h][2] * sp[h], ep[h][3] * sp[h])};
    o.mi[h] = v2u{pk(em[h][0] * sm[h], em[h][1] * sm[h]), pk(em[h][2] * sm[h], em[h][3] * sm[h])};
    o.ex[h] = pk(ep[h][4] * sp[h], em[h][4] * sm[h]);
  }
  return o;
}
DEV f4 lnfix(f4 a, f4 cs, float nmean, float rstd) {
  f4 o;
#pragma unroll
  for (int j = 0; j < 4; ++j) o[j] = __builtin_fmaf(nmean, cs[j], a[j]) * rstd;
  return o;
}
DEV void poll_flags(rsrc_t ws, u32 voff, bool active, v2u tag) {
#pragma unroll 1
  for (u32 s = 0; s < SPIN_CAP; ++s) {
    v2u v = tag;
    if (active) v = ld8c(ws, voff, 0);
    if (wall(v.x == tag.x && v.y == tag.y)) break;
    asm volatile("" ::: "memory");
  }
  asm volatile("" ::: "memory");
}
DEV void lds_wait(u32 a) {
#pragma unroll 1
  for (u32 s = 0; s < (SPIN_CAP << 4); ++s) {
    if (__builtin_amdgcn_readfirstlane(*(volatile LAS u32*)a) != 0u) break;
    __builtin_amdgcn_s_sleep(1);
  }
  asm volatile("" ::: "memory");
}
DEV void lds_set(u32 a) { *(volatile LAS u32*)a = 1u; }

#define STAMP(i) do { stamp[i] = (u32)__builtin_amdgcn_s_memrealtime(); } while (0)

#define LAM __attribute__((always_inline)) NOPK
template <int V> struct IC { static constexpr int v = V; };
template <int A, int B, class F> DEV void sfor(F&& f) {
  if constexpr (A < B) { f(IC<A>{}); sfor<A + 1, B>(f); }
}
DEV void poll_level(rsrc_t ws, u32 voff, bool active, v2u tag, u32 level) {
#pragma unroll 1
  for (u32 s = 0; s < SPIN_CAP; ++s) {
    v2u v = {tag.x | 15u, tag.y};
    if (active) v = ld8c(ws, voff, 0);
    if (wall((v.x & ~15u) == tag.x && v.y == tag.y && (v.x & 15u) >= level)) break;
    asm volatile("" ::: "memory");
  }
  asm volatile("" ::: "memory");
}

__global__ __launch_bounds__(512, 2) NOPK void fused_block(const u16* __restrict__ x, const u16* __restrict__ wqkv, const u16* __restrict__ wphi,
                                                           const u16* __restrict__ wo, const u16* __restrict__ wg, const u16* __restrict__ wu,
                                                           const u16* __restrict__ wd, u16* __restrict__ out, char* __restrict__ wsp) {
  u32 stamp[32];
#pragma unroll
  for (int i = 0; i < 32; ++i) stamp[i] = 0;
  stamp[20] = (u32)__builtin_amdgcn_s_memtime();
  STAMP(0);
  asm volatile("" ::"s"(x), "s"(wqkv), "s"(wphi), "s"(wo), "s"(wg), "s"(wu), "s"(wd), "s"(out), "s"(wsp));
  const u32 tid = threadIdx.x, lane = tid & 63, w = __builtin_amdgcn_readfirstlane(tid >> 6);
  const u32 q = lane >> 4, i16 = lane & 15, tq = (lane >> 2) & 3, tp = lane & 3;
  const u32 b = blockIdx.x, xcd = b & 7, idx = b >> 3;
  const u32 team = xcd * 2 + (idx >> 4), rank = idx & 15;
  const u32 bid = team * 16 + rank;
  const rsrc_t ws = mk_rsrc(wsp, W_END);
  const v4u sx = mk_srd(x, SEQ * DM * 2), sqkv = mk_srd(wqkv, DM * 3 * DM * 2), sws = mk_srd(wsp, W_END);
  u64 ep;
  asm volatile("s_load_dwordx2 %0, %1, 0x0" : "=s"(ep) : "s"(wsp));

  st16c(v4u{SENT, SENT, SENT, SENT}, ws, lane * 16, W_PART + (rank * 16 + team) * 8192 + w * 1024);
  if (lane < 4) st8c(v2u{SENT, SENT}, ws, lane * 8, W_RED + rank * 4096 + team * 256 + w * 32);
  if (w < 2) dma16(lane * 16, mk_srd(wphi, 32 * 400 * 2), rank * 1600 + w * 1024, L_WPHI + w * 1024);
  const bool w5 = w < 5;
  u32 vx[2], vw[2];
#pragma unroll
  for (int j = 0; j < 2; ++j) {
    const u32 row = 8 * j + (lane >> 3);
    vx[j] = row * 1280 + (((lane & 7) ^ ((row >> 1) & 7)) << 4);
    const u32 pi = 128 * w + 64 * j + lane, srow = pi / 10, pc = pi % 10;
    vw[j] = swap23(srow) * 3840 + (pc < 5 ? 1280 + pc * 16 : 2560 + (pc - 5) * 16);
  }
  const u32 xs0 = (team * 128 + w * 16) * 1280;
  const u32 rot = ROT;
  auto rc = [&](int c) LAM -> u32 { const u32 v = c + rot; return v >= 10 ? v - 10 : v; };
  auto stage1 = [&](int c, int n) LAM {
    const u32 sl = L_RING + (c & 3) * SLOT, cc = rc(c);
    if (n < 2) dma16(vx[n], sx, xs0 + cc * 128, sl + w * 2048 + n * 1024);
    else if (w5) dma16(vw[n - 2], sqkv, rank * 80 + cc * (64 * 3840), sl + 16384 + w * 2048 + (n - 2) * 1024);
  };
#pragma unroll
  for (int c = 0; c < 3; ++c)
#pragma unroll
    for (int n = 0; n < 4; ++n) stage1(c, n);
  if (tid < 16) lst<u32>(L_SYNC + tid * 4, 0u);

  const u32 piece = w * 256 + b;
  const bool has_piece = piece < 1620;
  const u32 pmat = piece / 540, prem = piece % 540;
  const u32 pk_ = pmat < 2 ? prem / 27 : prem / 10, pcb = pmat < 2 ? prem % 27 : prem % 10;
  v4u rt[4];

  const f4 z4 = {0.f, 0.f, 0.f, 0.f};
  v4u onesv = {0x3F803F80u, 0x3F803F80u, 0x3F803F80u, 0x3F803F80u};
  OPQ(onesv);
  const s8 ones = as8(onesv);
  v4u xa[20];
  s8 wf1[2][12];
  f4 acc[6] = {z4, z4, z4, z4, z4, z4};
  float s1[2] = {0.f, 0.f}, s2[2] = {0.f, 0.f};
  s8 A0[2], A1[2];
  v2u tag;
  const u32 ax0 = w * 2048 + i16 * 128 + ((q ^ ((i16 >> 1) & 7)) << 4);
  const u32 rowb = 16 * (q >> 1) + 4 * (q & 1) + tq;
  const u32 aw1 = 16384 + rowb * 160 + 8 * tp, aw2 = 16384 + rowb * 160 + 32 + 40 * tp;
  const u32 awc = w == 2 ? aw2 : aw1 + (w == 0 ? 0 : w == 1 ? 40 : w == 3 ? 80 : 120);
  u32 vq;
  {
    const u32 pi = 64 * w + lane, srow = pi / 5, pc = pi % 5;
    vq = ((srow & ~7u) + 4 * (srow & 1) + ((srow >> 1) & 3)) * 3840 + pc * 16;
  }
#pragma unroll
  for (int c = 0; c <= 10; ++c) {
    u32 bx0, bx1, b1, b2, bc;
    if (c < 10) {
      if (c <= 7) { if (w5) WAITV(8); else WAITV(4); }
      else if (c == 8) { if (w5) WAITV(9); else WAITV(2); }
      else { if (w5) WAITV(10); else WAITV(0); }
      bar();
      if (c == 0) {
        STAMP(1);
        asm volatile("" : "+s"(ep));
        ep = (ep & ~15ull) + 16ull;
        tag = v2u{(u32)ep, (u32)(ep >> 32)};
      }
      const u32 sl = L_RING + (c & 3) * SLOT;
      bx0 = sl + ax0; bx1 = sl + (ax0 ^ 64); b1 = sl + aw1; b2 = sl + aw2; bc = sl + awc;
      OPQ(b1); OPQ(b2); OPQ(bc);
      if (c == 1 && tid == 448) st8c(tag, ws, 0, W_FS0 + bid * 128);
    }
#pragma unroll
    for (int m = 0; m < 12; ++m) {
      const int k2 = m / 6, t = m % 6;
      if (c < 10) {
        if (m == 0) xa[2 * c] = lld<v4u>(bx0);
        if (m == 1) xa[2 * c + 1] = lld<v4u>(bx1);
        const u32 ba = (t == 2 ? b2 : t == 5 ? bc : b1) + k2 * 5120 + (t == 1 ? 40 : t == 3 ? 80 : t == 4 ? 120 : 0);
        if (!(ABL & 4) || c == 0) wf1[c & 1][m] = cat(trd(ba), trd(ba + 1280)); else wf1[c & 1][m] = wf1[0][m];
      }
      if (c >= 1 && (!(ABL & 8) || c == 10)) {
        const v4u xv = xa[2 * (c - 1) + k2];
        acc[t] = mfma16(wf1[(c - 1) & 1][m], t < 5 ? as8(xv) : ones, acc[t]);
        if (t < 4 && !(ABL & 1)) { s1[t & 1] = dot2(xv[t], 0x3F803F80u, s1[t & 1]); s2[t & 1] = dot2(xv[t], xv[t], s2[t & 1]); OPQ(s1[t & 1]); OPQ(s2[t & 1]); }
      }
      SB();
      if (c <= 6 && m % 3 == 1 && !(ABL & 2)) stage1(c + 3, m / 3);
      if ((c == 7 || c == 8) && m % 2 == 1 && m < 10 && w5) {
        const int cq = (c - 7) * 5 + m / 2;
        dma16(vq, sqkv, rank * 80 + rc(cq) * (64 * 3840), L_WQ + cq * 5120 + w * 1024);
      }
      if (c == 9 && m == 2 && has_piece) {
        const v4u sp = mk_srd(pmat == 0 ? wg : pmat == 1 ? wu : wd, DM * FF * 2);
        const u32 rs = pmat < 2 ? 3456 : 1280, vo = (lane >> 3) * rs + (lane & 7) * 16;
#pragma unroll
        for (int n = 0; n < 4; ++n)
          asm volatile("buffer_load_dwordx4 %0, %1, %2, %3 offen" : "=v"(rt[n]) : "v"(vo), "s"(sp), "s"((pk_ * 32 + n * 8) * rs + pcb * 128) : "memory");
      }
      SB();
    }
    if (c == 0) {
#pragma unroll
      for (int h = 0; h < 2; ++h) {
        s8 a0, a1;
#pragma unroll
        for (int j = 0; j < 8; ++j) {
          const u32 d = j < 4 ? 4 * q + j : 12 + j;
          const bool ok = j < 4 || q == (u32)h;
          const short v0 = lld<short>(L_WPHI + h * 800 + d * 40 + i16 * 2);
          const short v1 = lld<short>(L_WPHI + h * 800 + d * 40 + 32 + (i16 >> 2) * 2);
          a0[j] = ok ? v0 : (short)0;
          a1[j] = (ok && (i16 & 3) == (u32)h) ? v1 : (short)0;
        }
        OPQ(a0); OPQ(a1);
        A0[h] = a0; A1[h] = a1;
      }
      SB();
    }
  }
  STAMP(2);
  if (w < 5 && i16 == 0) lst<f4>(L_CS1 + w * 64 + q * 16, acc[5]);
  float sa = s1[0] + s1[1], sb = s2[0] + s2[1];
  red2(sa, sb);
  float nmean1 = -sa * (1.0f / DM);
  float rstd1 = __builtin_amdgcn_rsqf(__builtin_fmaxf(__builtin_fmaf(-nmean1, nmean1, sb * (1.0f / DM)), 0.f) + 1e-5f);
  OPQ(nmean1); OPQ(rstd1);
  bar();
  {
    f4 t[5];
#pragma unroll
    for (int n = 0; n < 5; ++n) t[n] = lnfix(acc[n], lld<f4>(L_CS1 + n * 64 + q * 16), nmean1, rstd1);
    const Phi ph = feature_map(t[0], t[1], t[2], A0, A1);
    const u32 ib = L_IMG + (w * 16 + i16) * 264 + q * 8;
    lst<v2u>(ib + 0, ph.pl[0]); lst<v2u>(ib + 32, ph.mi[0]); lst<v2u>(ib + 64, ph.pl[1]); lst<v2u>(ib + 96, ph.mi[1]);
    lst<v2u>(ib + 128, ph.ex);
    lst<v2u>(ib + 160, v2u{pk(t[3][0], t[3][1]), pk(t[3][2], t[3][3])});
    lst<v2u>(ib + 192, v2u{pk(t[4][0], t[4][1]), pk(t[4][2], t[4][3])});
    const u32 e0 = q < 2 ? 0x3F803F80u : pk(t[2][0], t[2][1]), e1 = q < 2 ? 0x3F803F80u : pk(t[2][2], t[2][3]);
    const u32 eb = L_IMG + (w * 16 + i16) * 264 + 224 + (q ^ 2) * 2;
    lst<u16>(eb, (u16)e0); lst<u16>(eb + 8, (u16)(e0 >> 16)); lst<u16>(eb + 16, (u16)e1); lst<u16>(eb + 24, (u16)(e1 >> 16));
  }
  bar();
  STAMP(3);
  {
    const u32 a1c = w < 4 ? w * 32 : w < 6 ? (w - 4) * 32 : 128;
    const u32 b1c = w < 4 ? 160 + (w >> 1) * 32 : w == 6 ? 160 : 224;
    const u32 a2c = (w == 4 || w == 5) ? a1c + 64 : a1c;
    const u32 b2c = w == 6 ? 192 : b1c;
    const u32 rb = L_IMG + (2 * (q >> 1) + 16 * (q & 1) + 4 * tq) * 264 + 8 * tp;
    u32 ra[4] = {rb + a1c, rb + b1c, rb + a2c, rb + b2c};
    s8 fr[4][4];
#pragma unroll
    for (int n = 0; n < 4; ++n) OPQ(ra[n]);
#pragma unroll
    for (int ks = 0; ks < 4; ++ks)
#pragma unroll
      for (int n = 0; n < 4; ++n) fr[ks][n] = cat(trd(ra[n] + ks * 32 * 264), trd(ra[n] + ks * 32 * 264 + 264));
    SB();
    f4 d1 = z4, d2 = z4;
#pragma unroll
    for (int ks = 0; ks < 4; ++ks) {
      d1 = mfma16(fr[ks][0], fr[ks][1], d1);
      d2 = mfma16(fr[ks][2], fr[ks][3], d2);
    }
    const bool mlo = w != 6 && (lane & 1), mhi = w == 6 || (lane & 1);
    const v4u pv = {san(fu(mlo ? d2[0] : d1[0])), san(fu(mlo ? d2[1] : d1[1])), san(fu(mhi ? d2[2] : d1[2])), san(fu(mhi ? d2[3] : d1[3]))};
    st16c(pv, ws, lane * 16, W_PART + (rank * 16 + team) * 8192 + w * 1024);
  }
  WAITV(5);
  bar();
  STAMP(4);
  const u32 cblk = rank & 7, rhalf = rank >> 3;
  f4 qa[4] = {z4, z4, z4, z4};
  {
    const v4u swo = mk_srd(wo, DM * DM * 2);
    u32 vom, voe;
    {
      const u32 P = w * 64 + lane, srow = P / 10, pc = P % 10;
      const u32 g = 2 * ((srow >> 4) & 1) + ((srow >> 2) & 1), t = (srow >> 3) & 1, i = srow & 3;
      vom = (20 * t + 4 * g + i) * 1280 + pc * 16;
      voe = (40 * g + 20 * (i & 1) + 16 + 2 * t + (i >> 1)) * 1280 + pc * 16;
    }
    const u32 r1 = L_WQ + (8 * q + 2 * tq) * 80;
    u32 bq1 = r1 + 8 * tp, bq2 = r1 + 32 + 40 * (tp & 1);
    u32 bqc = w == 0 ? bq1 : w == 1 ? bq1 + 40 : bq2;
    OPQ(bq1); OPQ(bq2); OPQ(bqc);
    constexpr int QD = 7;
    s8 qf[QD + 1];
    auto ldq = [&](int f) LAM {
      const int t = f & 3;
      const u32 ba = (t == 2 ? bq2 : t == 3 ? bqc : bq1) + (f >> 2) * 2560 + (t == 1 ? 40 : 0);
      qf[f % (QD + 1)] = cat(trd(ba), trd(ba + 80));
    };
#pragma unroll
    for (int f = 0; f < QD; ++f) ldq(f);
    SB();
#pragma unroll
    for (int f = 0; f < 80; ++f) {
      if (f + QD < 80) ldq(f + QD);
      qa[f & 3] = mfma16(qf[f % (QD + 1)], (f & 3) < 3 ? as8(xa[f >> 2]) : ones, qa[f & 3]);
      SB();
      if ((f & 3) == 1 && w5) {
        const int ks = f >> 2;
        dma16(ks < 16 ? vom : voe, swo, cblk * 160 + (ks < 16 ? ks * 40 * 1280 : (ks - 16) * 160 * 1280), L_WO + ks * 5120 + w * 1024);
      }
      SB();
    }
    if (w < 3 && i16 == 0) lst<f4>(L_CS1 + (5 + w) * 64 + q * 16, qa[3]);
  }
  bar();
  STAMP(5);
  if (w == 7) {
    poll_flags(ws, W_FS0 + (lane * 16 + rank) * 128, lane < 16, tag);
    const u32 vo = (lane >> 5) * 8 * 8192 + (lane & 31) * 16, so = W_PART + rank * 16 * 8192 + team * 512;
    v4u pv[8];
#pragma unroll 1
    for (u32 s = 0; s < SPIN_CAP; ++s) {
      bool ok = true;
#pragma unroll
      for (int n = 0; n < 8; ++n) pv[n] = ld16c(ws, vo + n * 8192, so);
      SB();
#pragma unroll
      for (int n = 0; n < 8; ++n) ok = ok && pv[n].x != SENT && pv[n].y != SENT && pv[n].z != SENT && pv[n].w != SENT;
      if (wall(ok)) break;
      asm volatile("" ::: "memory");
    }
    float sm[4];
#pragma unroll
    for (int j = 0; j < 4; ++j) {
      float a = 0.f;
#pragma unroll
      for (int n = 0; n < 8; ++n) a += uf(pv[n][j]);
      auto r = __builtin_amdgcn_permlane32_swap(fu(a), fu(a), false, false);
      sm[j] = uf(r[0]) + uf(r[1]);
    }
    if (lane < 32) st8c(v2u{san(pk(sm[0], sm[1])), san(pk(sm[2], sm[3]))}, ws, lane * 8, W_RED + rank * 4096 + team * 256);
    STAMP(6);
  }
  Phi pq;
  {
    f4 t[3];
#pragma unroll
    for (int n = 0; n < 3; ++n) t[n] = lnfix(qa[n], lld<f4>(L_CS1 + (5 + n) * 64 + q * 16), nmean1, rstd1);
    pq = feature_map(t[0], t[1], t[2], A0, A1);
    OPQ(pq.pl[0]); OPQ(pq.pl[1]); OPQ(pq.mi[0]); OPQ(pq.mi[1]); OPQ(pq.ex);
  }
  STAMP(7);
  if (has_piece) {
    if (w5) WAITV(21); else WAITV(1);
    SB();
    asm volatile("" : "+v"(rt[0]), "+v"(rt[1]), "+v"(rt[2]), "+v"(rt[3])::"memory");
#pragma unroll
    for (int n = 0; n < 4; ++n) lst<v4u>(L_RT + w * 4096 + n * 1024 + lane * 16, rt[n]);
    asm volatile("" ::: "memory");
    const u32 rb = L_RT + w * 4096 + (8 * q + tq) * 128 + (((tp & 1) << 1) | (tp >> 1)) * 8;
    v4u f[4];
#pragma unroll
    for (int sg = 0; sg < 4; ++sg) f[sg] = __builtin_bit_cast(v4u, cat(trd(rb + sg * 32), trd(rb + sg * 32 + 512)));
    if (pmat < 2) {
      float c[4];
#pragma unroll
      for (int sg = 0; sg < 4; ++sg) {
        st16c(f[sg], ws, (q >> 1) * 1024 + (i16 + 32 * (q & 1)) * 16, W_WGU + ((4 * pcb + sg) * 40 + 2 * pk_) * 1024 + pmat * 256);
        c[sg] = 0.f;
#pragma unroll
        for (int j = 0; j < 4; ++j) c[sg] = dot2(f[sg][j], 0x3F803F80u, c[sg]);
      }
      red4<false, false>(c[0], c[1], c[2], c[3]);
      const float cm = q == 0 ? c[0] : q == 1 ? c[1] : q == 2 ? c[2] : c[3];
      st4c(fu(cm), ws, q * 128 + swap23(i16) * 4, W_CSP + (pk_ * 108 + 4 * pcb) * 128 + pmat * 64);
    } else {
#pragma unroll
      for (int sg = 0; sg < 4; ++sg) {
        const u32 sgi = 4 * pcb + sg, cc = sgi / 5, pos = sgi % 5;
        const u32 tile = pos < 4 ? (pos & 2) + (i16 >> 3) : 4, row = pos < 4 ? 8 * (pos & 1) + (i16 & 7) : swap23(i16);
        st16c(f[sg], ws, tile * 1024 + (row + 16 * q) * 16, W_WD + (cc * 54 + pk_) * 5120);
      }
    }
  }
  if (w == 7) {
    v4u kv[4];
#pragma unroll 1
    for (u32 s = 0; s < SPIN_CAP; ++s) {
      bool ok = true;
#pragma unroll
      for (int n = 0; n < 4; ++n) kv[n] = ld16c(ws, lane * 16 + n * 1024, W_RED + rank * 4096);
      SB();
#pragma unroll
      for (int n = 0; n < 4; ++n) ok = ok && kv[n].x != SENT && kv[n].y != SENT && kv[n].z != SENT && kv[n].w != SENT;
      if (wall(ok)) break;
      asm volatile("" ::: "memory");
    }
#pragma unroll
    for (int n = 0; n < 4; ++n) lst<v4u>(L_KV + n * 1024 + lane * 16, kv[n]);
  }
  bar();
  STAMP(8);
  {
    v2u it[8];
#pragma unroll
    for (int n = 0; n < 8; ++n) it[n] = lld<v2u>(L_KV + n * 512 + lane * 8);
    const s8 b0 = as8(v4u{pq.pl[0].x, pq.pl[0].y, pq.mi[0].x, pq.mi[0].y}), b1 = as8(v4u{pq.pl[1].x, pq.pl[1].y, pq.mi[1].x, pq.mi[1].y});
    const s4 bx = as4(pq.ex);
    const bool odd = lane & 1;
    f4 n0 = mfma16(as8(v4u{it[0].x, it[0].y, it[1].x, it[1].y}), b0, z4);
    f4 n1 = mfma16(as8(v4u{it[2].x, it[2].y, it[3].x, it[3].y}), b1, z4);
    const v4u am = {it[4].x, it[4].y, it[5].x, it[5].y}, zz = {0u, 0u, 0u, 0u};
    f4 nx = mfma16(as8(odd ? zz : am), b0, z4);
    n0 = mfma16h(as4(v2u{it[6].x, 0u}), bx, n0);
    n1 = mfma16h(as4(v2u{0u, it[6].y}), bx, n1);
    nx = mfma16(as8(odd ? am : zz), b1, nx);
    nx = mfma16h(as4(odd ? v2u{0u, it[7].y} : v2u{it[7].x, 0u}), bx, nx);
    const float r0 = __builtin_amdgcn_rcpf(nx[2] + 1e-6f), r1 = __builtin_amdgcn_rcpf(nx[3] + 1e-6f);
    const u32 ob = W_O + (team * 8 + w) * 20480;
    st16c(v4u{pk(n0[0] * r0, n0[1] * r0), pk(n0[2] * r0, n0[3] * r0), pk(n1[0] * r1, n1[1] * r1), pk(n1[2] * r1, n1[3] * r1)}, ws, lane * 16, ob + rank * 1024);
    st4c(pk(nx[0] * r0, nx[1] * r1), ws, i16 * 16 + q * 4, ob + (16 + (rank >> 2)) * 1024 + (rank & 3) * 256);
  }
  WAITV(0);
  bar();
  STAMP(9);
  const bool u7 = rank < 12;
  if (w < 4) {
    u32 lane = __builtin_amdgcn_mbcnt_hi(~0u, __builtin_amdgcn_mbcnt_lo(~0u, 0u));
    OPQ(lane);
    const u32 q = lane >> 4, i16 = lane & 15, tq = (lane >> 2) & 3, tp = lane & 3, rowb = 16 * (q >> 1) + 4 * (q & 1) + tq;
    if (w == 0) st8c(tag, ws, lane << 26, W_FS2 + bid * 128);
    const u32 tk = rhalf * 64 + w * 16 + i16;
    const u32 xo = tk * 1280 + q * 16, xso = team * 128 * 1280 + cblk * 160;
    {
      const rsrc_t rx = mk_rsrc(x, SEQ * DM * 2);
      const v4u xr0 = __builtin_amdgcn_raw_buffer_load_b128(rx, xo, xso, 0), xr1 = __builtin_amdgcn_raw_buffer_load_b128(rx, xo + 64, xso, 0);
      const v2u xr2 = __builtin_amdgcn_raw_buffer_load_b64(rx, tk * 1280 + 128 + q * 8, xso, 0);
      constexpr int OD = 6;
      s8 wf[OD][5];
      const u32 r1 = L_WO + rowb * 160;
      u32 bA[2], bB[2], bC[2];
      bA[0] = r1 + 16 * tp + 8 * (tp >> 1); bB[0] = r1 + 16 * tp + 8 * (1 - (tp >> 1)); bC[0] = r1 + 128 + 8 * tp;
      bA[1] = bA[0] + 51200; bB[1] = bB[0] + 51200; bC[1] = bC[0] + 51200;
      OPQ(bA[0]); OPQ(bB[0]); OPQ(bC[0]); OPQ(bA[1]); OPQ(bB[1]); OPQ(bC[1]);
      auto ldo1 = [&](int ks, int t) LAM {
        const int hb = ks / 10;
        const u32 ba = (t == 4 ? bC[hb] : (t & 1) ? bB[hb] : bA[hb]) + (ks % 10) * 5120 + ((t == 2 || t == 3) ? 64 : 0);
        wf[ks % OD][t] = cat(trd(ba), trd(ba + 1280));
      };
#pragma unroll
      for (int ks = 0; ks < OD; ++ks)
#pragma unroll
        for (int t = 0; t < 5; ++t) ldo1(ks, t);
      __builtin_amdgcn_s_waitcnt(0xC07F);
      SB();
      if (w == 0) { poll_flags(ws, W_FS2 + (team * 16 + lane) * 128, lane < 16, tag); lds_set(L_SYNC + 0); }
      else lds_wait(L_SYNC + 0);
      STAMP(10);
      v4u of[20];
      const u32 oso = W_O + (team * 8 + rhalf * 4 + w) * 20480;
      constexpr int PD = 12;
#pragma unroll
      for (int ks = 0; ks < PD; ++ks) of[ks] = ld16c(ws, lane * 16, oso + ks * 1024);
      SB();
      f4 ha[5] = {z4, z4, z4, z4, z4};
#pragma unroll
      for (int ks = 0; ks < 20; ++ks) {
#pragma unroll
        for (int t = 0; t < 5; ++t) {
          ha[t] = mfma16(wf[ks % OD][t], as8(of[ks]), ha[t]);
          if (t == 0 && ks + PD < 20) of[ks + PD] = ld16c(ws, lane * 16, oso + (ks + PD) * 1024);
          if (ks + OD < 20) ldo1(ks + OD, t);
          SB();
        }
      }
      STAMP(11);
      v4u hc[2];
      const bool sw = q >= 2;
#pragma unroll
      for (int m = 0; m < 2; ++m) {
        const f4 lo = sw ? ha[2 * m + 1] : ha[2 * m], hi = sw ? ha[2 * m] : ha[2 * m + 1];
        const v4u xr = m ? xr1 : xr0;
        hc[m] = v4u{pk(lo[0] + blo(xr.x), lo[1] + bhi(xr.x)), pk(lo[2] + blo(xr.y), lo[3] + bhi(xr.y)), pk(hi[0] + blo(xr.z), hi[1] + bhi(xr.z)), pk(hi[2] + blo(xr.w), hi[3] + bhi(xr.w))};
      }
      const v2u ht = {pk(ha[4][0] + blo(xr2.x), ha[4][1] + bhi(xr2.x)), pk(ha[4][2] + blo(xr2.y), ha[4][3] + bhi(xr2.y))};
      const u32 hb = W_H + (team * 4 + rhalf * 2 + (w >> 1)) * 40960 + cblk * 5120;
      const u32 t32 = (w & 1) * 16 + i16;
      st16c(hc[0], ws, (q >> 1) * 1024 + (t32 + 32 * (q & 1)) * 16, hb);
      st16c(hc[1], ws, (q >> 1) * 1024 + (t32 + 32 * (q & 1)) * 16, hb + 2048);
      st8c(ht, ws, (t32 + 32 * (q >> 1)) * 16 + 8 * (q & 1), hb + 4096);
      float a = 0.f, c = 0.f;
#pragma unroll
      for (int m = 0; m < 2; ++m)
#pragma unroll
        for (int j = 0; j < 4; ++j) { a = dot2(hc[m][j], 0x3F803F80u, a); c = dot2(hc[m][j], hc[m][j], c); }
#pragma unroll
      for (int j = 0; j < 2; ++j) { a = dot2(ht[j], 0x3F803F80u, a); c = dot2(ht[j], ht[j], c); }
      red2(a, c);
      if (q == 0) st8c(v2u{fu(a), fu(c)}, ws, tk * 8, W_STAT + (team * 8 + cblk) * 1024);
      lst<v4u>(L_HST + w * 2560 + lane * 16, hc[0]);
      lst<v4u>(L_HST + w * 2560 + 1024 + lane * 16, hc[1]);
      lst<v2u>(L_HST + w * 2560 + 2048 + lane * 8, ht);
    }
    WAITV(0);
    bar();
    STAMP(12);
    if (w == 0) {
      if (lane == 0) st8c(tag, ws, 0, W_FS3 + bid * 128);
      poll_flags(ws, W_FS3 + (team * 16 + lane) * 128, lane < 16, tag);
    }
    bar();
    STAMP(13);
    {
      const u32 hh = lane >> 5, t32 = lane & 31;
      v2u sv[4];
#pragma unroll
      for (int n = 0; n < 4; ++n) sv[n] = ld8c(ws, (hh * 4 + n) * 1024 + t32 * 8, W_STAT + team * 8192 + w * 256);
      v4u hf[40];
      constexpr int HD = 12;
      const u32 hso = W_H + (team * 4 + w) * 40960;
#pragma unroll
      for (int s = 0; s < HD; ++s) hf[s] = ld16c(ws, lane * 16, hso + s * 1024);
      SB();
      u32 lb[3] = {L_HS + lane * 16, L_HS + 3 * HSLOT + lane * 16, L_HS + 6 * HSLOT + lane * 16};
      OPQ(lb[0]); OPQ(lb[1]); OPQ(lb[2]);
      auto ldw = [&](int n, int s) LAM -> s8 {
        const int hs = (2 * n + s / 20 + 5) % 7;
        return lld<s8>(lb[hs / 3] + (hs % 3) * HSLOT + (s % 20) * 1024);
      };
      const u32 csb = L_CS3 + hh * 32;
      auto ldcs = [&](int n) LAM -> f16v {
        const f4 g0 = lld<f4>(csb + n * 128), g1 = lld<f4>(csb + n * 128 + 16), u0 = lld<f4>(csb + n * 128 + 64), u1 = lld<f4>(csb + n * 128 + 80);
        return __builtin_shufflevector(__builtin_shufflevector(g0, g1, 0, 1, 2, 3, 4, 5, 6, 7), __builtin_shufflevector(u0, u1, 0, 1, 2, 3, 4, 5, 6, 7), 0, 1, 2, 3, 4,
                                       5, 6, 7, 8, 9, 10, 11, 12, 13, 14, 15);
      };
      f16v ac[3];
      ac[0] = ldcs(0); ac[1] = ldcs(1); ac[2] = ldcs(2);
      constexpr int RD = 4;
      s8 rg[RD];
#pragma unroll
      for (int f = 0; f < RD; ++f) rg[f] = ldw(f & 1, f >> 1);
      SB();
      float nmean, rstd;
      {
        const float a = (uf(sv[0].x) + uf(sv[1].x)) + (uf(sv[2].x) + uf(sv[3].x)), c = (uf(sv[0].y) + uf(sv[1].y)) + (uf(sv[2].y) + uf(sv[3].y));
        auto r = __builtin_amdgcn_permlane32_swap(fu(a), fu(a), false, false);
        auto r2 = __builtin_amdgcn_permlane32_swap(fu(c), fu(c), false, false);
        nmean = -(uf(r[0]) + uf(r[1])) * (1.0f / DM);
        rstd = __builtin_amdgcn_rsqf(__builtin_fmaxf(__builtin_fmaf(-nmean, nmean, (uf(r2[0]) + uf(r2[1])) * (1.0f / DM)), 0.f) + 1e-5f);
      }
#pragma unroll
      for (int j = 0; j < 16; ++j) { ac[0][j] *= nmean; ac[1][j] *= nmean; }
      SB();
#pragma unroll
      for (int f = 0; f < 80; ++f) {
        ac[f & 1] = mfma32(rg[f % RD], as8(hf[f >> 1]), ac[f & 1]);
        const int fn = f + RD;
        rg[f % RD] = fn < 80 ? ldw(fn & 1, fn >> 1) : ldw(2, fn - 80);
        if (f >= 8 && f < 24) ac[2][f - 8] *= nmean;
        if ((f & 1) == 0 && (f >> 1) + HD < 40) hf[(f >> 1) + HD] = ld16c(ws, lane * 16, hso + ((f >> 1) + HD) * 1024);
        SB();
      }
      STAMP(14);
      bar_nw();
      const u32 gvo = (t32 >> 4) * 54 * 1024 + ((t32 & 15) + 16 * hh) * 16 + (rank & 1) * 512;
      const u32 gso = W_G + (team * 8 + 2 * w) * 54 * 1024 + (rank >> 1) * 1024;
      const u32 fso = W_FS4 + (bid * 4 + w) * 128, fvo = lane << 26;
      const float k1 = -2.0f * 0.7978845608028654f * 1.4426950408889634f, k2 = k1 * 0.044715f;
      float gg, uu, tt, ov[2];
      v4u gp;
      auto gstep = [&](const f16v& a, int j, int m) LAM {
        if (m == 0) { gg = a[j] * rstd; tt = gg * gg; tt = __builtin_fmaf(tt, k2, k1); }
        else if (m == 1) { tt = tt * gg; uu = a[8 + j] * rstd; tt = __builtin_amdgcn_exp2f(tt); }
        else if (m == 2) { uu = uu * gg; tt = tt + 1.0f; tt = __builtin_amdgcn_rcpf(tt); }
        else { ov[j & 1] = uu * tt; if (j & 1) gp[j >> 1] = pk(ov[0], ov[1]); }
      };
      auto pass = [&](int n, bool last) LAM {
#pragma unroll
        for (int s = 0; s < 40; ++s) {
          ac[n % 3] = mfma32(rg[s % RD], as8(hf[s]), ac[n % 3]);
          const int sn = s + RD;
          if (sn < 40) rg[s % RD] = ldw(n, sn);
          else if (!last) rg[s % RD] = ldw(n + 1, sn - 40);
          if (n == 2) {
            if (s < 32) { const int un = s >> 4, j = (s & 15) >> 1; gstep(ac[un], j, 2 * (s & 1)); gstep(ac[un], j, 2 * (s & 1) + 1); }
            if (s == 15) st16c(gp, ws, gvo, gso);
            if (s == 17) ac[0] = ldcs(3);
            if (s == 31) st16c(gp, ws, gvo, gso + 8192);
            if (s >= 32) { ac[0][2 * (s - 32)] *= nmean; ac[0][2 * (s - 32) + 1] *= nmean; }
          } else {
            if (s < 32) gstep(ac[(n - 1) % 3], s >> 2, s & 3);
            if (s == 31) st16c(gp, ws, gvo, gso + (n - 1) * 8192);
            if (!last) {
              if (s == 2) ac[(n + 1) % 3] = ldcs(n + 1);
              if (s >= 32) { ac[(n + 1) % 3][2 * (s - 32)] *= nmean; ac[(n + 1) % 3][2 * (s - 32) + 1] *= nmean; }
            }
            if (s == 20) {
              WAITV(0);
              st8c(v2u{tag.x | (u32)(n - 1), tag.y}, ws, fvo, fso);
            }
          }
          SB();
        }
        bar_nw();
      };
      auto tail = [&](int n) LAM {
#pragma unroll
        for (int s = 0; s < 32; ++s) gstep(ac[n % 3], s >> 2, s & 3);
        WAITV(0);
        st8c(v2u{tag.x | (u32)n, tag.y}, ws, fvo, fso);
        st16c(gp, ws, gvo, gso + n * 8192);
        WAITV(0);
        st8c(v2u{tag.x | (u32)(n + 1), tag.y}, ws, fvo, fso);
      };

      pass(2, false); STAMP(22); pass(3, false); STAMP(23); pass(4, false); STAMP(24); pass(5, false); STAMP(25);
      if (u7) { pass(6, true); STAMP(15); tail(6); }
      else { STAMP(15); tail(5); }
    }
    STAMP(16);
    bar();
    STAMP(17);
    {
      u32 ln = __builtin_amdgcn_mbcnt_hi(~0u, __builtin_amdgcn_mbcnt_lo(~0u, 0u));
      OPQ(ln);
      f4 r[5][4];
#pragma unroll
      for (int n = 0; n < 5; ++n)
#pragma unroll
        for (int j = 0; j < 4; ++j) r[n][j] = lld<f4>(L_HS + w * HSLOT + j * 5120 + n * 1024 + ln * 16);
      v4u hc[2];
      hc[0] = lld<v4u>(L_HST + w * 2560 + ln * 16); hc[1] = lld<v4u>(L_HST + w * 2560 + 1024 + ln * 16);
      const v2u ht = lld<v2u>(L_HST + w * 2560 + 2048 + ln * 8);
      SB();
#pragma unroll
      for (int n = 0; n < 5; ++n) r[n][0] = (r[n][0] + r[n][1]) + (r[n][2] + r[n][3]);
      const rsrc_t ro = mk_rsrc(out, SEQ * DM * 2);
      const u32 oso = (team * 128 + rhalf * 64 + w * 16) * 1280 + cblk * 160, ovo = (ln & 15) * 1280;
#pragma unroll
      for (int m = 0; m < 2; ++m) {
        const f4 lo = r[2 * m][0], hi = r[2 * m + 1][0];
        const v4u o = {pk(lo[0] + blo(hc[m].x), lo[1] + bhi(hc[m].x)), pk(lo[2] + blo(hc[m].y), lo[3] + bhi(hc[m].y)),
                       pk(hi[0] + blo(hc[m].z), hi[1] + bhi(hc[m].z)), pk(hi[2] + blo(hc[m].w), hi[3] + bhi(hc[m].w))};
        st16p(o, ro, ovo + (ln >> 4) * 16 + 64 * m, oso);
      }
      const v2u o = {pk(r[4][0][0] + blo(ht.x), r[4][0][1] + bhi(ht.x)), pk(r[4][0][2] + blo(ht.y), r[4][0][3] + bhi(ht.y))};
      __builtin_amdgcn_raw_buffer_store_b64(o, ro, ovo + 128 + (ln >> 4) * 8, oso, 0);
      if (b == 0 && w == 0) st8c(tag, ws, ln << 26, W_EPOCH);
    }
    STAMP(18);
  } else {
    const u32 j = w - 4;
    u32 lane = __builtin_amdgcn_mbcnt_hi(~0u, __builtin_amdgcn_mbcnt_lo(~0u, 0u));
    OPQ(lane);
    {
      bool done = false;
#pragma unroll 1
      for (u32 s = 0; s < SPIN_CAP; ++s) {
        if (!done) { const v2u v = ld8c(ws, (j * 64 + lane) * 128, W_FS2); done = v.x == tag.x && v.y == tag.y; }
        if (wall(done)) break;
        asm volatile("" ::: "memory");
      }
      if (lane == 0) __hip_atomic_fetch_add((LAS u32*)(L_SYNC + 4), 1u, __ATOMIC_RELAXED, __HIP_MEMORY_SCOPE_WORKGROUP);
#pragma unroll 1
      for (u32 s = 0; s < (SPIN_CAP << 4); ++s) {
        if (__builtin_amdgcn_readfirstlane(*(volatile LAS u32*)(L_SYNC + 4)) >= 4u) break;
        __builtin_amdgcn_s_sleep(1);
      }
      asm volatile("" ::: "memory");
    }
    STAMP(10);
    const u32 vl = lane * 16;
    auto stage_u = [&](u32 hs) LAM {
      const u32 so = W_WGU + ((16 * (hs >> 1) + rank) * 40 + (hs & 1) * 20 + j * 5) * 1024, dst = L_HS + ((hs + 5) % 7) * HSLOT + j * 5120;
#pragma unroll
      for (int f = 0; f < 5; ++f) dma16c(vl, sws, so + f * 1024, dst + f * 1024);
    };
    stage_u(0); stage_u(1);
    {
      const u32 t = j * 64 + lane, u = 16 * (t >> 5) + rank;
      if (u < 108) {
        u32 cv[20];
#pragma unroll
        for (int k = 0; k < 20; ++k) cv[k] = ld4c(ws, u * 128 + (t & 31) * 4, W_CSP + k * 108 * 128);
        SB();
        float c = 0.f;
#pragma unroll
        for (int k = 0; k < 20; ++k) c += uf(cv[k]);
        lst<float>(L_CS3 + t * 4, c);
      }
    }
    v4u wf[14][5];
    const u32 wso = W_WD + (cblk * 54 + 2 * j) * 5120;
    const u32 slast = j < 3 ? 0u : 0x80000000u;
    auto ldw = [&](int i) LAM {
#pragma unroll
      for (int t = 0; t < 5; ++t) wf[i][t] = ld16c(ws, (i >= 12 ? vl | slast : vl) + t * 1024, wso + ((i >> 1) * 8 + (i & 1)) * 5120);
    };
    auto stage_d = [&](int i) LAM {
#pragma unroll
      for (int t = 0; t < 5; ++t) dma16c(i >= 12 ? vl | slast : vl, sws, wso + ((i >> 1) * 8 + (i & 1)) * 5120 + t * 1024, L_HS + ((i - 1) % 7) * HSLOT + j * 5120 + t * 1024);
    };
#pragma unroll
    for (int i = 0; i < 6; ++i) ldw(i);
    bar();
    stage_u(2); stage_u(3); stage_u(4); stage_u(5); stage_u(6);
    WAITV(10);
    bar_nw();
    WAITV(0);
    bar_nw();
    stage_u(7); stage_u(8); stage_u(9); stage_u(10);
    WAITV(10);
    bar_nw();
    stage_u(11);
    if (u7) { stage_u(12); WAITV(10); } else WAITV(5);
    bar_nw();
    if (u7) stage_u(13);
    stage_d(6);
    if (u7) WAITV(10); else WAITV(5);
    bar_nw();
    stage_d(7); stage_d(8);
    WAITV(15);
    bar_nw();
    stage_d(9); stage_d(10);
    v4u gf[14][4];
    const u32 gso = W_G + (team * 8 + rhalf * 4) * 54 * 1024 + 2 * j * 1024;
    auto ldg = [&](int i) LAM {
#pragma unroll
      for (int r = 0; r < 4; ++r) gf[i][r] = ld16c(ws, i >= 12 ? vl | slast : vl, gso + r * 54 * 1024 + ((i >> 1) * 8 + (i & 1)) * 1024);
    };
    const u32 fvo = W_FS4 + ((team * 16 + 4 * j + ((lane >> 4) & 3)) * 4 + 2 * rhalf + ((lane >> 3) & 1)) * 128;
    poll_level(ws, fvo, true, tag, 3);
    ldg(0); ldg(1);
    OPQ(gf[1][3]);
    SB();
    if (u7) bar_nw();
    STAMP(16);
    stage_d(11); stage_d(12);
    f4 da[4][5];
#pragma unroll
    for (int r = 0; r < 4; ++r)
#pragma unroll
      for (int t = 0; t < 5; ++t) da[r][t] = z4;
    u32 ldb[3];
    auto ldg1 = [&](int i, int r) LAM {
      gf[i][r] = ld16c(ws, i >= 12 ? vl | slast : vl, gso + r * 54 * 1024 + ((i >> 1) * 8 + (i & 1)) * 1024);
    };
    sfor<0, 14>([&](auto ic) LAM {
      constexpr int i = decltype(ic)::v;
      constexpr int lvl = i == 2 ? 4 : i == 3 ? 5 : i == 6 ? 6 : i == 9 ? 7 : 0;
      constexpr int gl = i == 1 ? 2 : i == 2 ? 4 : i == 3 ? 6 : i == 4 ? 8 : i == 7 ? 10 : i == 10 ? 12 : -1;
      v2u fl;
      if (i == 5) {
        u32 v2 = vl;
        OPQ(v2);
        ldb[0] = L_HS + j * 5120 + v2; ldb[1] = L_HS + 3 * HSLOT + j * 5120 + v2; ldb[2] = L_HS + 6 * HSLOT + j * 5120 + v2;
        OPQ(ldb[0]); OPQ(ldb[1]); OPQ(ldb[2]);
      }
      SB();
#pragma unroll
      for (int m = 0; m < 20; ++m) {
        const int r = m / 5, t = m % 5;
        da[r][t] = mfma16(as8(wf[i][t]), as8(gf[i][r]), da[r][t]);
        if constexpr (gl >= 0) { if (m < 8) ldg1(gl + (m >> 2), m & 3); }
        if (gl == 12 && m >= 8 && m < 13) wf[13][m - 8] = ld16c(ws, (vl | slast) + (m - 8) * 1024, wso + 49 * 5120);
        if (lvl && m == 8) fl = ld8c(ws, fvo, 0);
        if constexpr (i + 1 >= 6 && i + 1 <= 12) { if (m >= 10 && m < 15) wf[i + 1][m - 10] = lld<v4u>(ldb[(i % 7) / 3] + ((i % 7) % 3) * HSLOT + (m - 10) * 1024); }
        SB();
      }
      if (lvl) {
        const u32 need = (lvl < 7 || j < 3) ? lvl : 6;
        if (!wall((fl.x & ~15u) == tag.x && fl.y == tag.y && (fl.x & 15u) >= need)) poll_level(ws, fvo, true, tag, need);
        asm volatile("" ::: "memory");
        if (lvl == 6) STAMP(17);
      }
      SB();
    });
    STAMP(18);
#pragma unroll
    for (int r = 0; r < 4; ++r)
#pragma unroll
      for (int t = 0; t < 5; ++t) lst<f4>(ldb[r / 3] + (r % 3) * HSLOT + t * 1024, da[r][t]);
    bar();
  }
  STAMP(19);
  if (w == 0) { const u32 d = stamp[0] - stamp[0]; while ((u32)__builtin_amdgcn_s_memrealtime() - stamp[19] < d) __builtin_amdgcn_s_sleep(1); }
  asm volatile("" :: "s"(stamp[1]), "s"(stamp[2]), "s"(stamp[9]), "s"(stamp[13]));
}

extern "C" void kernel_launch(void* const* d_in, const int* in_sizes, int n_in, void* d_out, int out_size, void* d_ws, size_t ws_size,
                              hipStream_t stream) {
  (void)hipFuncSetAttribute((const void*)fused_block, hipFuncAttributeMaxDynamicSharedMemorySize, L_BYTES);
  fused_block<<<256, 512, L_BYTES, stream>>>((const u16*)d_in[0], (const u16*)d_in[1], (const u16*)d_in[2], (const u16*)d_in[3],
                                             (const u16*)d_in[5], (const u16*)d_in[6], (const u16*)d_in[7], (u16*)d_out, (char*)d_ws);
}
```

```cpp
#include <hip/hip_runtime.h>
#include <hip/hip_bf16.h>

typedef unsigned short u16;
typedef unsigned int u32;
typedef unsigned long long u64;
typedef u32 v4u __attribute__((ext_vector_type(4)));
typedef u32 v2u __attribute__((ext_vector_type(2)));
typedef float f4 __attribute__((ext_vector_type(4)));
typedef float f2 __attribute__((ext_vector_type(2)));
typedef float f16v __attribute__((ext_vector_type(16)));
typedef short s4 __attribute__((ext_vector_type(4)));
typedef short s8 __attribute__((ext_vector_type(8)));
typedef __bf16 bf2 __attribute__((ext_vector_type(2)));
#define LAS __attribute__((address_space(3)))
#define CAS __attribute__((address_space(4)))
#if defined(__HIP_DEVICE_COMPILE__)
#define NOPK __attribute__((target("no-packed-fp32-ops")))
#else
#define NOPK
#endif
#define DEV __device__ __forceinline__ NOPK
#define SB() __builtin_amdgcn_sched_barrier(0)
#define OPQ(x) asm volatile("" : "+v"(x))
typedef __amdgpu_buffer_rsrc_t rsrc_t;

#define DBG 0
#define ROT ((rank + 3 * (team & 1)) % 10)
#define P1ST 0
#define HOPA0 28
#define HOPA1 56
#define HOPB 1
#define FAS 32
#define NRM1 3
#define NRM2 8
#define HOPA 2
#define NWPRE 2
#define HOPD 12
#define HOPE 4
#define FAD 14
#define FAR 4
#define PD2 14
#define KDEP 6
#define FBD 0
#define LVS 8
#define WD6 8
#define NW2 6
#define NW0 4
#define NW1 4
#define XD3 1
#define HPRIO 0
#define XMAP 4

constexpr int SEQ = 2048, DM = 640, FF = 1728;
constexpr u32 W_EPOCH = 0;
constexpr u32 W_FA = 4096;
constexpr u32 W_FB = W_FA + 256 * 128;
constexpr u32 W_FS2 = W_FB + 256 * 32;
constexpr u32 W_FS3 = W_FS2 + 256 * 4 * 128;
constexpr u32 W_FS4 = W_FS3 + 256 * 4 * 128;
constexpr u32 W_RED = W_FS4 + 256 * 4 * 8 * 128;
constexpr u32 W_STAT = W_RED + 65536;
constexpr u32 W_CSP = W_STAT + 131072;
constexpr u32 W_CS1 = W_CSP + 20 * 108 * 128;
constexpr u32 W_PART = W_CS1 + 20 * 7680;
constexpr u32 W_O = W_PART + 16 * 16 * 8192;
constexpr u32 W_H = W_O + 16 * 8 * 20 * 1024;
constexpr u32 W_G = W_H + 16 * 4 * 40 * 1024;
constexpr u32 W_WGU = W_G + 16 * 8 * 54 * 1024;
constexpr u32 W_WD = W_WGU + 108 * 40 * 1024;
constexpr u32 W_WO = W_WD + 8 * 54 * 5 * 1024;
constexpr u32 W_W1 = W_WO + 8 * 20 * 5 * 1024;
constexpr u32 W_DBG = W_W1 + 16 * 153600;
constexpr u32 W_END = W_DBG + 256 * 8 * 32 * 4;
constexpr u32 L_WPHI = 0;
constexpr u32 L_CS3 = 3200;
constexpr u32 L_SYNC = 3072;
constexpr u32 L_CS1 = 2048;
constexpr u32 S_PST = L_SYNC, S_FA = L_SYNC + 4, S_XD = L_SYNC + 8, S_CS = L_SYNC + 12, S_KVD = L_SYNC + 16, S_WOC = L_SYNC + 20, S_RT = L_SYNC + 24, S_FB = L_SYNC + 28, S_OP = L_SYNC + 52, S_OL = L_SYNC + 56, S_HL = L_SYNC + 60;
constexpr u32 S_L1 = L_SYNC + 32, L_LEV = L_SYNC + 64, L_LAND = L_SYNC + 80, L_CONS = L_SYNC + 100;
constexpr u32 L_KV = 4096;
constexpr u32 L_A = 8192;
constexpr u32 L_B = L_A + 102400;
constexpr u32 L_PH2 = L_B + 51200;
constexpr u32 L_IMG = L_A, L_WO = L_A;
constexpr u32 L_HS = L_A, HSLOT = 20480;
constexpr u32 L_HST = L_HS + 7 * HSLOT;
constexpr u32 L_BYTES = 163840;
constexpr u32 OOB = 0x80000000u;
constexpr u32 SENT = 0xFFFFFFFFu;
constexpr u32 SPIN_CAP = 1u << 18;

DEV v4u mk_srd(const void* p, u32 bytes) { return v4u{(u32)(size_t)p, (u32)((size_t)p >> 32), bytes, 0x00020000u}; }
DEV rsrc_t mk_rsrc(const void* p, u32 bytes) { return __builtin_amdgcn_make_buffer_rsrc((void*)p, 0, bytes, 0x00020000); }
DEV void dma16(u32 voff, v4u srd, u32 soff, u32 lds) {
  asm volatile("s_mov_b32 m0, %2\n\ts_nop 0\n\tbuffer_load_dwordx4 %0, %1, %3 offen lds" ::"v"(voff), "s"(srd), "s"(lds), "s"(soff) : "memory");
}
DEV void dma16c(u32 voff, v4u srd, u32 soff, u32 lds) {
  asm volatile("s_mov_b32 m0, %2\n\ts_nop 0\n\tbuffer_load_dwordx4 %0, %1, %3 offen sc1 lds" ::"v"(voff), "s"(srd), "s"(lds), "s"(soff) : "memory");
}
DEV void dma_a(u32 voff, v4u srd, u32 sb, u32 sa, u32 lb, u32 la) {
  u32 t;
  asm volatile("s_add_u32 m0, %5, %6\n\ts_add_u32 %0, %3, %4\n\tbuffer_load_dwordx4 %1, %2, %0 offen lds"
               : "=&s"(t) : "v"(voff), "s"(srd), "s"(sb), "s"(sa), "s"(lb), "s"(la) : "memory", "scc");
}
DEV void dma_ac(u32 voff, v4u srd, u32 sb, u32 sa, u32 lb, u32 la) {
  u32 t;
  asm volatile("s_add_u32 m0, %5, %6\n\ts_add_u32 %0, %3, %4\n\tbuffer_load_dwordx4 %1, %2, %0 offen sc1 lds"
               : "=&s"(t) : "v"(voff), "s"(srd), "s"(sb), "s"(sa), "s"(lb), "s"(la) : "memory", "scc");
}
DEV void dma_i(u32 voff, v4u srd, u32 sb, u32 sa, u32 lb, u32 la) {
  u32 t;
  asm volatile("s_add_u32 m0, %5, %6\n\ts_add_u32 %0, %3, %4\n\tbuffer_load_dwordx4 %1, %2, %0 offen lds"
               : "=&s"(t) : "v"(voff), "s"(srd), "s"(sb), "i"(sa), "s"(lb), "i"(la) : "memory", "scc");
}
DEV void dma5c(u32 voff, v4u srd, u32 so, u32 lds) {
  u32 t;
  asm volatile("s_mov_b32 m0, %4\n\ts_add_u32 %0, %3, 0x1000\n\t"
               "buffer_load_dwordx4 %1, %2, %3 offen sc1 lds\n\t"
               "buffer_load_dwordx4 %1, %2, %3 offen offset:1024 sc1 lds\n\t"
               "buffer_load_dwordx4 %1, %2, %3 offen offset:2048 sc1 lds\n\t"
               "buffer_load_dwordx4 %1, %2, %3 offen offset:3072 sc1 lds\n\t"
               "s_add_u32 m0, %4, 0x1000\n\ts_nop 0\n\t"
               "buffer_load_dwordx4 %1, %2, %0 offen sc1 lds"
               : "=&s"(t) : "v"(voff), "s"(srd), "s"(so), "s"(lds) : "memory", "scc");
}
DEV u32 sgpr(u32 x) {
  u32 o;
  asm volatile("s_mov_b32 %0, %1" : "=s"(o) : "s"(__builtin_amdgcn_readfirstlane(x)));
  return o;
}
#define OPS(x) x = sgpr(x)
template <int N> DEV void waitv() { asm volatile("s_waitcnt vmcnt(%0)" ::"n"(N) : "memory"); }
#define WAITV(n) asm volatile("s_waitcnt vmcnt(" #n ")" ::: "memory")
DEV void bar() {
  asm volatile("" ::: "memory");
  __builtin_amdgcn_s_waitcnt(0xC07F);
  __builtin_amdgcn_s_barrier();
  asm volatile("" ::: "memory");
  SB();
}
DEV void bar_nw() {
  asm volatile("" ::: "memory");
  __builtin_amdgcn_s_barrier();
  asm volatile("" ::: "memory");
  SB();
}
DEV s4 trd(u32 a) { return __builtin_amdgcn_ds_read_tr16_b64_v4i16((LAS s4*)a); }
DEV s8 cat(s4 a, s4 b) { return __builtin_shufflevector(a, b, 0, 1, 2, 3, 4, 5, 6, 7); }
DEV s8 as8(v4u a) { return __builtin_bit_cast(s8, a); }
DEV s4 as4(v2u a) { return __builtin_bit_cast(s4, a); }
DEV f4 mfma16(s8 a, s8 b, f4 c) { return __builtin_amdgcn_mfma_f32_16x16x32_bf16(a, b, c, 0, 0, 0); }
DEV f4 mfma16h(s4 a, s4 b, f4 c) { return __builtin_amdgcn_mfma_f32_16x16x16bf16_1k(a, b, c, 0, 0, 0); }
DEV f16v mfma32(s8 a, s8 b, f16v c) { return __builtin_amdgcn_mfma_f32_32x32x16_bf16(a, b, c, 0, 0, 0); }
DEV u32 pk(float a, float b) { return __builtin_bit_cast(u32, __builtin_convertvector(f2{a, b}, bf2)); }
DEV float blo(u32 x) { return __builtin_bit_cast(float, x << 16); }
DEV float bhi(u32 x) { return __builtin_bit_cast(float, x & 0xFFFF0000u); }
DEV float dot2(u32 a, u32 b, float c) { return __builtin_amdgcn_fdot2_f32_bf16(__builtin_bit_cast(bf2, a), __builtin_bit_cast(bf2, b), c, false); }
DEV u32 fu(float x) { return __builtin_bit_cast(u32, x); }
DEV float uf(u32 x) { return __builtin_bit_cast(float, x); }
template <class T> DEV T lld(u32 a) { return *(LAS T*)a; }
template <class T> DEV void lst(u32 a, T v) { *(LAS T*)a = v; }
DEV v4u ld16c(rsrc_t r, u32 vo, u32 so) { return __builtin_amdgcn_raw_buffer_load_b128(r, vo, so, 16); }
DEV v2u ld8c(rsrc_t r, u32 vo, u32 so) { return __builtin_amdgcn_raw_buffer_load_b64(r, vo, so, 16); }
DEV u32 ld4c(rsrc_t r, u32 vo, u32 so) { return __builtin_amdgcn_raw_buffer_load_b32(r, vo, so, 16); }
DEV void st16c(v4u v, rsrc_t r, u32 vo, u32 so) {
  __builtin_amdgcn_raw_buffer_store_b128(v, r, vo, so, 16);
  asm volatile("s_nop 1" ::"v"(v) : "memory");
}
DEV void st16p(v4u v, rsrc_t r, u32 vo, u32 so) {
  __builtin_amdgcn_raw_buffer_store_b128(v, r, vo, so, 0);
  asm volatile("s_nop 1" ::"v"(v) : "memory");
}
DEV void st8c(v2u v, rsrc_t r, u32 vo, u32 so) { __builtin_amdgcn_raw_buffer_store_b64(v, r, vo, so, 16); }
DEV void st4c(u32 v, rsrc_t r, u32 vo, u32 so) { __builtin_amdgcn_raw_buffer_store_b32(v, r, vo, so, 16); }
DEV bool wall(bool c) { return __builtin_amdgcn_ballot_w64(!c) == 0ull; }
DEV u32 san(u32 x) { return x < 0xFFFFFFFEu ? x : 0xFFFFFFFEu; }
DEV u32 swap23(u32 k) { return (k & ~12u) | ((k & 4u) << 1) | ((k & 8u) >> 1); }

template <bool MAXOP, bool RCP> DEV void red4(float& a, float& b, float& c, float& d) {
  auto r1 = __builtin_amdgcn_permlane16_swap(fu(a), fu(b), false, false);
  auto r2 = __builtin_amdgcn_permlane16_swap(fu(c), fu(d), false, false);
  float ab = MAXOP ? __builtin_elementwise_maximum(uf(r1[0]), uf(r1[1])) : uf(r1[0]) + uf(r1[1]);
  float cd = MAXOP ? __builtin_elementwise_maximum(uf(r2[0]), uf(r2[1])) : uf(r2[0]) + uf(r2[1]);
  auto r3 = __builtin_amdgcn_permlane32_swap(fu(ab), fu(cd), false, false);
  float z = MAXOP ? __builtin_elementwise_maximum(uf(r3[0]), uf(r3[1])) : uf(r3[0]) + uf(r3[1]);
  if (RCP) z = __builtin_amdgcn_rcpf(z);
  auto g1 = __builtin_amdgcn_permlane32_swap(fu(z), fu(z), false, false);
  auto g2 = __builtin_amdgcn_permlane16_swap(g1[0], g1[0], false, false);
  auto g3 = __builtin_amdgcn_permlane16_swap(g1[1], g1[1], false, false);
  a = uf(g2[0]); b = uf(g2[1]); c = uf(g3[0]); d = uf(g3[1]);
}
DEV void red2(float& a, float& b) {
  auto r1 = __builtin_amdgcn_permlane16_swap(fu(a), fu(b), false, false);
  float ab = uf(r1[0]) + uf(r1[1]);
  auto r3 = __builtin_amdgcn_permlane32_swap(fu(ab), fu(ab), false, false);
  float z = uf(r3[0]) + uf(r3[1]);
  auto g2 = __builtin_amdgcn_permlane16_swap(fu(z), fu(z), false, false);
  a = uf(g2[0]); b = uf(g2[1]);
}

#define LAM __attribute__((always_inline)) NOPK
template <int V> struct IC { static constexpr int v = V; };
template <int A, int B, class F> DEV void sfor(F&& f) {
  if constexpr (A < B) { f(IC<A>{}); sfor<A + 1, B>(f); }
}
struct Phi { v2u pl[2], mi[2], ex; };
DEV float fmx(float a, float b) { return __builtin_elementwise_maximum(a, b); }
DEV float fmn(float a, float b) { return __builtin_elementwise_minimum(a, b); }
DEV void phi_mm(s8 b0, s8 b1, const s8 (&A)[4], f4 (&p)[3]) {
  const f4 z = {0.f, 0.f, 0.f, 0.f};
  p[2] = mfma16(A[2], b0, z);
  p[0] = mfma16(A[0], b0, z);
  p[1] = mfma16(A[1], b1, z);
  p[2] = mfma16(A[3], b1, p[2]);
}
DEV Phi phi_sm(const f4 (&pp)[3]) {
  const float p[2][5] = {{pp[0][0], pp[0][1], pp[0][2], pp[0][3], pp[2][0]}, {pp[1][0], pp[1][1], pp[1][2], pp[1][3], pp[2][1]}};
  float mx[2], nm[2];
#pragma unroll
  for (int h = 0; h < 2; ++h) {
    mx[h] = fmx(fmx(fmx(fmx(p[h][0], p[h][1]), p[h][2]), p[h][3]), p[h][4]);
    nm[h] = fmx(fmx(fmx(fmx(-p[h][0], -p[h][1]), -p[h][2]), -p[h][3]), -p[h][4]);
  }
  red4<true, false>(mx[0], nm[0], mx[1], nm[1]);
  float ep[2][5], em[2][5], sp[2], sm[2];
#pragma unroll
  for (int h = 0; h < 2; ++h) {
#pragma unroll
    for (int j = 0; j < 5; ++j) {
      ep[h][j] = __builtin_amdgcn_exp2f(p[h][j] - mx[h]);
      em[h][j] = __builtin_amdgcn_exp2f(-p[h][j] - nm[h]);
    }
    sp[h] = ((ep[h][0] + ep[h][1]) + (ep[h][2] + ep[h][3])) + ep[h][4];
    sm[h] = ((em[h][0] + em[h][1]) + (em[h][2] + em[h][3])) + em[h][4];
  }
  red4<false, true>(sp[0], sm[0], sp[1], sm[1]);
  Phi o;
#pragma unroll
  for (int h = 0; h < 2; ++h) {
    o.pl[h] = v2u{pk(ep[h][0] * sp[h], ep[h][1] * sp[h]), pk(ep[h][2] * sp[h], ep[h][3] * sp[h])};
    o.mi[h] = v2u{pk(em[h][0] * sm[h], em[h][1] * sm[h]), pk(em[h][2] * sm[h], em[h][3] * sm[h])};
    o.ex[h] = pk(ep[h][4] * sp[h], em[h][4] * sm[h]);
  }
  return o;
}
DEV v2u lnpk(f4 a, f4 cs, float nmean, float sc) {
  return v2u{pk(__builtin_fmaf(nmean, cs[0], a[0]) * sc, __builtin_fmaf(nmean, cs[1], a[1]) * sc),
             pk(__builtin_fmaf(nmean, cs[2], a[2]) * sc, __builtin_fmaf(nmean, cs[3], a[3]) * sc)};
}
DEV f4 lcs(u32 a) { return lld<f4>(a) + lld<f4>(a + 512); }
DEV u32 bsel(u32 m, u32 a, u32 b) { return (a & m) | (b & ~m); }
DEV void poll_flags(rsrc_t ws, u32 voff, bool active, v2u tag) {
#pragma unroll 1
  for (u32 s = 0; s < SPIN_CAP; ++s) {
    v2u v = tag;
    if (active) v = ld8c(ws, voff, 0);
    if (wall(v.x == tag.x && v.y == tag.y)) break;
    asm volatile("" ::: "memory");
  }
  asm volatile("" ::: "memory");
}
template <int SL = 1> DEV void lds_check(u32 lv, u32 a, u32 n) {
  u32 sv, cnt;
  asm volatile(
      "s_nop 0\n\tv_readfirstlane_b32 %0, %2\n\ts_cmp_ge_u32 %0, %4\n\ts_cbranch_scc1 2f\n\ts_mov_b32 %1, 0x400000\n"
      "1:\n\tv_mov_b32 %2, %3\n\tds_read_b32 %2, %2\n\ts_waitcnt lgkmcnt(0)\n\tv_readfirstlane_b32 %0, %2\n\ts_cmp_ge_u32 %0, %4\n\ts_cbranch_scc1 2f\n\t"
      "s_sleep %5\n\ts_sub_u32 %1, %1, 1\n\ts_cbranch_scc0 1b\n2:"
      : "=&s"(sv), "=&s"(cnt), "+v"(lv) : "s"(a), "s"(n), "n"(SL) : "memory", "scc");
}
template <int SL = 1> DEV void lds_wait(u32 a, u32 n = 4) { lds_check<SL>(0u, a, n); }
DEV void lds_inc(u32 a) {
  u32 t0, t1;
  asm volatile("v_mov_b32 %0, %2\n\tv_mov_b32 %1, 1\n\ts_mov_b64 exec, 1\n\tds_add_u32 %0, %1\n\ts_mov_b64 exec, -1" : "=&v"(t0), "=&v"(t1) : "s"(a) : "memory");
}
DEV void poll256(rsrc_t ws, u32 base, u32 stride, u32 lane, v2u tag) {
#pragma unroll 1
  for (u32 s = 0; s < SPIN_CAP; ++s) {
    v2u f[4];
    bool ok = true;
#pragma unroll
    for (int n = 0; n < 4; ++n) f[n] = ld8c(ws, lane * stride, base + n * 64 * stride);
#pragma unroll
    for (int n = 0; n < 4; ++n) ok = ok && f[n].x == tag.x && f[n].y == tag.y;
    if (wall(ok)) break;
    asm volatile("" ::: "memory");
  }
  asm volatile("" ::: "memory");
}
DEV u32 lane_id() {
  u32 l;
  asm volatile("v_mbcnt_lo_u32_b32 %0, -1, 0\n\tv_mbcnt_hi_u32_b32 %0, -1, %0" : "=v"(l));
  return l;
}
DEV u32 umax(u32 a, u32 b) { return a > b ? a : b; }
DEV u32 umax4(u32 m, v4u v) { return umax(umax(umax(umax(m, v.x), v.y), v.z), v.w); }

#define STAMP(i) do {} while (0)

DEV void poll_level(rsrc_t ws, u32 voff, v2u tag, u32 level) {
#pragma unroll 1
  for (u32 s = 0; s < SPIN_CAP; ++s) {
    const v2u v = ld8c(ws, voff, level * 128);
    voff = (v.x == tag.x && v.y == tag.y) ? OOB : voff;
    if (wall(voff == OOB)) break;
    __builtin_amdgcn_s_sleep(LVS);
    asm volatile("" ::: "memory");
  }
  asm volatile("" ::: "memory");
}

template <bool TWO> DEV void kv_job(rsrc_t ws, u32 jb, u32 so) {
  const u32 lane = lane_id();
  const u32 q = lane >> 4, tq = (lane >> 2) & 3, tp = lane & 3;
  const u32 a1c = !TWO ? jb * 32 : jb < 6 ? (jb - 4) * 32 : 128;
  const u32 b1c = !TWO ? 160 + (jb >> 1) * 32 : jb == 6 ? 160 : 224;
  const u32 a2c = jb < 6 ? a1c + 64 : a1c;
  const u32 b2c = jb == 6 ? 192 : b1c;
  const u32 rb = L_IMG + (2 * (q >> 1) + 16 * (q & 1) + 4 * tq) * 264 + 8 * tp;
  constexpr int NO = TWO ? 4 : 2;
  u32 ra[4] = {rb + a1c, rb + b1c, rb + a2c, rb + b2c};
  u32 mlo = (jb != 6 && (lane & 1)) ? ~0u : 0u, mhi = (jb == 6 || (lane & 1)) ? ~0u : 0u, vl = lane * 16;
#pragma unroll
  for (int n = 0; n < NO; ++n) OPQ(ra[n]);
  OPQ(vl);
  if (TWO) { OPQ(mlo); OPQ(mhi); }
  bar();
  s8 fr[4][NO];
#pragma unroll
  for (int ks = 0; ks < 4; ++ks)
#pragma unroll
    for (int n = 0; n < NO; ++n) fr[ks][n] = cat(trd(ra[n] + ks * 32 * 264), trd(ra[n] + ks * 32 * 264 + 264));
  SB();
  f4 d1 = {0.f, 0.f, 0.f, 0.f}, d2 = d1;
#pragma unroll
  for (int ks = 0; ks < 4; ++ks) {
    d1 = mfma16(fr[ks][0], fr[ks][1], d1);
    if (TWO) d2 = mfma16(fr[ks][NO - 2], fr[ks][NO - 1], d2);
  }
  if (TWO) d1 = f4{uf(bsel(mlo, fu(d2[0]), fu(d1[0]))), uf(bsel(mlo, fu(d2[1]), fu(d1[1]))), uf(bsel(mhi, fu(d2[2]), fu(d1[2]))), uf(bsel(mhi, fu(d2[3]), fu(d1[3])))};
  st16c(v4u{san(fu(d1[0])), san(fu(d1[1])), san(fu(d1[2])), san(fu(d1[3]))}, ws, vl, so + jb * 1024);
}

typedef u64 v8q __attribute__((ext_vector_type(8)));
__global__ __launch_bounds__(512, 2) NOPK void fused_block(const u16* x_, const u16* wqkv_, const u16* wphi_, const u16* wo_, const u16* wg_, const u16* wu_,
                                                           const u16* wd_, u16* out_, char* wsp_) {
  v8q ka;
  u64 kw;
  asm volatile("s_load_dwordx16 %0, %2, 0x0\n\ts_load_dwordx2 %1, %2, 0x40" : "=&s"(ka), "=&s"(kw) : "s"(__builtin_amdgcn_kernarg_segment_ptr()));
  SB();
  STAMP(0);
  const u32 wv = __builtin_amdgcn_readfirstlane(threadIdx.x >> 6);
  const u32 b = blockIdx.x, xcd = b & 7, idx = b >> 3;
  const u32 team = (xcd >> 1) * 4 + (idx >> 3), rank = ((idx & 4) << 1) | ((xcd & 1) << 2) | (idx & 3);
  const u32 bid = team * 16 + rank, hq = rank & 7, th = rank >> 3;
  const bool u7 = rank < 12;
  const f4 z4 = {0.f, 0.f, 0.f, 0.f};
#define GET_ARGS()                                                                                         \
  SB();                                                                                                    \
  asm volatile("s_waitcnt lgkmcnt(0)" ::: "memory");                                                       \
  SB();                                                                                                    \
  asm volatile("" : "+s"(ka), "+s"(kw));                                                                   \
  char* const wsp = (char*)kw;                                                                             \
  const rsrc_t ws = mk_rsrc(wsp, W_END);
#define GET_EPOCH()                                                                                        \
  asm volatile("s_waitcnt lgkmcnt(0)" ::: "memory");                                                       \
  SB();                                                                                                    \
  asm volatile("" : "+s"(ep));                                                                             \
  ep = (ep & ~15ull) + 16ull;                                                                              \
  const v2u tag = {(u32)ep, (u32)(ep >> 32)};

  if (wv >= 4) {
    const u32 w = wv - 4;
    const u32 lane = lane_id();
    const u32 q = lane >> 4, i16 = lane & 15;
    const u32 vx0 = (lane >> 3) * 1280 + (((lane & 7) ^ (lane >> 4)) << 4), vx1 = (vx0 ^ 64) + 8 * 1280;
    u32 xs0 = (team * 128 + w * 32) * 1280, xl0 = L_B + w * 12288;
    OPS(xs0); OPS(xl0);
    GET_ARGS();
    const u16* const x = (const u16*)ka[0];
    u16* const out = (u16*)ka[7];
    const v4u sx = mk_srd(x, SEQ * DM * 2);
    bar_nw();
    u64 ep;
    asm volatile("s_load_dwordx2 %0, %1, 0x0" : "=s"(ep) : "s"(wsp));
    auto stagex = [&](int c) LAM {
      dma_i(vx0, sx, xs0, c * 128, xl0, (c % 3) * 4096);
      dma_i(vx1, sx, xs0, c * 128, xl0, (c % 3) * 4096 + 1024);
      dma_i(vx0, sx, xs0, c * 128 + 16 * 1280, xl0, (c % 3) * 4096 + 2048);
      dma_i(vx1, sx, xs0, c * 128 + 16 * 1280, xl0, (c % 3) * 4096 + 3072);
    };
    stagex(0); stagex(1);
    stagex(2);
    v4u xa[2][20];
    float nm1[2], rs1[2];
    {
      v4u onesv = {0x3F803F80u, 0x3F803F80u, 0x3F803F80u, 0x3F803F80u};
      OPQ(onesv);
      const s8 ones = as8(onesv);
      f4 as[2] = {z4, z4}, ag[2] = {z4, z4};
      u32 ax0 = L_B + w * 12288 + i16 * 128 + ((q ^ ((i16 >> 1) & 7)) << 4), ax1 = ax0 ^ 64;
      OPQ(ax0); OPQ(ax1);
      sfor<0, 11>([&](auto cc_) LAM {
        constexpr int c = decltype(cc_)::v;
        if constexpr (c < 10) waitv<(c < 8 ? 8 : c == 8 ? 4 : 0)>();
        __builtin_amdgcn_s_waitcnt(0xC07F);
        SB();
        if constexpr (c < 10) {
#pragma unroll
          for (int t = 0; t < 2; ++t) { xa[t][2 * c] = lld<v4u>(ax0 + (c % 3) * 4096 + t * 2048); xa[t][2 * c + 1] = lld<v4u>(ax1 + (c % 3) * 4096 + t * 2048); }
        }
        if constexpr (c < 7) { __builtin_amdgcn_s_waitcnt(0xC07F); SB(); stagex(c + 3); }
        if constexpr (c == 10) lds_inc(S_XD);
        if constexpr (c >= 1) {
#pragma unroll
          for (int k = 2 * c - 2; k < 2 * c; ++k)
#pragma unroll
            for (int t = 0; t < 2; ++t) { as[t] = mfma16(ones, as8(xa[t][k]), as[t]); ag[t] = mfma16(as8(xa[t][k]), as8(xa[t][k]), ag[t]); }
        }
        SB();
      });
      STAMP(23);
      float g[2];
#pragma unroll
      for (int t = 0; t < 2; ++t) {
        u32 v = 0;
#pragma unroll
        for (int r = 0; r < 4; ++r) v |= fu(ag[t][r]) & (i16 == 4 * q + r ? ~0u : 0u);
        g[t] = uf(v);
      }
      red2(g[0], g[1]);
#pragma unroll
      for (int t = 0; t < 2; ++t) {
        nm1[t] = -as[t][0] * (1.0f / DM);
        rs1[t] = __builtin_amdgcn_rsqf(__builtin_fmaxf(__builtin_fmaf(-nm1[t], nm1[t], g[t] * (1.0f / DM)), 0.f) + 1e-5f);
        OPQ(nm1[t]); OPQ(rs1[t]);
      }
    }
    GET_EPOCH();
    __builtin_amdgcn_s_setprio(2);
    const float L2E = 1.4426950408889634f;
    {
      f4 acc[2][5] = {{z4, z4, z4, z4, z4}, {z4, z4, z4, z4, z4}};
      u32 bw[2] = {L_A + lane * 16, L_A + 51200 + lane * 16};
      OPQ(bw[0]); OPQ(bw[1]);
      constexpr int KD = KDEP;
      s8 kf[KD + 1];
      u32 lv;
      lds_wait(S_FA);
      { v2u tg = tag; OPQ(tg); st8c(tg, ws, lane_id() << 26, w == 0 ? W_FB + bid * 32 : OOB); }
      lds_wait(S_L1);
      STAMP(25);
#pragma unroll
      for (int f = 0; f < KD; ++f) kf[f] = lld<s8>(bw[0] + f * 1024);
      SB();
      sfor<0, 100>([&](auto gc) LAM {
        constexpr int g = decltype(gc)::v, gn = g + KD;
        if constexpr (gn < 100 && gn % 20 == 0) {
          lds_check(lv, S_L1 + 4 * (gn / 20), 4);

        }
        if constexpr (gn < 100) kf[gn % (KD + 1)] = lld<s8>(bw[gn / 50] + (gn % 50) * 1024);
        if constexpr (gn == 44) lds_inc(S_CS);
        if constexpr (gn % 20 == 14) lv = *(volatile LAS u32*)(gn < 80 ? S_L1 + 4 * (gn / 20 + 1) : S_CS);
        acc[0][g % 5] = mfma16(kf[g % (KD + 1)], as8(xa[0][g / 5]), acc[0][g % 5]);
        acc[1][g % 5] = mfma16(kf[g % (KD + 1)], as8(xa[1][g / 5]), acc[1][g % 5]);
        SB();
      });
      lds_inc(S_KVD);
      lds_check(lv, S_CS, 8);
      STAMP(2);
      const u32 cb = L_CS1 + 160 + q * 16;
      const u32 mq = q < 2 ? ~0u : 0u;
      f4 pp[2][3];
      {
        const f4 c0 = lcs(cb), c1 = lcs(cb + 80), c2 = lcs(L_CS1 + 224 + q * 80);
        const s8 A[4] = {lld<s8>(L_WPHI + lane * 16), lld<s8>(L_WPHI + 1024 + lane * 16), lld<s8>(L_PH2 + lane * 16), lld<s8>(L_PH2 + 1024 + lane * 16)};
#pragma unroll
        for (int t = 0; t < 2; ++t) {
          const float sk = rs1[t] * L2E;
          const v2u k0 = lnpk(acc[t][0], c0, nm1[t], sk), k1 = lnpk(acc[t][1], c1, nm1[t], sk), k2 = lnpk(acc[t][2], c2, nm1[t], uf(bsel(mq, fu(sk), fu(rs1[t]))));
          phi_mm(as8(v4u{k0.x, k0.y, k2.x, k2.y}), as8(v4u{k1.x, k1.y, k2.x, k2.y}), A, pp[t]);
          const u32 e0 = bsel(mq, 0x3F803F80u, k2.x), e1 = bsel(mq, 0x3F803F80u, k2.y), eb = L_IMG + (w * 32 + t * 16 + i16) * 264 + 224 + (q ^ 2) * 2;
          lst<u16>(eb, (u16)e0); lst<u16>(eb + 8, (u16)(e0 >> 16)); lst<u16>(eb + 16, (u16)e1); lst<u16>(eb + 24, (u16)(e1 >> 16));
          SB();
        }
      }
      {
        const f4 c3 = lcs(cb + 160), c4 = lcs(cb + 240);
#pragma unroll
        for (int t = 0; t < 2; ++t) {
          const u32 ib = L_IMG + (w * 32 + t * 16 + i16) * 264;
          lst<v2u>(ib + q * 8 + 160, lnpk(acc[t][3], c3, nm1[t], rs1[t]));
          lst<v2u>(ib + q * 8 + 192, lnpk(acc[t][4], c4, nm1[t], rs1[t]));
        }
      }
      SB();
#pragma unroll
      for (int t = 0; t < 2; ++t) {
        const Phi ph = phi_sm(pp[t]);
        const u32 ib = L_IMG + (w * 32 + t * 16 + i16) * 264 + q * 8;
        lst<v2u>(ib + 0, ph.pl[0]); lst<v2u>(ib + 32, ph.mi[0]); lst<v2u>(ib + 64, ph.pl[1]); lst<v2u>(ib + 96, ph.mi[1]);
        lst<v2u>(ib + 128, ph.ex);
      }
    }
    STAMP(3);
    kv_job<false>(ws, w, W_PART + (rank * 16 + team) * 8192);
    STAMP(4);
    bar_nw();
    {
      f4 qa[2][3] = {{z4, z4, z4}, {z4, z4, z4}};
      u32 bq = L_B + lane * 16, bq2 = L_B + 4096 + ((lane & 7) + 16 * q) * 16;
      OPQ(bq); OPQ(bq2);
      constexpr int QD = 4;
      s8 qf[QD + 1];
      auto ldq = [&](int f) LAM {
        const int t = f % 3, ks = f / 3;
        qf[f % (QD + 1)] = lld<s8>(t == 2 ? bq2 + (ks >> 1) * 5120 + (ks & 1) * 128 : bq + (ks >> 1) * 5120 + ((ks & 1) * 2 + t) * 1024);
      };
#pragma unroll
      for (int f = 0; f < QD; ++f) ldq(f);
      SB();
#pragma unroll
      for (int f = 0; f < 60; ++f) {
        if (f + QD < 60) ldq(f + QD);
        qa[0][f % 3] = mfma16(qf[f % (QD + 1)], as8(xa[0][f / 3]), qa[0][f % 3]);
        qa[1][f % 3] = mfma16(qf[f % (QD + 1)], as8(xa[1][f / 3]), qa[1][f % 3]);
        SB();
      }
      STAMP(5);
      const f4 c0 = lcs(L_CS1 + q * 16), c1 = lcs(L_CS1 + 80 + q * 16), c2 = lcs(L_CS1 + 64 + (q & 1) * 80);
      const s8 A[4] = {lld<s8>(L_WPHI + lane * 16), lld<s8>(L_WPHI + 1024 + lane * 16), lld<s8>(L_PH2 + lane * 16), lld<s8>(L_PH2 + 1024 + lane * 16)};
      f4 pp[2][3];
      Phi pq[2];
#pragma unroll
      for (int t = 0; t < 2; ++t) {
        const float sk = rs1[t] * L2E;
        const v2u k0 = lnpk(qa[t][0], c0, nm1[t], sk), k1 = lnpk(qa[t][1], c1, nm1[t], sk), k2 = lnpk(qa[t][2], c2, nm1[t], sk);
        phi_mm(as8(v4u{k0.x, k0.y, k2.x, k2.y}), as8(v4u{k1.x, k1.y, k2.x, k2.y}), A, pp[t]);
      }
      v4u b0[2], b1[2], bx[2];
#pragma unroll
      for (int t = 0; t < 2; ++t) {
        pq[t] = phi_sm(pp[t]);
        b0[t] = v4u{pq[t].pl[0].x, pq[t].pl[0].y, pq[t].mi[0].x, pq[t].mi[0].y};
        b1[t] = v4u{pq[t].pl[1].x, pq[t].pl[1].y, pq[t].mi[1].x, pq[t].mi[1].y};
        bx[t] = v4u{pq[t].ex.x, pq[t].ex.y, 0u, 0u};
        OPQ(b0[t]); OPQ(b1[t]); OPQ(bx[t]);
      }
      u32 mo = (lane & 1) ? ~0u : 0u, ve = i16 * 16 + q * 4;
      OPQ(mo); OPQ(ve);
      STAMP(7);
      bar_nw();
      STAMP(8);
      v2u it[8];
#pragma unroll
      for (int n = 0; n < 8; ++n) it[n] = lld<v2u>(L_KV + n * 512 + lane * 8);
      const s8 ae = as8(v4u{it[4].x & ~mo, it[4].y & ~mo, it[5].x & ~mo, it[5].y & ~mo}), ao = as8(v4u{it[4].x & mo, it[4].y & mo, it[5].x & mo, it[5].y & mo});
      const s8 ax = as8(v4u{it[7].x & ~mo, it[7].y & mo, 0u, 0u});
      f4 nx[2], n0[2], n1[2];
#pragma unroll
      for (int t = 0; t < 2; ++t) {
        nx[t] = mfma16(ae, as8(b0[t]), z4);
        nx[t] = mfma16(ao, as8(b1[t]), nx[t]);
        nx[t] = mfma16(ax, as8(bx[t]), nx[t]);
      }
#pragma unroll
      for (int t = 0; t < 2; ++t) {
        n0[t] = mfma16(as8(v4u{it[0].x, it[0].y, it[1].x, it[1].y}), as8(b0[t]), z4);
        n1[t] = mfma16(as8(v4u{it[2].x, it[2].y, it[3].x, it[3].y}), as8(b1[t]), z4);
        n0[t] = mfma16(as8(v4u{it[6].x, 0u, 0u, 0u}), as8(bx[t]), n0[t]);
        n1[t] = mfma16(as8(v4u{0u, it[6].y, 0u, 0u}), as8(bx[t]), n1[t]);
      }
#pragma unroll
      for (int t = 0; t < 2; ++t) {
        const float r0 = __builtin_amdgcn_rcpf(nx[t][2] + 1e-6f), r1 = __builtin_amdgcn_rcpf(nx[t][3] + 1e-6f);
        const u32 ob = W_O + (team * 8 + 2 * w + t) * 20480;
        st4c(pk(nx[t][0] * r0, nx[t][1] * r1), ws, ve, ob + 16384 + rank * 256);
        st16c(v4u{pk(n0[t][0] * r0, n0[t][1] * r0), pk(n0[t][2] * r0, n0[t][3] * r0), pk(n1[t][0] * r1, n1[t][1] * r1), pk(n1[t][2] * r1, n1[t][3] * r1)}, ws,
              lane * 16, ob + rank * 1024);
      }
    }
    {
      const u32 tk = th * 64 + w * 16 + i16;
      const u32 xo = tk * 1280 + q * 16, xso = team * 128 * 1280 + hq * 160;
      const rsrc_t rx = mk_rsrc(x, SEQ * DM * 2);
      const v4u xr0 = __builtin_amdgcn_raw_buffer_load_b128(rx, xo, xso, 0), xr1 = __builtin_amdgcn_raw_buffer_load_b128(rx, xo + 64, xso, 0);
      const v2u xr2 = __builtin_amdgcn_raw_buffer_load_b64(rx, tk * 1280 + 128 + q * 8, xso, 0);
      constexpr int OD = 6;
      s8 wf[OD][5];
      u32 bo[2] = {L_WO + lane * 16, L_WO + 51200 + lane * 16};
      OPQ(bo[0]); OPQ(bo[1]);
      auto ldo1 = [&](int ks, int t) LAM { wf[ks % OD][t] = lld<s8>(bo[ks / 10] + (ks % 10) * 5120 + t * 1024); };
#pragma unroll
      for (int ks = 0; ks < OD; ++ks)
#pragma unroll
        for (int t = 0; t < 5; ++t) ldo1(ks, t);
      WAITV(3);
      st8c(tag, ws, lane << 26, W_FS2 + (bid * 4 + w) * 128);
      lds_inc(S_OP);
      STAMP(9);
      __builtin_amdgcn_s_waitcnt(0xC07F);
      __builtin_amdgcn_s_sleep(PD2);
      SB();
      poll_flags(ws, W_FS2 + ((team * 16 + lane) * 4 + th * 2 + (w >> 1)) * 128, lane < 16, tag);
      STAMP(10);
      v4u of[20];
      const u32 oso = W_O + (team * 8 + th * 4 + w) * 20480;
      constexpr int PD = 12;
#pragma unroll
      for (int ks = 0; ks < PD; ++ks) of[ks] = ld16c(ws, lane * 16, oso + ks * 1024);
      SB();
      f4 ha[5] = {z4, z4, z4, z4, z4};
#pragma unroll
      for (int ks = 0; ks < 20; ++ks) {
#pragma unroll
        for (int t = 0; t < 5; ++t) {
          ha[t] = mfma16(wf[ks % OD][t], as8(of[ks]), ha[t]);
          if (t == 0 && ks + PD < 20) of[ks + PD] = ld16c(ws, lane * 16, oso + (ks + PD) * 1024);
          if (t == 1 && ks + PD == 19) lds_inc(S_OL);
          if (ks + OD < 20) ldo1(ks + OD, t);
          SB();
        }
      }
      STAMP(11);
      v4u hc[2];
#pragma unroll
      for (int m = 0; m < 2; ++m) {
        const f4 lo = ha[2 * m], hi = ha[2 * m + 1];
        const v4u xr = m ? xr1 : xr0;
        hc[m] = v4u{pk(lo[0] + blo(xr.x), lo[1] + bhi(xr.x)), pk(lo[2] + blo(xr.y), lo[3] + bhi(xr.y)), pk(hi[0] + blo(xr.z), hi[1] + bhi(xr.z)), pk(hi[2] + blo(xr.w), hi[3] + bhi(xr.w))};
      }
      const v2u ht = {pk(ha[4][0] + blo(xr2.x), ha[4][1] + bhi(xr2.x)), pk(ha[4][2] + blo(xr2.y), ha[4][3] + bhi(xr2.y))};
      const u32 hb = W_H + (team * 4 + th * 2 + (w >> 1)) * 40960 + hq * 5120;
      const u32 t32 = (w & 1) * 16 + i16;
      st16c(hc[0], ws, (q >> 1) * 1024 + (t32 + 32 * (q & 1)) * 16, hb);
      st16c(hc[1], ws, (q >> 1) * 1024 + (t32 + 32 * (q & 1)) * 16, hb + 2048);
      st8c(ht, ws, (t32 + 32 * (q >> 1)) * 16 + 8 * (q & 1), hb + 4096);
      float a[2] = {0.f, 0.f}, c[2] = {0.f, 0.f};
#pragma unroll
      for (int m = 0; m < 2; ++m) {
#pragma unroll
        for (int j = 0; j < 4; ++j) { a[m] = dot2(hc[m][j], 0x3F803F80u, a[m]); c[m] = dot2(hc[m][j], hc[m][j], c[m]); }
        a[m] = dot2(ht[m], 0x3F803F80u, a[m]); c[m] = dot2(ht[m], ht[m], c[m]);
      }
      float a2 = a[0] + a[1], c2 = c[0] + c[1];
      red2(a2, c2);
      if (q == 0) st8c(v2u{fu(a2), fu(c2)}, ws, tk * 8, W_STAT + (team * 8 + hq) * 1024);
      lst<v4u>(L_HST + w * 2560 + lane * 16, hc[0]);
      lst<v4u>(L_HST + w * 2560 + 1024 + lane * 16, hc[1]);
      lst<v2u>(L_HST + w * 2560 + 2048 + lane * 8, ht);
    }
    WAITV(0);
    st8c(tag, ws, lane << 26, W_FS3 + (bid * 4 + w) * 128);
    lds_inc(S_WOC);
    STAMP(12);
    __builtin_amdgcn_s_sleep(PD2);
    poll_flags(ws, W_FS3 + ((team * 16 + (w >> 1) * 8 + (lane >> 1)) * 4 + 2 * (w & 1) + (lane & 1)) * 128, lane < 16, tag);
    STAMP(13);
    {
      const u32 hh = lane >> 5, t32 = lane & 31;
      v2u sv[4];
#pragma unroll
      for (int n = 0; n < 4; ++n) sv[n] = ld8c(ws, (hh * 4 + n) * 1024 + t32 * 8, W_STAT + team * 8192 + w * 256);
      v4u hf[40];
      constexpr int HD = 12;
      const u32 hso = W_H + (team * 4 + w) * 40960;
#pragma unroll
      for (int s = 0; s < HD; ++s) hf[s] = ld16c(ws, lane * 16, hso + s * 1024);
      SB();
      lds_wait(L_LAND, 4);
      u32 lb[3] = {L_HS + lane * 16, L_HS + 3 * HSLOT + lane * 16, L_HS + 6 * HSLOT + lane * 16};
      OPQ(lb[0]); OPQ(lb[1]); OPQ(lb[2]);
      auto ldw = [&](int n, int s) LAM -> s8 {
        const int hs = (2 * n + s / 20 + 5) % 7;
        return lld<s8>(lb[hs / 3] + (hs % 3) * HSLOT + (s % 20) * 1024);
      };
      const u32 csb = L_CS3 + hh * 32;
      auto ldcs = [&](int n) LAM -> f16v {
        const f4 g0 = lld<f4>(csb + n * 128), g1 = lld<f4>(csb + n * 128 + 16), u0 = lld<f4>(csb + n * 128 + 64), u1 = lld<f4>(csb + n * 128 + 80);
        return __builtin_shufflevector(__builtin_shufflevector(g0, g1, 0, 1, 2, 3, 4, 5, 6, 7), __builtin_shufflevector(u0, u1, 0, 1, 2, 3, 4, 5, 6, 7), 0, 1, 2, 3, 4,
                                       5, 6, 7, 8, 9, 10, 11, 12, 13, 14, 15);
      };
      f16v ac[3];
      ac[0] = ldcs(0); ac[1] = ldcs(1); ac[2] = ldcs(2);
      constexpr int RD = 4;
      s8 rg[RD];
#pragma unroll
      for (int f = 0; f < RD; ++f) rg[f] = ldw(f & 1, f >> 1);
      SB();
      float nmean, rstd;
      {
        const float a = (uf(sv[0].x) + uf(sv[1].x)) + (uf(sv[2].x) + uf(sv[3].x)), c = (uf(sv[0].y) + uf(sv[1].y)) + (uf(sv[2].y) + uf(sv[3].y));
        auto r = __builtin_amdgcn_permlane32_swap(fu(a), fu(a), false, false);
        auto r2 = __builtin_amdgcn_permlane32_swap(fu(c), fu(c), false, false);
        nmean = -(uf(r[0]) + uf(r[1])) * (1.0f / DM);
        rstd = __builtin_amdgcn_rsqf(__builtin_fmaxf(__builtin_fmaf(-nmean, nmean, (uf(r2[0]) + uf(r2[1])) * (1.0f / DM)), 0.f) + 1e-5f);
      }
#pragma unroll
      for (int j = 0; j < 16; ++j) { ac[0][j] *= nmean; ac[1][j] *= nmean; }
      SB();
      u32 lv;
      auto endpass = [&](int n) LAM {
        lds_inc(L_CONS + 4 * n);
        lds_check(lv, L_LAND + 4 * n, 4);
        SB();
      };
#pragma unroll
      for (int f = 0; f < 80; ++f) {
        ac[f & 1] = mfma32(rg[f % RD], as8(hf[f >> 1]), ac[f & 1]);
        const int fn = f + RD;
        rg[f % RD] = fn < 80 ? ldw(fn & 1, fn >> 1) : ldw(2, fn - 80);
        if (f >= 8 && f < 24) ac[2][f - 8] *= nmean;
        if ((f & 1) == 0 && (f >> 1) + HD < 40) hf[(f >> 1) + HD] = ld16c(ws, lane * 16, hso + ((f >> 1) + HD) * 1024);
        if (f == 2 * (39 - HD) + 1) lds_inc(S_HL);
        if (f == 72) lv = *(volatile LAS u32*)(L_LAND + 4);
        SB();
      }
      STAMP(14);
      endpass(1);
      const u32 gvo = (t32 >> 4) * 54 * 1024 + ((t32 & 15) + 16 * hh) * 16 + (rank & 1) * 512;
      const u32 gso = W_G + (team * 8 + 2 * w) * 54 * 1024 + (rank >> 1) * 1024;
      const u32 fso = W_FS4 + (bid * 4 + w) * 1024, fvo = lane << 26;
      const float k1 = -2.0f * 0.7978845608028654f * 1.4426950408889634f, k2 = k1 * 0.044715f;
      float gg, uu, tv, ov[2];
      v4u gp;
      auto gstep = [&](const f16v& a, int j, int m) LAM {
        if (m == 0) { gg = a[j] * rstd; tv = gg * gg; tv = __builtin_fmaf(tv, k2, k1); }
        else if (m == 1) { tv = tv * gg; uu = a[8 + j] * rstd; tv = __builtin_amdgcn_exp2f(tv); }
        else if (m == 2) { uu = uu * gg; tv = tv + 1.0f; tv = __builtin_amdgcn_rcpf(tv); }
        else { ov[j & 1] = uu * tv; if (j & 1) gp[j >> 1] = pk(ov[0], ov[1]); }
      };
      auto pass = [&](int n, bool last) LAM {
#pragma unroll
        for (int s = 0; s < 40; ++s) {
          ac[n % 3] = mfma32(rg[s % RD], as8(hf[s]), ac[n % 3]);
          const int sn = s + RD;
          if (sn < 40) rg[s % RD] = ldw(n, sn);
          else if (!last) rg[s % RD] = ldw(n + 1, sn - 40);
          if (n == 2) {
            if (s < 32) { const int un = s >> 4, j = (s & 15) >> 1; gstep(ac[un], j, 2 * (s & 1)); gstep(ac[un], j, 2 * (s & 1) + 1); }
            if (s == 15) st16c(gp, ws, gvo, gso);
            if (s == 17) ac[0] = ldcs(3);
            if (s == 31) st16c(gp, ws, gvo, gso + 8192);
            if (s >= 32) { ac[0][2 * (s - 32)] *= nmean; ac[0][2 * (s - 32) + 1] *= nmean; }
          } else if (last) {
            if (s >= 1 && s < 17) { gstep(ac[(n - 1) % 3], (s - 1) >> 1, 2 * ((s - 1) & 1)); gstep(ac[(n - 1) % 3], (s - 1) >> 1, 2 * ((s - 1) & 1) + 1); }
            if (s == 16) { WAITV(0); st8c(tag, ws, fvo, fso + (n - 1) * 128); st16c(gp, ws, gvo, gso + (n - 1) * 8192); }
          } else {
            if (s >= 1 && s < 33) gstep(ac[(n - 1) % 3], (s - 1) >> 2, (s - 1) & 3);
            if (s == 32) st16c(gp, ws, gvo, gso + (n - 1) * 8192);
            if (!last) {
              if (s == 0) ac[(n + 1) % 3] = ldcs(n + 1);
              if (s >= 32) { ac[(n + 1) % 3][2 * (s - 32)] *= nmean; ac[(n + 1) % 3][2 * (s - 32) + 1] *= nmean; }
            }
            if (s == 30) {
              WAITV(0);
              st8c(tag, ws, fvo, fso + (n - 1) * 128);
            }
          }
          if (s == 33 && !last) lv = *(volatile LAS u32*)(L_LAND + 4 * n);
          SB();
        }
        if (!last) endpass(n);
      };
      auto tail = [&](int n) LAM {
        lds_inc(L_CONS + 24);
#pragma unroll
        for (int s = 0; s < 32; ++s) gstep(ac[n % 3], s >> 2, s & 3);
        st16c(gp, ws, gvo, gso + n * 8192);
        STAMP(30);
        WAITV(1);
        st8c(tag, ws, fvo, fso + n * 128);
        STAMP(31);
        WAITV(1);
        st8c(tag, ws, fvo, fso + (n + 1) * 128);
      };
      pass(2, false); STAMP(26); pass(3, false); STAMP(27); pass(4, false); STAMP(28); pass(5, false); STAMP(29);
      if (u7) { pass(6, true); STAMP(15); tail(6); }
      else { STAMP(15); tail(5); }
    }
    STAMP(16);
    {
      const u32 ln = lane_id();
      const u32 fv = W_FS4 + ((team * 16 + 4 * w + (ln >> 4)) * 4 + 2 * th + ((ln >> 3) & 1)) * 1024;
      if (!u7) __builtin_amdgcn_s_sleep(24);
#pragma unroll 1
      for (u32 l = 5; l < (w < 3 ? 8u : 7u); ++l) {
        poll_level(ws, fv, tag, l);
        *(volatile LAS u32*)(L_LEV + w * 4) = l;
      }
      *(volatile LAS u32*)(L_LEV + w * 4) = 7u;
    }
    bar();
    STAMP(17);
    {
      const u32 ln = lane_id();
      f4 r[5][4];
#pragma unroll
      for (int n = 0; n < 5; ++n)
#pragma unroll
        for (int j = 0; j < 4; ++j) r[n][j] = lld<f4>(L_HS + w * HSLOT + j * 5120 + n * 1024 + ln * 16);
      v4u hc[2];
      hc[0] = lld<v4u>(L_HST + w * 2560 + ln * 16); hc[1] = lld<v4u>(L_HST + w * 2560 + 1024 + ln * 16);
      const v2u ht = lld<v2u>(L_HST + w * 2560 + 2048 + ln * 8);
      SB();
#pragma unroll
      for (int n = 0; n < 5; ++n) r[n][0] = (r[n][0] + r[n][1]) + (r[n][2] + r[n][3]);
      const rsrc_t ro = mk_rsrc(out, SEQ * DM * 2);
      const u32 oso = (team * 128 + th * 64 + w * 16) * 1280 + hq * 160, ovo = (ln & 15) * 1280;
#pragma unroll
      for (int m = 0; m < 2; ++m) {
        const f4 lo = r[2 * m][0], hi = r[2 * m + 1][0];
        const v4u o = {pk(lo[0] + blo(hc[m].x), lo[1] + bhi(hc[m].x)), pk(lo[2] + blo(hc[m].y), lo[3] + bhi(hc[m].y)),
                       pk(hi[0] + blo(hc[m].z), hi[1] + bhi(hc[m].z)), pk(hi[2] + blo(hc[m].w), hi[3] + bhi(hc[m].w))};
        st16p(o, ro, ovo + (ln >> 4) * 16 + 64 * m, oso);
      }
      const v2u o = {pk(r[4][0][0] + blo(ht.x), r[4][0][1] + bhi(ht.x)), pk(r[4][0][2] + blo(ht.y), r[4][0][3] + bhi(ht.y))};
      __builtin_amdgcn_raw_buffer_store_b64(o, ro, ovo + 128 + (ln >> 4) * 8, oso, 0);
      if (b == 0 && w == 0) st8c(tag, ws, ln << 26, W_EPOCH);
    }
    STAMP(18);

  } else {
    const u32 j = wv;
    __builtin_amdgcn_s_setprio(HPRIO);
    const u32 lane = lane_id();
    const u32 q = lane >> 4, i16 = lane & 15, tq = (lane >> 2) & 3, tp = lane & 3, vl = lane * 16;
    const u32 p1 = j * 256 + b;
    const bool isq = p1 < 600;
    const u32 pp = isq ? p1 : p1 - 600, pks = (pp * (isq ? 2185u : 6554u)) >> 16, pcb1 = pp - pks * (isq ? 30 : 10);
    u32 pv0, ps0, psn, pl0 = L_A + j * 4096;
    {
      const u32 jr = lane >> 3;
      const bool ex = pks >= 16;
      const u32 ra = 20 * (jr & 1) + 16 + (jr >> 1), rb = 20 * (jr >> 2) + (jr & 3);
      const u32 m1 = isq ? 3840 : 1280, m2 = isq ? 8 : ex ? 40 : 4, m3 = isq ? 32 : ex ? 160 : 40, m4 = (!isq && ex) ? 2560 : 0;
      pv0 = (isq ? jr : ex ? ra : rb) * m1 + (lane & 7) * 16;
      psn = m2 * m1;
      ps0 = (m3 * pks - m4) * m1 + pcb1 * 128;
    }
    OPS(ps0); OPS(psn); OPS(pl0); OPQ(pv0);
    if (lane < 32) lst<u32>(L_SYNC + lane * 4, 0u);
    GET_ARGS();
    const v4u sws = mk_srd(wsp, W_END);
    {
      const v4u sp = mk_srd((const void*)(isq ? ka[1] : ka[3]), pks >= 20 ? 0u : isq ? DM * 3 * DM * 2 : DM * DM * 2);
#pragma unroll
      for (int n = 0; n < 4; ++n) dma16(pv0, sp, ps0 + n * psn, pl0 + n * 1024);
    }
    bar();
    u64 ep;
    asm volatile("s_load_dwordx2 %0, %1, 0x0" : "=s"(ep) : "s"(wsp));
    if (j == 3) {
      const v4u sp = mk_srd((const void*)ka[2], 32 * 400 * 2);
      dma16(vl, sp, rank * 1600, L_WPHI); dma16(vl, sp, rank * 1600 + 1024, L_WPHI + 1024);
    }
    u32 selv = i16 == q ? 0x3F803F80u : 0u;
    OPQ(selv);
    const s8 sel = as8(v4u{selv, selv, selv, selv});
    const u32 pi4 = ((tp & 1) << 1) | (tp >> 1);
    {
      u32 dv, cv;
      if (isq) {
        const u32 sec = (pcb1 * 26) >> 8, cn = (pcb1 - sec * 10) * 64 + lane, head = (cn * 3277) >> 16, d = cn - head * 20, hh = head & 1;
        const u32 par = pks & 1;
        const u32 tb = sec == 0 ? par * 2048 : sec * 3072 - 3072, xb = sec == 0 ? 4096 - 256 + par * 128 : 2048 - 256 - 128 + sec * 128;
        const u32 ds = W_W1 + (sec == 0 ? 102400 + (pks >> 1) * 5120 : pks * 5120);
        dv = (head >> 1) * 153600 + d * 16 + (d < 16 ? tb + hh * 1024 : xb + hh * 64) + ds;
        cv = i16 < 4 ? W_CS1 + pks * 7680 + pcb1 * 256 + i16 * 64 + q * 16 : OOB;
      } else {
        const u32 sgi = 4 * pcb1 + q, cc = (sgi * 52) >> 8, pos = sgi - cc * 5;
        const u32 o1 = ((pos & 2) + (i16 >> 3)) * 1024 + (8 * (pos & 1) + (i16 & 7)) * 16, o2 = 4096 + swap23(i16) * 16;
        dv = pks >= 20 ? OOB : cc * 102400 + (pos < 4 ? o1 : o2) + W_WO + pks * 5120;
        cv = OOB;
      }
      u32 rb = L_A + j * 4096 + tq * 128 + q * 32 + (isq ? tp : pi4) * 8;
      OPQ(dv); OPQ(rb); OPQ(cv);
      WAITV(0);
      v4u f[4];
#pragma unroll
      for (int n = 0; n < 4; ++n) f[n] = __builtin_bit_cast(v4u, cat(trd(rb + n * 1024), trd(rb + n * 1024 + 512)));
      f4 cs = z4;
#pragma unroll
      for (int n = 0; n < 4; ++n) { st16c(f[n], ws, dv + n * 256, 0); cs = mfma16(as8(f[n]), sel, cs); }
      st16c(__builtin_bit_cast(v4u, cs), ws, cv, 0);
      v4u sent = {SENT, SENT, SENT, SENT};
      OPQ(sent);
      st16c(sent, ws, vl, W_PART + (rank * 16 + team) * 8192 + j * 2048);
      st16c(sent, ws, vl, W_PART + (rank * 16 + team) * 8192 + j * 2048 + 1024);
      st16c(sent, ws, lane < 4 ? vl : OOB, W_RED + rank * 4096 + team * 256 + j * 64);
      SB();
    }
    STAMP(1);
    if (j == 3) {
      v2u pl[2][4];
      const int src = ((lane & ~3u) | tq) * 4;
#pragma unroll
      for (int h = 0; h < 2; ++h) {
        const u32 pb = L_WPHI + h * 800;
        pl[h][0] = __builtin_bit_cast(v2u, trd(pb + (4 * q + tq) * 40 + 8 * tp)); pl[h][1] = __builtin_bit_cast(v2u, trd(pb + (16 + tq) * 40 + 8 * tp));
        pl[h][2] = __builtin_bit_cast(v2u, trd(pb + (4 * q + tq) * 40 + 32)); pl[h][3] = __builtin_bit_cast(v2u, trd(pb + (16 + tq) * 40 + 32));
      }
      asm volatile("" ::: "memory");
#pragma unroll
      for (int h = 0; h < 2; ++h) {
        const u32 qh = q == (u32)h ? ~0u : 0u, rh = tp == (u32)h ? ~0u : 0u;
        lst<v4u>(L_WPHI + h * 1024 + vl, v4u{pl[h][0].x, pl[h][0].y, pl[h][1].x & qh, pl[h][1].y & qh});
        lst<v4u>(L_PH2 + h * 1024 + vl, v4u{(u32)__builtin_amdgcn_ds_bpermute(src, pl[h][2].x) & rh, (u32)__builtin_amdgcn_ds_bpermute(src, pl[h][2].y) & rh,
                                            (u32)__builtin_amdgcn_ds_bpermute(src, pl[h][3].x) & rh & qh, (u32)__builtin_amdgcn_ds_bpermute(src, pl[h][3].y) & rh & qh});
      }
    }
    GET_EPOCH();
    WAITV(0);
    lds_inc(S_PST);
    if (j == 0) { lds_wait<0>(S_PST); st8c(tag, ws, lane << 26, W_FA + bid * FAS); }
    STAMP(9);
    u32 rdv[2], rdn[2], rcv[2], rvo[2], rs0[2], rsn[2];
    v4u rsp[2];
#pragma unroll
    for (int e = 0; e < 2; ++e) {
      const u32 piece = p1 + e * 1024;
      const bool isg = piece < 1080;
      const u32 p2 = isg ? piece : piece - 1080;
      const u32 pk_ = (p2 * (isg ? 1214u : 6554u)) >> 16, pcb = p2 - pk_ * (isg ? 54 : 10);
      const u32 mat = pcb >= 27 ? 1 : 0, rs = isg ? 3456 : 1280, u0 = 4 * (pcb - mat * 27);
      rsp[e] = mk_srd((const void*)(!isg ? ka[6] : mat ? ka[5] : ka[4]), piece < 1620 ? DM * FF * 2 : 0u);
      rs0[e] = pk_ * 32 * rs + u0 * 32; rsn[e] = 8 * rs; rvo[e] = (lane >> 3) * rs + (lane & 7) * 16;
      const u32 sgi = u0 + q, cc = (sgi * 52) >> 8, pos = sgi - cc * 5;
      const u32 o1 = ((pos & 2) + (i16 >> 3)) * 1024 + (8 * (pos & 1) + (i16 & 7)) * 16, o2 = 4096 + swap23(i16) * 16;
      const u32 dg = W_WGU + sgi * 40960 + i16 * 16 + pk_ * 2048 + mat * 256, dd = W_WD + cc * (54 * 5120) + (pos < 4 ? o1 : o2) + pk_ * 5120;
      rdv[e] = isg ? dg : piece < 1620 ? dd : OOB;
      rdn[e] = isg ? 512 : 256;
      rcv[e] = (isg && i16 < 4) ? W_CSP + (pk_ * 108 + u0 + i16) * 128 + mat * 64 + (((q & 1) << 1) | (q >> 1)) * 16 : OOB;
      OPQ(rdv[e]); OPQ(rcv[e]); OPQ(rvo[e]); OPS(rs0[e]); OPS(rsn[e]); OPS(rdn[e]);
      rsp[e].x = sgpr(rsp[e].x); rsp[e].y = sgpr(rsp[e].y); rsp[e].z = sgpr(rsp[e].z);
    }
    u32 c1v;
    { const u32 t = (j & 1) * 64 + lane, sec = (t * 1639) >> 16; c1v = t < 120 ? (sec * 600 + t) * 4 + (j >> 1) * (10 * 7680) : OOB; OPQ(c1v); }
    STAMP(23);
    __builtin_amdgcn_s_sleep(FAD);
    {
      u32 fv = (j * 64 + lane) * FAS;
#pragma unroll 1
      for (u32 s = 0; s < SPIN_CAP; ++s) {
        const v2u v = ld8c(ws, fv, W_FA);
        fv = (v.x == tag.x && v.y == tag.y) ? OOB : fv;
        if (wall(fv == OOB)) break;
        __builtin_amdgcn_s_sleep(FAR);
        asm volatile("" ::: "memory");
      }
    }
    WAITV(0);
    lds_inc(S_FA);
    lds_wait<0>(S_FA);
    STAMP(24);
    {
      u32 w1s = W_W1 + rank * 153600 + j * 5120, w1l = L_A + j * 5120;
      OPS(w1s); OPS(w1l);
      dma5c(vl, sws, w1s, w1l); dma5c(vl, sws, w1s + 20480, w1l + 20480);
      WAITV(5); lds_inc(S_L1);
      dma5c(vl, sws, w1s + 2 * 20480, w1l + 2 * 20480); dma5c(vl, sws, w1s + 3 * 20480, w1l + 3 * 20480);
      WAITV(10); lds_inc(S_L1 + 4);
      dma5c(vl, sws, w1s + 4 * 20480, w1l + 4 * 20480);
      WAITV(10); lds_inc(S_L1 + 8);
      u32 cv[10];
#pragma unroll
      for (int k = 0; k < 10; ++k) cv[k] = ld4c(ws, c1v, W_CS1 + k * 7680 + rank * 160);
      v4u rt[2][4];
#pragma unroll
      for (int e = 0; e < 2; ++e) {
        const rsrc_t sp = mk_rsrc((const void*)(((u64)rsp[e].y << 32) | rsp[e].x), rsp[e].z);
#pragma unroll
        for (int n = 0; n < 4; ++n) rt[e][n] = __builtin_amdgcn_raw_buffer_load_b128(sp, rvo[e], rs0[e] + n * rsn[e], 0);
      }
      WAITV(23); lds_inc(S_L1 + 12);
      WAITV(18); lds_inc(S_L1 + 16);
      {
        const float c = (((uf(cv[0]) + uf(cv[1])) + (uf(cv[2]) + uf(cv[3]))) + ((uf(cv[4]) + uf(cv[5])) + (uf(cv[6]) + uf(cv[7])))) + (uf(cv[8]) + uf(cv[9]));
        lst<float>(L_CS1 + j * 256 + lane * 4, c);
      }
      lds_inc(S_CS);
      const u32 rb = L_KV + j * 1024 + tq * 128 + q * 32 + pi4 * 8;
#pragma unroll
      for (int e = 0; e < 2; ++e) {
        v4u rf[4];
        f4 cs = z4;
#pragma unroll
        for (int n = 0; n < 4; ++n) {
          lst<v4u>(L_KV + j * 1024 + vl, rt[e][n]);
          asm volatile("" ::: "memory");
          rf[n] = __builtin_bit_cast(v4u, cat(trd(rb), trd(rb + 512)));
          asm volatile("" ::: "memory");
        }
#pragma unroll
        for (int n = 0; n < 4; ++n) { cs = mfma16(as8(rf[n]), sel, cs); st16c(rf[n], ws, rdv[e], n * rdn[e]); }
        st16c(__builtin_bit_cast(v4u, cs), ws, rcv[e], 0);
      }
      lds_wait(S_XD);
      dma5c(vl, sws, w1s + 5 * 20480, w1l + 5 * 20480); dma5c(vl, sws, w1s + 6 * 20480, w1l + 6 * 20480);
      WAITV(10);
      lds_inc(S_RT);
      if (j < 2) dma5c(vl, sws, w1s + 7 * 20480, w1l + 7 * 20480);
      if (j == 3) { lds_wait<0>(S_RT); st8c(tag, ws, lane << 26, W_FB + bid * 32); }
    }
    STAMP(5);
    STAMP(25);
    u32 wos = W_WO + hq * 102400;
    OPS(wos);
    v4u wf[14][5], gf[14][4];
    const u32 wso = W_WD + (hq * 54 + 2 * j) * 5120;
    const u32 gso = W_G + (team * 8 + th * 4) * 54 * 1024 + 2 * j * 1024;
    const u32 slast = j < 3 ? 0u : 0x80000000u;
    auto ldw1 = [&](int i, int t) LAM { wf[i][t] = ld16c(ws, (i >= 12 ? vl | slast : vl) + t * 1024, wso + ((i >> 1) * 8 + (i & 1)) * 5120); };
    auto ldg1 = [&](int i, int r) LAM { gf[i][r] = ld16c(ws, i >= 12 ? vl | slast : vl, gso + r * 54 * 1024 + ((i >> 1) * 8 + (i & 1)) * 1024); };
    STAMP(2);
    lds_wait<2>(S_KVD);
#pragma unroll
    for (int n = 0; n < 3; ++n) dma5c(vl, sws, wos + (8 + 4 * n + j) * 5120, L_WO + (8 + 4 * n + j) * 5120);
    WAITV(15);
    STAMP(3);
    kv_job<true>(ws, j + 4, W_PART + (rank * 16 + team) * 8192);
    STAMP(4);
    bar_nw();
    dma5c(vl, sws, wos + j * 5120, L_WO + j * 5120);
    dma5c(vl, sws, wos + (4 + j) * 5120, L_WO + (4 + j) * 5120);
    if (j == 3) {
      const u32 pvo = (lane >> 5) * 8 * 8192 + (lane & 31) * 16, pso = W_PART + rank * 16 * 8192 + team * 512;
      v4u pv[8];
      __builtin_amdgcn_s_setprio(3);
      __builtin_amdgcn_s_sleep(HOPD);
#pragma unroll 1
      for (u32 s = 0; s < SPIN_CAP; ++s) {
#pragma unroll
        for (int n = 0; n < 8; ++n) pv[n] = ld16c(ws, pvo, pso + n * 8192);
        SB();
        u32 mx = 0;
#pragma unroll
        for (int n = 0; n < 8; ++n) mx = umax4(mx, pv[n]);
        if (wall(mx != SENT)) break;
        asm volatile("" ::: "memory");
      }
      float sm[4];
#pragma unroll
      for (int k = 0; k < 4; ++k) {
        const float a = ((uf(pv[0][k]) + uf(pv[1][k])) + (uf(pv[2][k]) + uf(pv[3][k]))) + ((uf(pv[4][k]) + uf(pv[5][k])) + (uf(pv[6][k]) + uf(pv[7][k])));
        auto r = __builtin_amdgcn_permlane32_swap(fu(a), fu(a), false, false);
        sm[k] = uf(r[0]) + uf(r[1]);
      }
      st8c(v2u{san(pk(sm[0], sm[1])), san(pk(sm[2], sm[3]))}, ws, lane < 32 ? lane * 8 : OOB, W_RED + rank * 4096 + team * 256);
      STAMP(6);
    } else if (j == 2) { poll256(ws, W_FB, 32, lane, tag); lds_inc(S_FB); }
    lds_wait(S_FB, 1);
    STAMP(22);
    sfor<0, 5 * NW1>([&](auto ic) LAM { ldw1(decltype(ic)::v / 5, decltype(ic)::v % 5); });
    SB();
    if (j == 3) {
      __builtin_amdgcn_s_sleep(HOPE);
      v4u kv[4];
#pragma unroll 1
      for (u32 s = 0; s < SPIN_CAP; ++s) {
#pragma unroll
        for (int n = 0; n < 4; ++n) kv[n] = ld16c(ws, vl, W_RED + rank * 4096 + n * 1024);
        SB();
        u32 mx = 0;
#pragma unroll
        for (int n = 0; n < 4; ++n) mx = umax4(mx, kv[n]);
        if (wall(mx != SENT)) break;
        asm volatile("" ::: "memory");
      }
#pragma unroll
      for (int n = 0; n < 4; ++n) lst<v4u>(L_KV + n * 1024 + vl, kv[n]);
    }
    WAITV(0);
    STAMP(7);
    bar();
    __builtin_amdgcn_s_setprio(HPRIO);
    STAMP(8);
    auto stage_u = [&](u32 hs) LAM {
      const u32 so = W_WGU + ((16 * (hs >> 1) + rank) * 40 + (hs & 1) * 20 + j * 5) * 1024, dst = L_HS + ((hs + 5) % 7) * HSLOT + j * 5120;
      dma5c(vl, sws, so, dst);
    };
    lds_wait(S_OP);
    stage_u(0); stage_u(1);
    {
      const u32 t = j * 64 + lane, u = 16 * (t >> 5) + rank;
      if (u < 108) {
        u32 cv[20];
#pragma unroll
        for (int k = 0; k < 20; ++k) cv[k] = ld4c(ws, u * 128 + (t & 31) * 4, W_CSP + k * 108 * 128);
        SB();
        float c = 0.f;
#pragma unroll
        for (int k = 0; k < 20; ++k) c += uf(cv[k]);
        lst<float>(L_CS3 + t * 4, c);
      }
    }
    u32 sdb[3] = {L_HS + j * 5120 + vl, L_HS + 3 * HSLOT + j * 5120 + vl, L_HS + 6 * HSLOT + j * 5120 + vl};
    OPQ(sdb[0]); OPQ(sdb[1]); OPQ(sdb[2]);
    auto spill = [&](int i) LAM {
#pragma unroll
      for (int t = 0; t < 5; ++t) lst<v4u>(sdb[((i + 1) % 7) / 3] + (((i + 1) % 7) % 3) * HSLOT + t * 1024, wf[i][t]);
      SB();
    };
    auto ldw = [&](auto lo, auto hi) LAM { sfor<decltype(lo)::v * 5, decltype(hi)::v * 5>([&](auto ic) LAM { ldw1(decltype(ic)::v / 5, decltype(ic)::v % 5); }); SB(); };
    auto ldg = [&](int i) LAM {
#pragma unroll
      for (int r = 0; r < 4; ++r) ldg1(i, r);
      SB();
    };
    STAMP(10);
    lds_wait(S_OL);
    ldw(IC<NW1>{}, IC<NW2>{});
    lds_wait<2>(S_WOC);
    STAMP(11);
    auto landed = [&](int k) LAM { lds_inc(L_LAND + 4 * k); };
    stage_u(2); stage_u(3); stage_u(4);
    WAITV(0); landed(0);
    STAMP(13);
    stage_u(5); stage_u(6);
    WAITV(0); landed(1);
    lds_wait<2>(S_HL);
    ldw(IC<NW2>{}, IC<9>{});
    lds_wait<2>(L_CONS + 4);
    stage_u(7); stage_u(8); stage_u(9); stage_u(10);
    WAITV(10); landed(2);
    WAITV(0); landed(3);
    lds_wait<2>(L_CONS + 8);
    stage_u(11);
    if (u7) stage_u(12);
    ldw(IC<9>{}, IC<11>{});
    WAITV(10); landed(4);
    const u32 fvo = W_FS4 + ((team * 16 + 4 * j + (lane >> 4)) * 4 + 2 * th + ((lane >> 3) & 1)) * 1024;
    lds_wait<2>(L_CONS + 12);
    if (u7) stage_u(13);
    poll_level(ws, fvo, tag, 2);
    WAITV(0); landed(5);
    STAMP(26);
    spill(4); ldg(0);
    lds_wait<2>(L_CONS + 16);
    STAMP(27);
    spill(5); spill(6); ldg(1); ldg(2);
    lds_wait<2>(L_CONS + 20);
    STAMP(28);
    v2u fl = ld8c(ws, fvo, 3 * 128);
    spill(7); spill(8); ldg(3);
    if (!wall(fl.x == tag.x && fl.y == tag.y)) poll_level(ws, fvo, tag, 3);
    asm volatile("" ::: "memory");
    STAMP(29);
    lds_wait<1>(L_CONS + 24);
    STAMP(16);
    __builtin_amdgcn_s_setprio(0);
    spill(9); spill(10);
    f4 da[4][5];
#pragma unroll
    for (int r = 0; r < 4; ++r)
#pragma unroll
      for (int t = 0; t < 5; ++t) da[r][t] = z4;
    u32 lv;
    sfor<0, 14>([&](auto ic) LAM {
      constexpr int i = decltype(ic)::v;
      SB();
#pragma unroll
      for (int m = 0; m < 20; ++m) {
        const int r = m / 5, t = m % 5;
        da[r][t] = mfma16(as8(wf[i][t]), as8(gf[i][r]), da[r][t]);
        if constexpr (i == 0) { if (m == 0) fl = ld8c(ws, fvo, 4 * 128); }
        if constexpr (i <= 1) { if (t == 4) ldg1(i + 4, r); }
        if constexpr (i == 3 || i == 5 || i == 7 || i == 10) { if (m < 8) ldg1((i == 3 ? 6 : i == 5 ? 8 : i == 7 ? 10 : 12) + (m >> 2), m & 3); }
        if constexpr (i >= 8 && i <= 10) { if (m >= 10 && m < 15) ldw1(i + 3, m - 10); }
        if constexpr (i >= 3 && i <= 9) { if (m >= 12 && m < 17) wf[i + 1][m - 12] = lld<v4u>(sdb[((i + 2) % 7) / 3] + (((i + 2) % 7) % 3) * HSLOT + (m - 12) * 1024); }
        if constexpr (i == 4 || i == 6 || i == 9) { if (m == 10) lv = *(volatile LAS u32*)(L_LEV + j * 4); }
        if constexpr (i == 13) { if (m >= 6) lst<f4>(sdb[((m - 6) / 5) / 3] + (((m - 6) / 5) % 3) * HSLOT + ((m - 6) % 5) * 1024, da[(m - 6) / 5][(m - 6) % 5]); }
        SB();
      }
      if constexpr (i == 2) {
        if (!wall(fl.x == tag.x && fl.y == tag.y)) poll_level(ws, fvo, tag, 4);
        asm volatile("" ::: "memory");
      }
      if constexpr (i == 4 || i == 6 || i == 9) {
        STAMP(i == 4 ? 30 : i == 6 ? 14 : 15);
        lds_check(lv, L_LEV + j * 4, i == 4 ? 5u : i == 6 ? 6u : 7u);
        STAMP(i == 4 ? 31 : i == 6 ? 17 : 12);
      }
      SB();
    });
    STAMP(18);
#pragma unroll
    for (int m = 14; m < 20; ++m) lst<f4>(sdb[(m / 5) / 3] + ((m / 5) % 3) * HSLOT + (m % 5) * 1024, da[m / 5][m % 5]);
    bar();
  }

}

extern "C" void kernel_launch(void* const* d_in, const int* in_sizes, int n_in, void* d_out, int out_size, void* d_ws, size_t ws_size,
                              hipStream_t stream) {
  (void)hipFuncSetAttribute((const void*)fused_block, hipFuncAttributeMaxDynamicSharedMemorySize, L_BYTES);
  fused_block<<<256, 512, L_BYTES, stream>>>((const u16*)d_in[0], (const u16*)d_in[1], (const u16*)d_in[2], (const u16*)d_in[3],
                                             (const u16*)d_in[5], (const u16*)d_in[6], (const u16*)d_in[7], (u16*)d_out, (char*)d_ws);
}
```

```cpp
#include <hip/hip_runtime.h>
#include <hip/hip_bf16.h>

typedef unsigned short u16;
typedef unsigned int u32;
typedef unsigned long long u64;
typedef u32 v4u __attribute__((ext_vector_type(4)));
typedef u32 v2u __attribute__((ext_vector_type(2)));
typedef float f4 __attribute__((ext_vector_type(4)));
typedef float f2 __attribute__((ext_vector_type(2)));
typedef float f16v __attribute__((ext_vector_type(16)));
typedef short s4 __attribute__((ext_vector_type(4)));
typedef short s8 __attribute__((ext_vector_type(8)));
typedef __bf16 bf2 __attribute__((ext_vector_type(2)));
#define LAS __attribute__((address_space(3)))
#define CAS __attribute__((address_space(4)))
#if defined(__HIP_DEVICE_COMPILE__)
#define NOPK __attribute__((target("no-packed-fp32-ops")))
#else
#define NOPK
#endif
#define DEV __device__ __forceinline__ NOPK
#define SB() __builtin_amdgcn_sched_barrier(0)
#define OPQ(x) asm volatile("" : "+v"(x))
typedef __amdgpu_buffer_rsrc_t rsrc_t;

#define DBG 0
#define ROT ((rank + 3 * (team & 1)) % 10)
#define P1ST 0
#define HOPA0 28
#define HOPA1 56
#define HOPB 1
#define FAS 32
#define NRM1 3
#define NRM2 8
#define HOPA 2
#define NWPRE 2
#define HOPD 12
#define HOPE 4
#define FAD 0
#define FAR 4
#define PD2 14
#define KDEP 6
#define FBD 0
#define LVS 8
#define WD6 8
#define NW2 6
#define NW0 4
#define NW1 4
#define XD3 1
#define HPRIO 0
#define RDEP 4
#define CODEPF 1
#define CODE_BYTES 45056
#define HROT 0
#define OROT 0
#define P4PRIO 3
#define ABL 0
#define NW3 7
#define WLP 0
#define PACE 63
#define XMAP 4

constexpr int SEQ = 2048, DM = 640, FF = 1728;
constexpr u32 W_EPOCH = 0;
constexpr u32 W_FA = 4096;
constexpr u32 W_FB = W_FA + 256 * 128;
constexpr u32 W_FS2 = W_FB + 256 * 32;
constexpr u32 W_FS3 = W_FS2 + 256 * 4 * 128;
constexpr u32 W_FS4 = W_FS3 + 256 * 4 * 128;
constexpr u32 W_RED = W_FS4 + 256 * 4 * 8 * 128;
constexpr u32 W_STAT = W_RED + 65536;
constexpr u32 W_CSP = W_STAT + 131072;
constexpr u32 W_CS1 = W_CSP + 20 * 108 * 128;
constexpr u32 W_PART = W_CS1 + 20 * 7680;
constexpr u32 W_O = W_PART + 16 * 16 * 8192;
constexpr u32 W_H = W_O + 16 * 8 * 20 * 1024;
constexpr u32 W_G = W_H + 16 * 4 * 40 * 1024;
constexpr u32 W_WGU = W_G + 16 * 8 * 54 * 1024;
constexpr u32 W_WD = W_WGU + 108 * 40 * 1024;
constexpr u32 W_WO = W_WD + 8 * 54 * 5 * 1024;
constexpr u32 W_W1 = W_WO + 8 * 20 * 5 * 1024;
constexpr u32 W_DBG = W_W1 + 16 * 153600;
constexpr u32 W_END = W_DBG + 256 * 8 * 32 * 4;
constexpr u32 L_WPHI = 0;
constexpr u32 L_CS3 = 3200;
constexpr u32 L_SYNC = 3072;
constexpr u32 L_CS1 = 2048;
constexpr u32 S_PST = L_SYNC, S_FA = L_SYNC + 4, S_XD = L_SYNC + 8, S_CS = L_SYNC + 12, S_KVD = L_SYNC + 16, S_WOC = L_SYNC + 20, S_RT = L_SYNC + 24, S_FB = L_SYNC + 28, S_OP = L_SYNC + 52, S_OL = L_SYNC + 56, S_HL = L_SYNC + 60;
constexpr u32 S_L1 = L_SYNC + 32, S_SP = L_SYNC + 64, S_HP = L_SYNC + 68, L_LAND = L_SYNC + 80, L_CONS = L_SYNC + 100;
constexpr u32 L_KV = 4096;
constexpr u32 L_A = 8192;
constexpr u32 L_B = L_A + 102400;
constexpr u32 L_PH2 = L_B + 51200;
constexpr u32 L_IMG = L_A, L_WO = L_A;
constexpr u32 L_HS = L_A, HSLOT = 20480;
constexpr u32 L_HST = L_HS + 7 * HSLOT;
constexpr u32 L_BYTES = 163840;
constexpr u32 OOB = 0x80000000u;
constexpr u32 SENT = 0xFFFFFFFFu;
constexpr u32 SPIN_CAP = 1u << 18;

DEV v4u mk_srd(const void* p, u32 bytes) { return v4u{(u32)(size_t)p, (u32)((size_t)p >> 32), bytes, 0x00020000u}; }
DEV rsrc_t mk_rsrc(const void* p, u32 bytes) { return __builtin_amdgcn_make_buffer_rsrc((void*)p, 0, bytes, 0x00020000); }
DEV void dma16(u32 voff, v4u srd, u32 soff, u32 lds) {
  asm volatile("s_mov_b32 m0, %2\n\ts_nop 0\n\tbuffer_load_dwordx4 %0, %1, %3 offen lds" ::"v"(voff), "s"(srd), "s"(lds), "s"(soff) : "memory");
}
DEV void dma16c(u32 voff, v4u srd, u32 soff, u32 lds) {
  asm volatile("s_mov_b32 m0, %2\n\ts_nop 0\n\tbuffer_load_dwordx4 %0, %1, %3 offen sc1 lds" ::"v"(voff), "s"(srd), "s"(lds), "s"(soff) : "memory");
}
DEV void dma_a(u32 voff, v4u srd, u32 sb, u32 sa, u32 lb, u32 la) {
  u32 t;
  asm volatile("s_add_u32 m0, %5, %6\n\ts_add_u32 %0, %3, %4\n\tbuffer_load_dwordx4 %1, %2, %0 offen lds"
               : "=&s"(t) : "v"(voff), "s"(srd), "s"(sb), "s"(sa), "s"(lb), "s"(la) : "memory", "scc");
}
DEV void dma_ac(u32 voff, v4u srd, u32 sb, u32 sa, u32 lb, u32 la) {
  u32 t;
  asm volatile("s_add_u32 m0, %5, %6\n\ts_add_u32 %0, %3, %4\n\tbuffer_load_dwordx4 %1, %2, %0 offen sc1 lds"
               : "=&s"(t) : "v"(voff), "s"(srd), "s"(sb), "s"(sa), "s"(lb), "s"(la) : "memory", "scc");
}
DEV void dma_i(u32 voff, v4u srd, u32 sb, u32 sa, u32 lb, u32 la) {
  u32 t;
  asm volatile("s_add_u32 m0, %5, %6\n\ts_add_u32 %0, %3, %4\n\tbuffer_load_dwordx4 %1, %2, %0 offen lds"
               : "=&s"(t) : "v"(voff), "s"(srd), "s"(sb), "i"(sa), "s"(lb), "i"(la) : "memory", "scc");
}
DEV void dma5c(u32 voff, v4u srd, u32 so, u32 lds) {
  u32 t;
  asm volatile("s_mov_b32 m0, %4\n\ts_add_u32 %0, %3, 0x1000\n\t"
               "buffer_load_dwordx4 %1, %2, %3 offen sc1 lds\n\t"
               "buffer_load_dwordx4 %1, %2, %3 offen offset:1024 sc1 lds\n\t"
               "buffer_load_dwordx4 %1, %2, %3 offen offset:2048 sc1 lds\n\t"
               "buffer_load_dwordx4 %1, %2, %3 offen offset:3072 sc1 lds\n\t"
               "s_add_u32 m0, %4, 0x1000\n\ts_nop 0\n\t"
               "buffer_load_dwordx4 %1, %2, %0 offen sc1 lds"
               : "=&s"(t) : "v"(voff), "s"(srd), "s"(so), "s"(lds) : "memory", "scc");
}
DEV u32 sgpr(u32 x) {
  u32 o;
  asm volatile("s_mov_b32 %0, %1" : "=s"(o) : "s"(__builtin_amdgcn_readfirstlane(x)));
  return o;
}
#define OPS(x) x = sgpr(x)
template <int N> DEV void waitv() { asm volatile("s_waitcnt vmcnt(%0)" ::"n"(N) : "memory"); }
#define WAITV(n) asm volatile("s_waitcnt vmcnt(" #n ")" ::: "memory")
DEV void bar() {
  asm volatile("" ::: "memory");
  __builtin_amdgcn_s_waitcnt(0xC07F);
  __builtin_amdgcn_s_barrier();
  asm volatile("" ::: "memory");
  SB();
}
DEV void bar_nw() {
  asm volatile("" ::: "memory");
  __builtin_amdgcn_s_barrier();
  asm volatile("" ::: "memory");
  SB();
}
DEV s4 trd(u32 a) { return __builtin_amdgcn_ds_read_tr16_b64_v4i16((LAS s4*)a); }
DEV s8 cat(s4 a, s4 b) { return __builtin_shufflevector(a, b, 0, 1, 2, 3, 4, 5, 6, 7); }
DEV s8 as8(v4u a) { return __builtin_bit_cast(s8, a); }
DEV s4 as4(v2u a) { return __builtin_bit_cast(s4, a); }
DEV f4 mfma16(s8 a, s8 b, f4 c) { return __builtin_amdgcn_mfma_f32_16x16x32_bf16(a, b, c, 0, 0, 0); }
DEV f4 mfma16h(s4 a, s4 b, f4 c) { return __builtin_amdgcn_mfma_f32_16x16x16bf16_1k(a, b, c, 0, 0, 0); }
DEV f16v mfma32(s8 a, s8 b, f16v c) { return __builtin_amdgcn_mfma_f32_32x32x16_bf16(a, b, c, 0, 0, 0); }
DEV u32 pk(float a, float b) { return __builtin_bit_cast(u32, __builtin_convertvector(f2{a, b}, bf2)); }
DEV float blo(u32 x) { return __builtin_bit_cast(float, x << 16); }
DEV float bhi(u32 x) { return __builtin_bit_cast(float, x & 0xFFFF0000u); }
DEV float dot2(u32 a, u32 b, float c) { return __builtin_amdgcn_fdot2_f32_bf16(__builtin_bit_cast(bf2, a), __builtin_bit_cast(bf2, b), c, false); }
DEV u32 fu(float x) { return __builtin_bit_cast(u32, x); }
DEV float uf(u32 x) { return __builtin_bit_cast(float, x); }
template <class T> DEV T lld(u32 a) { return *(LAS T*)a; }
template <class T> DEV void lst(u32 a, T v) { *(LAS T*)a = v; }
DEV v4u ld16c(rsrc_t r, u32 vo, u32 so) { return __builtin_amdgcn_raw_buffer_load_b128(r, vo, so, 16); }
DEV v2u ld8c(rsrc_t r, u32 vo, u32 so) { return __builtin_amdgcn_raw_buffer_load_b64(r, vo, so, 16); }
DEV u32 ld4c(rsrc_t r, u32 vo, u32 so) { return __builtin_amdgcn_raw_buffer_load_b32(r, vo, so, 16); }
DEV void st16c(v4u v, rsrc_t r, u32 vo, u32 so) {
  __builtin_amdgcn_raw_buffer_store_b128(v, r, vo, so, 16);
  asm volatile("s_nop 1" ::"v"(v) : "memory");
}
DEV void st16p(v4u v, rsrc_t r, u32 vo, u32 so) {
  __builtin_amdgcn_raw_buffer_store_b128(v, r, vo, so, 0);
  asm volatile("s_nop 1" ::"v"(v) : "memory");
}
DEV void st8c(v2u v, rsrc_t r, u32 vo, u32 so) { __builtin_amdgcn_raw_buffer_store_b64(v, r, vo, so, 16); }
DEV void st4c(u32 v, rsrc_t r, u32 vo, u32 so) { __builtin_amdgcn_raw_buffer_store_b32(v, r, vo, so, 16); }
DEV bool wall(bool c) { return __builtin_amdgcn_ballot_w64(!c) == 0ull; }
DEV u32 san(u32 x) { return x < 0xFFFFFFFEu ? x : 0xFFFFFFFEu; }
DEV u32 swap23(u32 k) { return (k & ~12u) | ((k & 4u) << 1) | ((k & 8u) >> 1); }

template <bool MAXOP, bool RCP> DEV void red4(float& a, float& b, float& c, float& d) {
  auto r1 = __builtin_amdgcn_permlane16_swap(fu(a), fu(b), false, false);
  auto r2 = __builtin_amdgcn_permlane16_swap(fu(c), fu(d), false, false);
  float ab = MAXOP ? __builtin_elementwise_maximum(uf(r1[0]), uf(r1[1])) : uf(r1[0]) + uf(r1[1]);
  float cd = MAXOP ? __builtin_elementwise_maximum(uf(r2[0]), uf(r2[1])) : uf(r2[0]) + uf(r2[1]);
  auto r3 = __builtin_amdgcn_permlane32_swap(fu(ab), fu(cd), false, false);
  float z = MAXOP ? __builtin_elementwise_maximum(uf(r3[0]), uf(r3[1])) : uf(r3[0]) + uf(r3[1]);
  if (RCP) z = __builtin_amdgcn_rcpf(z);
  auto g1 = __builtin_amdgcn_permlane32_swap(fu(z), fu(z), false, false);
  auto g2 = __builtin_amdgcn_permlane16_swap(g1[0], g1[0], false, false);
  auto g3 = __builtin_amdgcn_permlane16_swap(g1[1], g1[1], false, false);
  a = uf(g2[0]); b = uf(g2[1]); c = uf(g3[0]); d = uf(g3[1]);
}
DEV void red2(float& a, float& b) {
  auto r1 = __builtin_amdgcn_permlane16_swap(fu(a), fu(b), false, false);
  float ab = uf(r1[0]) + uf(r1[1]);
  auto r3 = __builtin_amdgcn_permlane32_swap(fu(ab), fu(ab), false, false);
  float z = uf(r3[0]) + uf(r3[1]);
  auto g2 = __builtin_amdgcn_permlane16_swap(fu(z), fu(z), false, false);
  a = uf(g2[0]); b = uf(g2[1]);
}

#define LAM __attribute__((always_inline)) NOPK
template <int V> struct IC { static constexpr int v = V; };
template <int A, int B, class F> DEV void sfor(F&& f) {
  if constexpr (A < B) { f(IC<A>{}); sfor<A + 1, B>(f); }
}
struct Phi { v2u pl[2], mi[2], ex; };
DEV float fmx(float a, float b) { return __builtin_elementwise_maximum(a, b); }
DEV float fmn(float a, float b) { return __builtin_elementwise_minimum(a, b); }
DEV void phi_mm(s8 b0, s8 b1, const s8 (&A)[4], f4 (&p)[3]) {
  const f4 z = {0.f, 0.f, 0.f, 0.f};
  p[2] = mfma16(A[2], b0, z);
  p[0] = mfma16(A[0], b0, z);
  p[1] = mfma16(A[1], b1, z);
  p[2] = mfma16(A[3], b1, p[2]);
}
DEV Phi phi_sm(const f4 (&pp)[3]) {
  const float p[2][5] = {{pp[0][0], pp[0][1], pp[0][2], pp[0][3], pp[2][0]}, {pp[1][0], pp[1][1], pp[1][2], pp[1][3], pp[2][1]}};
  float mx[2], nm[2];
#pragma unroll
  for (int h = 0; h < 2; ++h) {
    mx[h] = fmx(fmx(fmx(fmx(p[h][0], p[h][1]), p[h][2]), p[h][3]), p[h][4]);
    nm[h] = fmx(fmx(fmx(fmx(-p[h][0], -p[h][1]), -p[h][2]), -p[h][3]), -p[h][4]);
  }
  red4<true, false>(mx[0], nm[0], mx[1], nm[1]);
  float ep[2][5], em[2][5], sp[2], sm[2];
#pragma unroll
  for (int h = 0; h < 2; ++h) {
#pragma unroll
    for (int j = 0; j < 5; ++j) {
      ep[h][j] = __builtin_amdgcn_exp2f(p[h][j] - mx[h]);
      em[h][j] = __builtin_amdgcn_exp2f(-p[h][j] - nm[h]);
    }
    sp[h] = ((ep[h][0] + ep[h][1]) + (ep[h][2] + ep[h][3])) + ep[h][4];
    sm[h] = ((em[h][0] + em[h][1]) + (em[h][2] + em[h][3])) + em[h][4];
  }
  red4<false, true>(sp[0], sm[0], sp[1], sm[1]);
  Phi o;
#pragma unroll
  for (int h = 0; h < 2; ++h) {
    o.pl[h] = v2u{pk(ep[h][0] * sp[h], ep[h][1] * sp[h]), pk(ep[h][2] * sp[h], ep[h][3] * sp[h])};
    o.mi[h] = v2u{pk(em[h][0] * sm[h], em[h][1] * sm[h]), pk(em[h][2] * sm[h], em[h][3] * sm[h])};
    o.ex[h] = pk(ep[h][4] * sp[h], em[h][4] * sm[h]);
  }
  return o;
}
DEV v2u lnpk(f4 a, f4 cs, float nmean, float sc) {
  return v2u{pk(__builtin_fmaf(nmean, cs[0], a[0]) * sc, __builtin_fmaf(nmean, cs[1], a[1]) * sc),
             pk(__builtin_fmaf(nmean, cs[2], a[2]) * sc, __builtin_fmaf(nmean, cs[3], a[3]) * sc)};
}
DEV f4 lcs(u32 a) { return lld<f4>(a) + lld<f4>(a + 512); }
DEV u32 bsel(u32 m, u32 a, u32 b) { return (a & m) | (b & ~m); }
DEV void poll_flags(rsrc_t ws, u32 voff, bool active, v2u tag) {
#pragma unroll 1
  for (u32 s = 0; s < SPIN_CAP; ++s) {
    v2u v = tag;
    if (active) v = ld8c(ws, voff, 0);
    if (wall(v.x == tag.x && v.y == tag.y)) break;
    asm volatile("" ::: "memory");
  }
  asm volatile("" ::: "memory");
}
template <int SL = 1> DEV void lds_check(u32 lv, u32 a, u32 n) {
  u32 sv, cnt;
  asm volatile(
      "s_nop 0\n\tv_readfirstlane_b32 %0, %2\n\ts_cmp_ge_u32 %0, %4\n\ts_cbranch_scc1 2f\n\ts_mov_b32 %1, 0x400000\n"
      "1:\n\tv_mov_b32 %2, %3\n\tds_read_b32 %2, %2\n\ts_waitcnt lgkmcnt(0)\n\tv_readfirstlane_b32 %0, %2\n\ts_cmp_ge_u32 %0, %4\n\ts_cbranch_scc1 2f\n\t"
      "s_sleep %5\n\ts_sub_u32 %1, %1, 1\n\ts_cbranch_scc0 1b\n2:"
      : "=&s"(sv), "=&s"(cnt), "+v"(lv) : "s"(a), "s"(n), "n"(SL) : "memory", "scc");
}
template <int SL = 1> DEV void lds_wait(u32 a, u32 n = 4) { lds_check<SL>(0u, a, n); }
DEV void lds_inc(u32 a) {
  u32 t0, t1;
  asm volatile("v_mov_b32 %0, %2\n\tv_mov_b32 %1, 1\n\ts_mov_b64 exec, 1\n\tds_add_u32 %0, %1\n\ts_mov_b64 exec, -1" : "=&v"(t0), "=&v"(t1) : "s"(a) : "memory");
}
DEV void flag_store(v4u srd, v2u tag, u32 so) {
  v2u t;
  asm volatile("v_mov_b64 %0, %2\n\ts_mov_b64 exec, 1\n\tbuffer_store_dwordx2 %0, off, %1, %3 sc1\n\ts_mov_b64 exec, -1"
               : "=&v"(t) : "s"(srd), "s"(tag), "s"(so) : "memory");
}
DEV void poll_mask(rsrc_t ws, u32 fv, u32 base, v2u tag, v2u v) {
#pragma unroll 1
  for (u32 s = 0; s < SPIN_CAP; ++s) {
    fv = (v.x == tag.x && v.y == tag.y) ? OOB : fv;
    if (wall(fv == OOB)) break;
    asm volatile("" ::: "memory");
    v = ld8c(ws, fv, base);
  }
  asm volatile("" ::: "memory");
}
DEV void poll256(rsrc_t ws, u32 base, u32 stride, u32 lane, v2u tag) {
#pragma unroll 1
  for (u32 s = 0; s < SPIN_CAP; ++s) {
    v2u f[4];
    bool ok = true;
#pragma unroll
    for (int n = 0; n < 4; ++n) f[n] = ld8c(ws, lane * stride, base + n * 64 * stride);
#pragma unroll
    for (int n = 0; n < 4; ++n) ok = ok && f[n].x == tag.x && f[n].y == tag.y;
    if (wall(ok)) break;
    asm volatile("" ::: "memory");
  }
  asm volatile("" ::: "memory");
}
DEV u32 lane_id() {
  u32 l;
  asm volatile("v_mbcnt_lo_u32_b32 %0, -1, 0\n\tv_mbcnt_hi_u32_b32 %0, -1, %0" : "=v"(l));
  return l;
}
DEV u32 umax(u32 a, u32 b) { return a > b ? a : b; }
DEV u32 umax4(u32 m, v4u v) { return umax(umax(umax(umax(m, v.x), v.y), v.z), v.w); }

#define STAMP(i) do {} while (0)

DEV void poll_level(rsrc_t ws, u32 voff, v2u tag, u32 level) {
#pragma unroll 1
  for (u32 s = 0; s < SPIN_CAP; ++s) {
    const v2u v = ld8c(ws, voff, level * 128);
    voff = (v.x == tag.x && v.y == tag.y) ? OOB : voff;
    if (wall(voff == OOB)) break;
    __builtin_amdgcn_s_sleep(LVS);
    asm volatile("" ::: "memory");
  }
  asm volatile("" ::: "memory");
}

template <bool TWO> DEV void kv_job(rsrc_t ws, u32 jb, u32 so) {
  const u32 lane = lane_id();
  const u32 q = lane >> 4, tq = (lane >> 2) & 3, tp = lane & 3;
  const u32 a1c = !TWO ? jb * 32 : jb < 6 ? (jb - 4) * 32 : 128;
  const u32 b1c = !TWO ? 160 + (jb >> 1) * 32 : jb == 6 ? 160 : 224;
  const u32 a2c = jb < 6 ? a1c + 64 : a1c;
  const u32 b2c = jb == 6 ? 192 : b1c;
  const u32 rb = L_IMG + (2 * (q >> 1) + 16 * (q & 1) + 4 * tq) * 264 + 8 * tp;
  constexpr int NO = TWO ? 4 : 2;
  u32 ra[4] = {rb + a1c, rb + b1c, rb + a2c, rb + b2c};
  u32 mlo = (jb != 6 && (lane & 1)) ? ~0u : 0u, mhi = (jb == 6 || (lane & 1)) ? ~0u : 0u, vl = lane * 16;
#pragma unroll
  for (int n = 0; n < NO; ++n) OPQ(ra[n]);
  OPQ(vl);
  if (TWO) { OPQ(mlo); OPQ(mhi); }
  bar();
  s8 fr[4][NO];
#pragma unroll
  for (int ks = 0; ks < 4; ++ks)
#pragma unroll
    for (int n = 0; n < NO; ++n) fr[ks][n] = cat(trd(ra[n] + ks * 32 * 264), trd(ra[n] + ks * 32 * 264 + 264));
  SB();
  f4 d1 = {0.f, 0.f, 0.f, 0.f}, d2 = d1;
#pragma unroll
  for (int ks = 0; ks < 4; ++ks) {
    d1 = mfma16(fr[ks][0], fr[ks][1], d1);
    if (TWO) d2 = mfma16(fr[ks][NO - 2], fr[ks][NO - 1], d2);
  }
  if (TWO) d1 = f4{uf(bsel(mlo, fu(d2[0]), fu(d1[0]))), uf(bsel(mlo, fu(d2[1]), fu(d1[1]))), uf(bsel(mhi, fu(d2[2]), fu(d1[2]))), uf(bsel(mhi, fu(d2[3]), fu(d1[3])))};
  st16c(v4u{san(fu(d1[0])), san(fu(d1[1])), san(fu(d1[2])), san(fu(d1[3]))}, ws, vl, so + jb * 1024);
}

typedef u64 v8q __attribute__((ext_vector_type(8)));
__global__ __launch_bounds__(512, 2) NOPK void fused_block(const u16* x_, const u16* wqkv_, const u16* wphi_, const u16* wo_, const u16* wg_, const u16* wu_,
                                                           const u16* wd_, u16* out_, char* wsp_) {
  v8q ka;
  u64 kw;
  asm volatile("s_load_dwordx16 %0, %2, 0x0\n\ts_load_dwordx2 %1, %2, 0x40" : "=&s"(ka), "=&s"(kw) : "s"(__builtin_amdgcn_kernarg_segment_ptr()));
  u64 pc;
  asm volatile("s_getpc_b64 %0" : "=s"(pc));
  SB();
  STAMP(0);
  const u32 wv = __builtin_amdgcn_readfirstlane(threadIdx.x >> 6);
  const u32 b = blockIdx.x, xcd = b & 7, idx = b >> 3;
  const u32 team = (xcd >> 1) * 4 + (idx >> 3), rank = ((idx & 4) << 1) | ((xcd & 1) << 2) | (idx & 3);
  const u32 bid = team * 16 + rank, hq = rank & 7, th = rank >> 3;
  const bool u7 = rank < 12;
  const f4 z4 = {0.f, 0.f, 0.f, 0.f};
#define DUMP()
#define GET_ARGS()                                                                                         \
  SB();                                                                                                    \
  asm volatile("s_waitcnt lgkmcnt(0)" ::: "memory");                                                       \
  SB();                                                                                                    \
  asm volatile("" : "+s"(ka), "+s"(kw));                                                                   \
  char* const wsp = (char*)kw;                                                                             \
  const rsrc_t ws = mk_rsrc(wsp, W_END);                                                                   \
  const v4u sws = mk_srd(wsp, W_END);
#define GET_EPOCH()                                                                                        \
  asm volatile("s_waitcnt lgkmcnt(0)" ::: "memory");                                                       \
  SB();                                                                                                    \
  asm volatile("" : "+s"(ep));                                                                             \
  ep = (ep & ~15ull) + 16ull;                                                                              \
  const v2u tag = {(u32)ep, (u32)(ep >> 32)};

  if (wv >= 4) {
    const u32 w = wv - 4;
    const u32 lane = lane_id();
    const u32 q = lane >> 4, i16 = lane & 15;
    {
      const v4u sc = mk_srd((const void*)(pc & ~255ull), CODE_BYTES);
      if (w == 0) asm volatile("s_mov_b32 m0, %2\n\ts_nop 0\n\tbuffer_load_dword %0, %1, 0 offen lds" ::"v"(lane < 16 ? (lane * 32 + idx) * 128 : OOB), "s"(sc), "s"(L_KV) : "memory");
    }
    const u32 vx0 = (lane >> 3) * 1280 + (((lane & 7) ^ (lane >> 4)) << 4), vx1 = (vx0 ^ 64) + 8 * 1280;
    u32 xs0 = (team * 128 + w * 32) * 1280, xl0 = L_B + w * 12288;
    OPS(xs0); OPS(xl0);
    GET_ARGS();
    const u16* const x = (const u16*)ka[0];
    u16* const out = (u16*)ka[7];
    const v4u sx = mk_srd(x, SEQ * DM * 2);
    bar_nw();
    u64 ep;
    asm volatile("s_load_dwordx2 %0, %1, 0x0" : "=s"(ep) : "s"(wsp));
    auto stagex = [&](int c) LAM {
      dma_i(vx0, sx, xs0, c * 128, xl0, (c % 3) * 4096);
      dma_i(vx1, sx, xs0, c * 128, xl0, (c % 3) * 4096 + 1024);
      dma_i(vx0, sx, xs0, c * 128 + 16 * 1280, xl0, (c % 3) * 4096 + 2048);
      dma_i(vx1, sx, xs0, c * 128 + 16 * 1280, xl0, (c % 3) * 4096 + 3072);
    };
    stagex(0); stagex(1);
    stagex(2);
    v4u xa[2][20];
    float nm1[2], rs1[2];
    {
      v4u onesv = {0x3F803F80u, 0x3F803F80u, 0x3F803F80u, 0x3F803F80u};
      OPQ(onesv);
      const s8 ones = as8(onesv);
      f4 as[2] = {z4, z4}, ag[2] = {z4, z4};
      u32 ax0 = L_B + w * 12288 + i16 * 128 + ((q ^ ((i16 >> 1) & 7)) << 4), ax1 = ax0 ^ 64;
      OPQ(ax0); OPQ(ax1);
      sfor<0, 11>([&](auto cc_) LAM {
        constexpr int c = decltype(cc_)::v;
        if constexpr (c < 10) waitv<(c < 8 ? 8 : c == 8 ? 4 : 0)>();
        __builtin_amdgcn_s_waitcnt(0xC07F);
        SB();
        if constexpr (c < 10) {
#pragma unroll
          for (int t = 0; t < 2; ++t) { xa[t][2 * c] = lld<v4u>(ax0 + (c % 3) * 4096 + t * 2048); xa[t][2 * c + 1] = lld<v4u>(ax1 + (c % 3) * 4096 + t * 2048); }
        }
        if constexpr (c < 7) { __builtin_amdgcn_s_waitcnt(0xC07F); SB(); stagex(c + 3); }
        if constexpr (c == 10) lds_inc(S_XD);
        if constexpr (c >= 1) {
#pragma unroll
          for (int k = 2 * c - 2; k < 2 * c; ++k)
#pragma unroll
            for (int t = 0; t < 2; ++t) { as[t] = mfma16(ones, as8(xa[t][k]), as[t]); ag[t] = mfma16(as8(xa[t][k]), as8(xa[t][k]), ag[t]); }
        }
        SB();
      });
      STAMP(23);
      float g[2];
#pragma unroll
      for (int t = 0; t < 2; ++t) {
        u32 v = 0;
#pragma unroll
        for (int r = 0; r < 4; ++r) v |= fu(ag[t][r]) & (i16 == 4 * q + r ? ~0u : 0u);
        g[t] = uf(v);
      }
      red2(g[0], g[1]);
#pragma unroll
      for (int t = 0; t < 2; ++t) {
        nm1[t] = -as[t][0] * (1.0f / DM);
        rs1[t] = __builtin_amdgcn_rsqf(__builtin_fmaxf(__builtin_fmaf(-nm1[t], nm1[t], g[t] * (1.0f / DM)), 0.f) + 1e-5f);
        OPQ(nm1[t]); OPQ(rs1[t]);
      }
    }
    GET_EPOCH();
    __builtin_amdgcn_s_setprio(2);
    const float L2E = 1.4426950408889634f;
    {
      f4 acc[2][5] = {{z4, z4, z4, z4, z4}, {z4, z4, z4, z4, z4}};
      u32 bw[2] = {L_A + lane * 16, L_A + 51200 + lane * 16};
      OPQ(bw[0]); OPQ(bw[1]);
      constexpr int KD = KDEP;
      s8 kf[KD + 1];
      u32 lv;
      lds_wait(S_FA);
      { v2u tg = tag; OPQ(tg); st8c(tg, ws, lane_id() << 26, w == 0 ? W_FB + bid * 32 : OOB); }
      lds_wait(S_L1);
      STAMP(25);
#pragma unroll
      for (int f = 0; f < KD; ++f) kf[f] = lld<s8>(bw[0] + f * 1024);
      SB();
      sfor<0, 100>([&](auto gc) LAM {
        constexpr int g = decltype(gc)::v, gn = g + KD;
        if constexpr (gn < 100 && gn % 20 == 0) {
          lds_check(lv, S_L1 + 4 * (gn / 20), 4);

        }
        if constexpr (gn < 100) kf[gn % (KD + 1)] = lld<s8>(bw[gn / 50] + (gn % 50) * 1024);
        if constexpr (gn == 44) lds_inc(S_CS);
        if constexpr (gn % 20 == 14) lv = *(volatile LAS u32*)(gn < 80 ? S_L1 + 4 * (gn / 20 + 1) : S_CS);
        acc[0][g % 5] = mfma16(kf[g % (KD + 1)], as8(xa[0][g / 5]), acc[0][g % 5]);
        acc[1][g % 5] = mfma16(kf[g % (KD + 1)], as8(xa[1][g / 5]), acc[1][g % 5]);
        SB();
      });
      lds_inc(S_KVD);
      lds_check(lv, S_CS, 8);
      STAMP(2);
      const u32 cb = L_CS1 + 160 + q * 16;
      const u32 mq = q < 2 ? ~0u : 0u;
      f4 pp[2][3];
      {
        const f4 c0 = lcs(cb), c1 = lcs(cb + 80), c2 = lcs(L_CS1 + 224 + q * 80);
        const s8 A[4] = {lld<s8>(L_WPHI + lane * 16), lld<s8>(L_WPHI + 1024 + lane * 16), lld<s8>(L_PH2 + lane * 16), lld<s8>(L_PH2 + 1024 + lane * 16)};
#pragma unroll
        for (int t = 0; t < 2; ++t) {
          const float sk = rs1[t] * L2E;
          const v2u k0 = lnpk(acc[t][0], c0, nm1[t], sk), k1 = lnpk(acc[t][1], c1, nm1[t], sk), k2 = lnpk(acc[t][2], c2, nm1[t], uf(bsel(mq, fu(sk), fu(rs1[t]))));
          phi_mm(as8(v4u{k0.x, k0.y, k2.x, k2.y}), as8(v4u{k1.x, k1.y, k2.x, k2.y}), A, pp[t]);
          const u32 e0 = bsel(mq, 0x3F803F80u, k2.x), e1 = bsel(mq, 0x3F803F80u, k2.y), eb = L_IMG + (w * 32 + t * 16 + i16) * 264 + 224 + (q ^ 2) * 2;
          lst<u16>(eb, (u16)e0); lst<u16>(eb + 8, (u16)(e0 >> 16)); lst<u16>(eb + 16, (u16)e1); lst<u16>(eb + 24, (u16)(e1 >> 16));
          SB();
        }
      }
      {
        const f4 c3 = lcs(cb + 160), c4 = lcs(cb + 240);
#pragma unroll
        for (int t = 0; t < 2; ++t) {
          const u32 ib = L_IMG + (w * 32 + t * 16 + i16) * 264;
          lst<v2u>(ib + q * 8 + 160, lnpk(acc[t][3], c3, nm1[t], rs1[t]));
          lst<v2u>(ib + q * 8 + 192, lnpk(acc[t][4], c4, nm1[t], rs1[t]));
        }
      }
      SB();
#pragma unroll
      for (int t = 0; t < 2; ++t) {
        const Phi ph = phi_sm(pp[t]);
        const u32 ib = L_IMG + (w * 32 + t * 16 + i16) * 264 + q * 8;
        lst<v2u>(ib + 0, ph.pl[0]); lst<v2u>(ib + 32, ph.mi[0]); lst<v2u>(ib + 64, ph.pl[1]); lst<v2u>(ib + 96, ph.mi[1]);
        lst<v2u>(ib + 128, ph.ex);
      }
    }
    STAMP(3);
    kv_job<false>(ws, w, W_PART + (rank * 16 + team) * 8192);
    STAMP(4);
    bar_nw();
    {
      const u32 lane = lane_id(), q = lane >> 4, i16 = lane & 15;
      f4 qa[2][3] = {{z4, z4, z4}, {z4, z4, z4}};
      u32 bq = L_B + lane * 16, bq2 = L_B + 4096 + ((lane & 7) + 16 * q) * 16;
      OPQ(bq); OPQ(bq2);
      constexpr int QD = 4;
      s8 qf[QD + 1];
      auto ldq = [&](int f) LAM {
        const int t = f % 3, ks = f / 3;
        qf[f % (QD + 1)] = lld<s8>(t == 2 ? bq2 + (ks >> 1) * 5120 + (ks & 1) * 128 : bq + (ks >> 1) * 5120 + ((ks & 1) * 2 + t) * 1024);
      };
#pragma unroll
      for (int f = 0; f < QD; ++f) ldq(f);
      SB();
#pragma unroll
      for (int f = 0; f < 60; ++f) {
        if (f + QD < 60) ldq(f + QD);
        qa[0][f % 3] = mfma16(qf[f % (QD + 1)], as8(xa[0][f / 3]), qa[0][f % 3]);
        qa[1][f % 3] = mfma16(qf[f % (QD + 1)], as8(xa[1][f / 3]), qa[1][f % 3]);
        SB();
      }
      STAMP(5);
      const f4 c0 = lcs(L_CS1 + q * 16), c1 = lcs(L_CS1 + 80 + q * 16), c2 = lcs(L_CS1 + 64 + (q & 1) * 80);
      const s8 A[4] = {lld<s8>(L_WPHI + lane * 16), lld<s8>(L_WPHI + 1024 + lane * 16), lld<s8>(L_PH2 + lane * 16), lld<s8>(L_PH2 + 1024 + lane * 16)};
      f4 pp[2][3];
      Phi pq[2];
#pragma unroll
      for (int t = 0; t < 2; ++t) {
        const float sk = rs1[t] * L2E;
        const v2u k0 = lnpk(qa[t][0], c0, nm1[t], sk), k1 = lnpk(qa[t][1], c1, nm1[t], sk), k2 = lnpk(qa[t][2], c2, nm1[t], sk);
        phi_mm(as8(v4u{k0.x, k0.y, k2.x, k2.y}), as8(v4u{k1.x, k1.y, k2.x, k2.y}), A, pp[t]);
      }
      v4u b0[2], b1[2], bx[2];
#pragma unroll
      for (int t = 0; t < 2; ++t) {
        pq[t] = phi_sm(pp[t]);
        b0[t] = v4u{pq[t].pl[0].x, pq[t].pl[0].y, pq[t].mi[0].x, pq[t].mi[0].y};
        b1[t] = v4u{pq[t].pl[1].x, pq[t].pl[1].y, pq[t].mi[1].x, pq[t].mi[1].y};
        bx[t] = v4u{pq[t].ex.x, pq[t].ex.y, 0u, 0u};
        OPQ(b0[t]); OPQ(b1[t]); OPQ(bx[t]);
      }
      u32 mo = (lane & 1) ? ~0u : 0u, ve = i16 * 16 + q * 4;
      OPQ(mo); OPQ(ve);
      STAMP(7);
      bar_nw();
      STAMP(8);
      v2u it[8];
#pragma unroll
      for (int n = 0; n < 8; ++n) it[n] = lld<v2u>(L_KV + n * 512 + lane * 8);
      const s8 ae = as8(v4u{it[4].x & ~mo, it[4].y & ~mo, it[5].x & ~mo, it[5].y & ~mo}), ao = as8(v4u{it[4].x & mo, it[4].y & mo, it[5].x & mo, it[5].y & mo});
      const s8 ax = as8(v4u{it[7].x & ~mo, it[7].y & mo, 0u, 0u});
      f4 nx[2], n0[2], n1[2];
#pragma unroll
      for (int t = 0; t < 2; ++t) {
        nx[t] = mfma16(ae, as8(b0[t]), z4);
        nx[t] = mfma16(ao, as8(b1[t]), nx[t]);
        nx[t] = mfma16(ax, as8(bx[t]), nx[t]);
      }
#pragma unroll
      for (int t = 0; t < 2; ++t) {
        n0[t] = mfma16(as8(v4u{it[0].x, it[0].y, it[1].x, it[1].y}), as8(b0[t]), z4);
        n1[t] = mfma16(as8(v4u{it[2].x, it[2].y, it[3].x, it[3].y}), as8(b1[t]), z4);
        n0[t] = mfma16(as8(v4u{it[6].x, 0u, 0u, 0u}), as8(bx[t]), n0[t]);
        n1[t] = mfma16(as8(v4u{0u, it[6].y, 0u, 0u}), as8(bx[t]), n1[t]);
      }
#pragma unroll
      for (int t = 0; t < 2; ++t) {
        const float r0 = __builtin_amdgcn_rcpf(nx[t][2] + 1e-6f), r1 = __builtin_amdgcn_rcpf(nx[t][3] + 1e-6f);
        const u32 ob = W_O + (team * 8 + 2 * w + t) * 20480;
        st4c(pk(nx[t][0] * r0, nx[t][1] * r1), ws, ve, ob + 16384 + rank * 256);
        st16c(v4u{pk(n0[t][0] * r0, n0[t][1] * r0), pk(n0[t][2] * r0, n0[t][3] * r0), pk(n1[t][0] * r1, n1[t][1] * r1), pk(n1[t][2] * r1, n1[t][3] * r1)}, ws,
              lane * 16, ob + rank * 1024);
      }
    }
    {
      const u32 lane = lane_id(), q = lane >> 4, i16 = lane & 15;
      const u32 tk = th * 64 + w * 16 + i16;
      const u32 xo = tk * 1280 + q * 16, xso = team * 128 * 1280 + hq * 160;
      const rsrc_t rx = mk_rsrc(x, SEQ * DM * 2);
      const v4u xr0 = __builtin_amdgcn_raw_buffer_load_b128(rx, xo, xso, 0), xr1 = __builtin_amdgcn_raw_buffer_load_b128(rx, xo + 64, xso, 0);
      const v2u xr2 = __builtin_amdgcn_raw_buffer_load_b64(rx, tk * 1280 + 128 + q * 8, xso, 0);
      constexpr int OD = 6;
      s8 wf[OD][5];
      u32 bo[2] = {L_WO + lane * 16, L_WO + 51200 + lane * 16};
      OPQ(bo[0]); OPQ(bo[1]);
      const u32 oso = W_O + (team * 8 + th * 4 + w) * 20480;
      auto ldo1 = [&](int ks, int t) LAM { wf[ks % OD][t] = lld<s8>(bo[ks / 10] + (ks % 10) * 5120 + t * 1024); };
#pragma unroll
      for (int ks = 0; ks < OD; ++ks)
#pragma unroll
        for (int t = 0; t < 5; ++t) ldo1(ks, t);
      WAITV(3);
      flag_store(sws, tag, W_FS2 + (bid * 4 + w) * 128);
      lds_inc(S_OP);
      STAMP(9);
      f4 ha[5] = {f4{blo(xr0.x), bhi(xr0.x), blo(xr0.y), bhi(xr0.y)}, f4{blo(xr0.z), bhi(xr0.z), blo(xr0.w), bhi(xr0.w)}, f4{blo(xr1.x), bhi(xr1.x), blo(xr1.y), bhi(xr1.y)},
                  f4{blo(xr1.z), bhi(xr1.z), blo(xr1.w), bhi(xr1.w)}, f4{blo(xr2.x), bhi(xr2.x), blo(xr2.y), bhi(xr2.y)}};
#pragma unroll
      for (int t = 0; t < 5; ++t) OPQ(ha[t]);
      __builtin_amdgcn_s_waitcnt(0xC07F);
      __builtin_amdgcn_s_sleep(PD2);
      SB();
      poll_flags(ws, W_FS2 + ((team * 16 + lane) * 4 + th * 2 + (w >> 1)) * 128, lane < 16, tag);
      STAMP(10);
      v4u of[20];
      constexpr int PD = 12;
      auto ldof = [&](int ks) LAM { of[ks] = ld16c(ws, lane * 16 + (ks & 3) * 1024, oso + (ks >> 2) * 4096); };
#pragma unroll
      for (int ks = 0; ks < PD; ++ks) ldof(ks);
      SB();
#pragma unroll
      for (int ks = 0; ks < 20; ++ks) {
#pragma unroll
        for (int t = 0; t < 5; ++t) {
          ha[t] = mfma16(wf[ks % OD][t], as8(of[ks]), ha[t]);
          if (t == 0 && ks + PD < 20) ldof(ks + PD);
          if (t == 1 && ks + PD == 19) lds_inc(S_OL);
          if (ks + OD < 20) ldo1(ks + OD, t);
          SB();
        }
      }
      STAMP(11);
      v4u hc[2];
#pragma unroll
      for (int m = 0; m < 2; ++m)
        hc[m] = v4u{pk(ha[2 * m][0], ha[2 * m][1]), pk(ha[2 * m][2], ha[2 * m][3]), pk(ha[2 * m + 1][0], ha[2 * m + 1][1]), pk(ha[2 * m + 1][2], ha[2 * m + 1][3])};
      const v2u ht = {pk(ha[4][0], ha[4][1]), pk(ha[4][2], ha[4][3])};
      const u32 hb = W_H + (team * 4 + th * 2 + (w >> 1)) * 40960 + hq * 5120;
      const u32 t32 = (w & 1) * 16 + i16;
      st16c(hc[0], ws, (q >> 1) * 1024 + (t32 + 32 * (q & 1)) * 16, hb);
      st16c(hc[1], ws, (q >> 1) * 1024 + (t32 + 32 * (q & 1)) * 16, hb + 2048);
      st8c(ht, ws, (t32 + 32 * (q >> 1)) * 16 + 8 * (q & 1), hb + 4096);
      float a[2] = {0.f, 0.f}, c[2] = {0.f, 0.f};
#pragma unroll
      for (int m = 0; m < 2; ++m) {
#pragma unroll
        for (int j = 0; j < 4; ++j) { a[m] = dot2(hc[m][j], 0x3F803F80u, a[m]); c[m] = dot2(hc[m][j], hc[m][j], c[m]); }
        a[m] = dot2(ht[m], 0x3F803F80u, a[m]); c[m] = dot2(ht[m], ht[m], c[m]);
      }
      float a2 = a[0] + a[1], c2 = c[0] + c[1];
      red2(a2, c2);
      st8c(v2u{fu(a2), fu(c2)}, ws, q == 0 ? tk * 8 : OOB, W_STAT + (team * 8 + hq) * 1024);
      lst<v4u>(L_HST + w * 2560 + lane * 16, hc[0]);
      lst<v4u>(L_HST + w * 2560 + 1024 + lane * 16, hc[1]);
      lst<v2u>(L_HST + w * 2560 + 2048 + lane * 8, ht);
    }
    WAITV(0);
    flag_store(sws, tag, W_FS3 + (bid * 4 + w) * 128);
    lds_inc(S_WOC);
    STAMP(12);
    __builtin_amdgcn_s_sleep(PD2);
    const u32 lane3 = lane_id();
#define lane lane3
    poll_flags(ws, W_FS3 + ((team * 16 + (w >> 1) * 8 + (lane >> 1)) * 4 + 2 * (w & 1) + (lane & 1)) * 128, lane < 16, tag);
    STAMP(13);
    v4u wl[3][5];
    u32 wlv;
    auto ldwl = [&](int k) LAM {
      const int i = 11 + k / 5;
      if (k == 0) wlv = lane_id() * 16;
      wl[k / 5][k % 5] = ld16c(ws, ((i >= 12 && w == 3) ? OOB : wlv) + (k % 5) * 1024, W_WD + (hq * 54 + 2 * w + (i >> 1) * 8 + (i & 1)) * 5120);
    };
    {
      const u32 hh = lane >> 5, t32 = lane & 31;
      v2u sv[4];
#pragma unroll
      for (int n = 0; n < 4; ++n) sv[n] = ld8c(ws, (hh * 4 + n) * 1024 + t32 * 8, W_STAT + team * 8192 + w * 256);
      v4u hf[40];
      constexpr int HD = 12;
      const u32 hso = W_H + (team * 4 + w) * 40960;
      auto ldh = [&](int s) LAM { hf[s] = ld16c(ws, lane * 16 + (s & 3) * 1024, hso + (s >> 2) * 4096); };
#pragma unroll
      for (int s = 0; s < HD; ++s) ldh(s);
      SB();
      lds_wait(L_LAND, 4);
      u32 lb[3] = {L_HS + lane * 16, L_HS + 3 * HSLOT + lane * 16, L_HS + 6 * HSLOT + lane * 16};
      OPQ(lb[0]); OPQ(lb[1]); OPQ(lb[2]);
      auto ldw = [&](int n, int s) LAM -> s8 {
        const int hs = (2 * n + s / 20 + 5) % 7;
        return lld<s8>(lb[hs / 3] + (hs % 3) * HSLOT + (s % 20) * 1024);
      };
      auto ldcs = [&](int n) LAM -> f16v {
        const u32 csb = L_CS3 + (lane_id() & 32);
        const f4 g0 = lld<f4>(csb + n * 128), g1 = lld<f4>(csb + n * 128 + 16), u0 = lld<f4>(csb + n * 128 + 64), u1 = lld<f4>(csb + n * 128 + 80);
        return __builtin_shufflevector(__builtin_shufflevector(g0, g1, 0, 1, 2, 3, 4, 5, 6, 7), __builtin_shufflevector(u0, u1, 0, 1, 2, 3, 4, 5, 6, 7), 0, 1, 2, 3, 4,
                                       5, 6, 7, 8, 9, 10, 11, 12, 13, 14, 15);
      };
      f16v ac[3];
      ac[0] = ldcs(0); ac[1] = ldcs(1); ac[2] = ldcs(2);
      constexpr int RD = RDEP;
      s8 rg[RD];
#pragma unroll
      for (int f = 0; f < RD; ++f) rg[f] = ldw(f & 1, f >> 1);
      SB();
      float nmean, cK1, cK2, cR;
      {
        const float a = (uf(sv[0].x) + uf(sv[1].x)) + (uf(sv[2].x) + uf(sv[3].x)), c = (uf(sv[0].y) + uf(sv[1].y)) + (uf(sv[2].y) + uf(sv[3].y));
        auto r = __builtin_amdgcn_permlane32_swap(fu(a), fu(a), false, false);
        auto r2 = __builtin_amdgcn_permlane32_swap(fu(c), fu(c), false, false);
        nmean = -(uf(r[0]) + uf(r[1])) * (1.0f / DM);
        cR = __builtin_fmaxf(__builtin_fmaf(-nmean, nmean, (uf(r2[0]) + uf(r2[1])) * (1.0f / DM)), 0.f) + 1e-5f;
        const float rstd = __builtin_amdgcn_rsqf(cR), k1 = -2.0f * 0.7978845608028654f * 1.4426950408889634f;
        cK1 = k1 * rstd; cK2 = cK1 * 0.044715f * rstd * rstd;
      }
#pragma unroll
      for (int j = 0; j < 16; ++j) { ac[0][j] *= nmean; ac[1][j] *= nmean; }
      SB();
      u32 lv;
#pragma unroll
      for (int f = 0; f < 80; ++f) {
        ac[f & 1] = mfma32(rg[f % RD], as8(hf[f >> 1]), ac[f & 1]);
        const int fn = f + RD;
        rg[f % RD] = fn < 80 ? ldw(fn & 1, fn >> 1) : ldw(2, fn - 80);
        if (f >= 8 && f < 24) ac[2][f - 8] *= nmean;
        if ((f & 1) == 0 && (f >> 1) + HD < 40) ldh((f >> 1) + HD);
        if (f == 2 * (39 - HD) + 1) lds_inc(S_HL);
        if (f == 80 - RD) lds_inc(L_CONS + 4);
        SB();
      }
      OPQ(cK1); OPQ(cK2); OPQ(cR);
      STAMP(14);
      const u32 gvo = (t32 >> 4) * 54 * 1024 + ((t32 & 15) + 16 * hh) * 16 + (rank & 1) * 512;
      const u32 gso = W_G + (team * 8 + 2 * w) * 54 * 1024 + (rank >> 1) * 1024;
      const u32 fso = W_FS4 + (bid * 4 + w) * 1024;
      float tv, au, pu, pr, ov[2];
      v4u gp;
      auto gstep = [&](const f16v& a, int e) LAM {
        const int j = e / 3, m = e % 3;
        if (m == 0 && j < 8) { tv = a[j] * a[j]; tv = __builtin_fmaf(tv, cK2, cK1); tv = tv * a[j]; au = a[j] * a[8 + j]; }
        if (m == 1) tv = __builtin_amdgcn_exp2f(tv);
        if ((m == 1 && j > 0) || e == 24) { ov[(j - 1) & 1] = pu * pr; if (((j - 1) & 1) == 1) gp[(j - 1) >> 1] = pk(ov[0], ov[1]); }
        if (m == 2) { pr = __builtin_amdgcn_rcpf(__builtin_fmaf(tv, cR, cR)); pu = au; }
      };
      auto pass = [&](auto nc) LAM {
        constexpr int n = decltype(nc)::v;
        sfor<0, 40>([&](auto sc) LAM {
          constexpr int s = decltype(sc)::v, g = (n - 2) * 40 + s, sn = s + RD;
          constexpr int G[6] = {1, 31, 61, 91, 122, 161}, I[4] = {26, 56, 86, 150};
          ac[n % 3] = mfma32(rg[s % RD], as8(hf[s]), ac[n % 3]);
          if constexpr (sn < 40) rg[s % RD] = ldw(n, sn);
          else if constexpr (n < 6) rg[s % RD] = ldw(n + 1, sn - 40);
          sfor<0, 6>([&](auto uc) LAM {
            constexpr int u = decltype(uc)::v;
            if constexpr (g >= G[u] && g <= G[u] + 24 && (!(ABL & 1) || g - G[u] < 6 || g - G[u] == 24)) gstep(ac[u % 3], g - G[u]);
            if constexpr (g == G[u] + 24) {
              if constexpr (u >= 2) { if (!(ABL & 2)) WAITV(0); flag_store(sws, tag, fso + u * 128); }
              st16c(gp, ws, gvo, gso + u * 8192);
            }
          });
          sfor<0, 4>([&](auto kc) LAM {
            constexpr int k = decltype(kc)::v;
            if constexpr (g == I[k] - 3) ac[k % 3] = ldcs(3 + k);
            if constexpr (g > I[k] && g <= I[k] + 4) {
#pragma unroll
              for (int j = 0; j < 4; ++j) ac[k % 3][4 * (g - I[k] - 1) + j] *= nmean;
            }
          });
          if constexpr (WLP && n == 6 && s >= 25) ldwl(s - 25);
          if constexpr (s == 40 - RD) lds_inc(L_CONS + 4 * n);
          if constexpr (s == 8) lv = *(volatile LAS u32*)(L_LAND + 4 * (n - 1));
          if constexpr (s == 14 && !(ABL & 4)) lds_check(lv, L_LAND + 4 * (n - 1), 4);
          SB();
        });
      };
      pass(IC<2>{}); STAMP(26); pass(IC<3>{}); STAMP(27); pass(IC<4>{}); STAMP(28); pass(IC<5>{}); STAMP(29);
      pass(IC<6>{});
      STAMP(15);
#pragma unroll
      for (int e = 0; e < 25; ++e) gstep(ac[0], e);
      st16c(gp, ws, u7 ? gvo : OOB, gso + 6 * 8192);
      if (WLP) WAITV(16); else WAITV(1);
      flag_store(sws, tag, fso + 6 * 128);
    }
#undef lane
    STAMP(30);
    {
      const u32 ln = lane_id(), vl = ln * 16;
      const u32 fv = W_FS4 + ((team * 16 + 4 * w + (ln >> 4)) * 4 + 2 * th + ((ln >> 3) & 1)) * 1024;
      const u32 gso = W_G + (team * 8 + th * 4) * 54 * 1024 + 2 * w * 1024;
      const u32 vz = w < 3 ? vl : OOB;
      u32 gsr[4];
#pragma unroll
      for (int r = 0; r < 4; ++r) gsr[r] = sgpr(gso + ((r + w + 1) & 3) * (54 * 1024));
      v4u wf[6][5], gf[6][4];
      auto ldg = [&](int i) LAM {
#pragma unroll
        for (int r = 0; r < 4; ++r) gf[i - 8][r] = ld16c(ws, i >= 12 ? vz : vl, gsr[r] + ((i >> 1) * 8 + (i & 1)) * 1024);
        SB();
      };
      v2u fl = ld8c(ws, fv, 5 * 128);
      if (!WLP) sfor<0, 5>([&](auto kc) LAM { ldwl(decltype(kc)::v); });
      if (WLP) WAITV(2); else WAITV(7);
      flag_store(sws, tag, W_FS4 + (bid * 4 + w) * 1024 + 7 * 128);
      STAMP(16);
      if (!wall(fl.x == tag.x && fl.y == tag.y)) poll_level(ws, fv, tag, 5);
      asm volatile("" ::: "memory");
      ldg(8); ldg(9);
      fl = ld8c(ws, fv, 6 * 128);
      if (!WLP) sfor<5, 15>([&](auto kc) LAM { ldwl(decltype(kc)::v); });
#pragma unroll
      for (int i = 11; i < 14; ++i)
#pragma unroll
        for (int t = 0; t < 5; ++t) wf[i - 8][t] = wl[i - 11][t];
      u32 sb = L_HS + 2 * HSLOT + w * 5120 + vl;
      OPQ(sb);
      lds_wait(S_SP);
#pragma unroll
      for (int i = 8; i < 10; ++i)
#pragma unroll
        for (int t = 0; t < 5; ++t) wf[i - 8][t] = lld<v4u>(sb + (i - 8) * HSLOT + t * 1024);
      if (!wall(fl.x == tag.x && fl.y == tag.y)) poll_level(ws, fv, tag, 6);
      asm volatile("" ::: "memory");
      STAMP(31);
      ldg(10);
      f4 da[4][5];
      auto step = [&](int i) LAM {
#pragma unroll
        for (int m = 0; m < 20; ++m) da[m / 5][m % 5] = mfma16(as8(wf[i - 8][m % 5]), as8(gf[i - 8][m / 5]), i == 8 ? z4 : da[m / 5][m % 5]);
        SB();
      };
      step(8);
      ldg(11);
      fl = ld8c(ws, fv, 7 * 128);
#pragma unroll
      for (int t = 0; t < 5; ++t) wf[2][t] = lld<v4u>(sb + 2 * HSLOT + t * 1024);
      SB();
      step(9);
      if (w < 3 && !wall(fl.x == tag.x && fl.y == tag.y)) poll_level(ws, fv, tag, 7);
      asm volatile("" ::: "memory");
      STAMP(1);
      ldg(12); ldg(13);
      step(10); step(11);
      STAMP(6);
      step(12);
#pragma unroll
      for (int m = 0; m < 21; ++m) {
        if (m < 20) da[m / 5][m % 5] = mfma16(as8(wf[5][m % 5]), as8(gf[5][m / 5]), da[m / 5][m % 5]);
        if (m >= 6) lst<f4>(sb + ((m - 6) / 5) * HSLOT + ((m - 6) % 5) * 1024, da[(m - 6) / 5][(m - 6) % 5]);
        SB();
      }
      u32 rb[4];
#pragma unroll
      for (int r = 0; r < 3; ++r) { rb[r] = L_HS + (2 + r) * HSLOT + ((w - r - 1) & 3) * 5120 + vl; OPQ(rb[r]); }
      rb[3] = L_HS + ((w + 5) % 7) * HSLOT + vl;
      OPQ(rb[3]);
      const rsrc_t ro = mk_rsrc(out, SEQ * DM * 2);
      const u32 oso = (team * 128 + th * 64 + w * 16) * 1280 + hq * 160;
      u32 ovo = (ln & 15) * 1280 + (ln >> 4) * 16, ov2 = (ln & 15) * 1280 + 128 + (ln >> 4) * 8;
      OPQ(ovo); OPQ(ov2);
      bar();
      STAMP(17);
      f4 r[5][4], rr[5];
#pragma unroll
      for (int n = 0; n < 5; ++n)
#pragma unroll
        for (int j = 0; j < 4; ++j) r[n][j] = lld<f4>(rb[j] + n * 1024);
      SB();
#pragma unroll
      for (int n = 0; n < 5; ++n) rr[n] = ((da[3][n] + r[n][0]) + (r[n][1] + r[n][2])) + r[n][3];
#pragma unroll
      for (int m = 0; m < 2; ++m)
        st16p(v4u{pk(rr[2 * m][0], rr[2 * m][1]), pk(rr[2 * m][2], rr[2 * m][3]), pk(rr[2 * m + 1][0], rr[2 * m + 1][1]), pk(rr[2 * m + 1][2], rr[2 * m + 1][3])}, ro,
              ovo + 64 * m, oso);
      __builtin_amdgcn_raw_buffer_store_b64(v2u{pk(rr[4][0], rr[4][1]), pk(rr[4][2], rr[4][3])}, ro, ov2, oso, 0);
      if (b == 0 && w == 0) flag_store(sws, tag, W_EPOCH);
    }
    STAMP(18);
    DUMP();
  } else {
    const u32 j = wv;
    __builtin_amdgcn_s_setprio(HPRIO);
    const u32 lane = lane_id();
    const u32 q = lane >> 4, i16 = lane & 15, tq = (lane >> 2) & 3, tp = lane & 3, vl = lane * 16;
    const u32 p1 = j * 256 + b;
    const bool isq = p1 < 600;
    const u32 pp = isq ? p1 : p1 - 600, pks = (pp * (isq ? 2185u : 6554u)) >> 16, pcb1 = pp - pks * (isq ? 30 : 10);
    u32 pv0, ps0, psn, pl0 = L_A + j * 4096;
    {
      const u32 jr = lane >> 3;
      const bool ex = pks >= 16;
      const u32 ra = 20 * (jr & 1) + 16 + (jr >> 1), rb = 20 * (jr >> 2) + (jr & 3);
      const u32 m1 = isq ? 3840 : 1280, m2 = isq ? 8 : ex ? 40 : 4, m3 = isq ? 32 : ex ? 160 : 40, m4 = (!isq && ex) ? 2560 : 0;
      pv0 = (isq ? jr : ex ? ra : rb) * m1 + (lane & 7) * 16;
      psn = m2 * m1;
      ps0 = (m3 * pks - m4) * m1 + pcb1 * 128;
    }
    OPS(ps0); OPS(psn); OPS(pl0); OPQ(pv0);
    if (lane < 32) lst<u32>(L_SYNC + lane * 4, 0u);
    GET_ARGS();
    {
      const v4u sp = mk_srd((const void*)(isq ? ka[1] : ka[3]), pks >= 20 ? 0u : isq ? DM * 3 * DM * 2 : DM * DM * 2);
#pragma unroll
      for (int n = 0; n < 4; ++n) dma16(pv0, sp, ps0 + n * psn, pl0 + n * 1024);
    }
    bar();
    u64 ep;
    asm volatile("s_load_dwordx2 %0, %1, 0x0" : "=s"(ep) : "s"(wsp));
    if (j == 3) {
      const v4u sp = mk_srd((const void*)ka[2], 32 * 400 * 2);
      dma16(vl, sp, rank * 1600, L_WPHI); dma16(vl, sp, rank * 1600 + 1024, L_WPHI + 1024);
    }
    u32 selv = i16 == q ? 0x3F803F80u : 0u;
    OPQ(selv);
    const s8 sel = as8(v4u{selv, selv, selv, selv});
    const u32 pi4 = ((tp & 1) << 1) | (tp >> 1);
    {
      u32 dv, cv;
      if (isq) {
        const u32 sec = (pcb1 * 26) >> 8, cn = (pcb1 - sec * 10) * 64 + lane, head = (cn * 3277) >> 16, d = cn - head * 20, hh = head & 1;
        const u32 par = pks & 1;
        const u32 tb = sec == 0 ? par * 2048 : sec * 3072 - 3072, xb = sec == 0 ? 4096 - 256 + par * 128 : 2048 - 256 - 128 + sec * 128;
        const u32 ds = W_W1 + (sec == 0 ? 102400 + (pks >> 1) * 5120 : pks * 5120);
        dv = (head >> 1) * 153600 + d * 16 + (d < 16 ? tb + hh * 1024 : xb + hh * 64) + ds;
        cv = i16 < 4 ? W_CS1 + pks * 7680 + pcb1 * 256 + i16 * 64 + q * 16 : OOB;
      } else {
        const u32 sgi = 4 * pcb1 + q, cc = (sgi * 52) >> 8, pos = sgi - cc * 5;
        const u32 o1 = ((pos & 2) + (i16 >> 3)) * 1024 + (8 * (pos & 1) + (i16 & 7)) * 16, o2 = 4096 + swap23(i16) * 16;
        dv = pks >= 20 ? OOB : cc * 102400 + (pos < 4 ? o1 : o2) + W_WO + pks * 5120;
        cv = OOB;
      }
      u32 rb = L_A + j * 4096 + tq * 128 + q * 32 + (isq ? tp : pi4) * 8;
      OPQ(dv); OPQ(rb); OPQ(cv);
      WAITV(0);
      v4u f[4];
#pragma unroll
      for (int n = 0; n < 4; ++n) f[n] = __builtin_bit_cast(v4u, cat(trd(rb + n * 1024), trd(rb + n * 1024 + 512)));
      f4 cs = z4;
#pragma unroll
      for (int n = 0; n < 4; ++n) { st16c(f[n], ws, dv + n * 256, 0); cs = mfma16(as8(f[n]), sel, cs); }
      st16c(__builtin_bit_cast(v4u, cs), ws, cv, 0);
      v4u sent = {SENT, SENT, SENT, SENT};
      OPQ(sent);
      st16c(sent, ws, vl, W_PART + (rank * 16 + team) * 8192 + j * 2048);
      st16c(sent, ws, vl, W_PART + (rank * 16 + team) * 8192 + j * 2048 + 1024);
      st16c(sent, ws, lane < 4 ? vl : OOB, W_RED + rank * 4096 + team * 256 + j * 64);
      SB();
    }
    STAMP(1);
    if (j == 3) {
      v2u pl[2][4];
      const int src = ((lane & ~3u) | tq) * 4;
#pragma unroll
      for (int h = 0; h < 2; ++h) {
        const u32 pb = L_WPHI + h * 800;
        pl[h][0] = __builtin_bit_cast(v2u, trd(pb + (4 * q + tq) * 40 + 8 * tp)); pl[h][1] = __builtin_bit_cast(v2u, trd(pb + (16 + tq) * 40 + 8 * tp));
        pl[h][2] = __builtin_bit_cast(v2u, trd(pb + (4 * q + tq) * 40 + 32)); pl[h][3] = __builtin_bit_cast(v2u, trd(pb + (16 + tq) * 40 + 32));
      }
      asm volatile("" ::: "memory");
#pragma unroll
      for (int h = 0; h < 2; ++h) {
        const u32 qh = q == (u32)h ? ~0u : 0u, rh = tp == (u32)h ? ~0u : 0u;
        lst<v4u>(L_WPHI + h * 1024 + vl, v4u{pl[h][0].x, pl[h][0].y, pl[h][1].x & qh, pl[h][1].y & qh});
        lst<v4u>(L_PH2 + h * 1024 + vl, v4u{(u32)__builtin_amdgcn_ds_bpermute(src, pl[h][2].x) & rh, (u32)__builtin_amdgcn_ds_bpermute(src, pl[h][2].y) & rh,
                                            (u32)__builtin_amdgcn_ds_bpermute(src, pl[h][3].x) & rh & qh, (u32)__builtin_amdgcn_ds_bpermute(src, pl[h][3].y) & rh & qh});
      }
    }
    GET_EPOCH();
    WAITV(0);
    lds_inc(S_PST);
    if (j == 0) { lds_wait<0>(S_PST); flag_store(sws, tag, W_FA + bid * FAS); }
    STAMP(9);
    u32 rdv[2], rdn[2], rcv[2], rvo[2], rs0[2], rsn[2];
    v4u rsp[2];
#pragma unroll
    for (int e = 0; e < 2; ++e) {
      const u32 piece = p1 + e * 1024;
      const bool isg = piece < 1080;
      const u32 p2 = isg ? piece : piece - 1080;
      const u32 pk_ = (p2 * (isg ? 1214u : 6554u)) >> 16, pcb = p2 - pk_ * (isg ? 54 : 10);
      const u32 mat = pcb >= 27 ? 1 : 0, rs = isg ? 3456 : 1280, u0 = 4 * (pcb - mat * 27);
      rsp[e] = mk_srd((const void*)(!isg ? ka[6] : mat ? ka[5] : ka[4]), piece < 1620 ? DM * FF * 2 : 0u);
      rs0[e] = pk_ * 32 * rs + u0 * 32; rsn[e] = 8 * rs; rvo[e] = (lane >> 3) * rs + (lane & 7) * 16;
      const u32 sgi = u0 + q, cc = (sgi * 52) >> 8, pos = sgi - cc * 5;
      const u32 o1 = ((pos & 2) + (i16 >> 3)) * 1024 + (8 * (pos & 1) + (i16 & 7)) * 16, o2 = 4096 + swap23(i16) * 16;
      const u32 dg = W_WGU + sgi * 40960 + i16 * 16 + pk_ * 2048 + mat * 256, dd = W_WD + cc * (54 * 5120) + (pos < 4 ? o1 : o2) + pk_ * 5120;
      rdv[e] = isg ? dg : piece < 1620 ? dd : OOB;
      rdn[e] = isg ? 512 : 256;
      rcv[e] = (isg && i16 < 4) ? W_CSP + (pk_ * 108 + u0 + i16) * 128 + mat * 64 + (((q & 1) << 1) | (q >> 1)) * 16 : OOB;
      OPQ(rdv[e]); OPQ(rcv[e]); OPQ(rvo[e]); OPS(rs0[e]); OPS(rsn[e]); OPS(rdn[e]);
      rsp[e].x = sgpr(rsp[e].x); rsp[e].y = sgpr(rsp[e].y); rsp[e].z = sgpr(rsp[e].z);
    }
    u32 c1v;
    { const u32 t = (j & 1) * 64 + lane, sec = (t * 1639) >> 16; c1v = t < 120 ? (sec * 600 + t) * 4 + (j >> 1) * (10 * 7680) : OOB; OPQ(c1v); }
    v4u rf[10];
    {
      v4u rt[2][4];
#pragma unroll
      for (int e = 0; e < 2; ++e) {
        const rsrc_t sp = mk_rsrc((const void*)(((u64)rsp[e].y << 32) | rsp[e].x), rsp[e].z);
#pragma unroll
        for (int n = 0; n < 4; ++n) rt[e][n] = __builtin_amdgcn_raw_buffer_load_b128(sp, rvo[e], rs0[e] + n * rsn[e], 0);
      }
      SB();
      const u32 rb = L_KV + j * 1024 + tq * 128 + q * 32 + pi4 * 8;
#pragma unroll
      for (int e = 0; e < 2; ++e) {
        f4 cs = z4;
#pragma unroll
        for (int n = 0; n < 4; ++n) {
          lst<v4u>(L_KV + j * 1024 + vl, rt[e][n]);
          asm volatile("" ::: "memory");
          rf[5 * e + n] = __builtin_bit_cast(v4u, cat(trd(rb), trd(rb + 512)));
          asm volatile("" ::: "memory");
        }
#pragma unroll
        for (int n = 0; n < 4; ++n) cs = mfma16(as8(rf[5 * e + n]), sel, cs);
        rf[5 * e + 4] = __builtin_bit_cast(v4u, cs);
        OPQ(rf[5 * e + 4]);
      }
    }
    auto rst = [&](int k) LAM { if (k % 5 < 4) st16c(rf[k], ws, rdv[k / 5], (k % 5) * rdn[k / 5]); else st16c(rf[k], ws, rcv[k / 5], 0); };
    STAMP(23);
    __builtin_amdgcn_s_sleep(FAD);
    {
      u32 fv = (j * 64 + lane) * FAS;
#pragma unroll 1
      for (u32 s = 0; s < SPIN_CAP; ++s) {
        const v2u v = ld8c(ws, fv, W_FA);
        fv = (v.x == tag.x && v.y == tag.y) ? OOB : fv;
        if (wall(fv == OOB)) break;
        __builtin_amdgcn_s_sleep(FAR);
        asm volatile("" ::: "memory");
      }
    }
    WAITV(0);
    lds_inc(S_FA);
    lds_wait<0>(S_FA);
    STAMP(24);
    {
      u32 w1s = W_W1 + rank * 153600 + j * 5120, w1l = L_A + j * 5120;
      OPS(w1s); OPS(w1l);
      auto dd = [&](int m) LAM { dma5c(vl, sws, w1s + m * 20480, w1l + m * 20480); };
      u32 cv[10];
      dd(0); dd(1); rst(0);
      WAITV(6); lds_inc(S_L1);
      dd(2); rst(1); dd(3); rst(2);
      WAITV(13); lds_inc(S_L1 + 4);
      dd(4); rst(3); rst(4);
      WAITV(14); lds_inc(S_L1 + 8);
#pragma unroll
      for (int k = 0; k < 10; ++k) cv[k] = ld4c(ws, c1v, W_CS1 + k * 7680 + rank * 160);
      rst(5);
      WAITV(19); lds_inc(S_L1 + 12);
      lds_wait(S_XD);
      dd(5); rst(6);
      WAITV(19); lds_inc(S_L1 + 16);
      dd(6); rst(7);
      if (j < 2) dd(7);
      rst(8); rst(9);
      {
        const float c = (((uf(cv[0]) + uf(cv[1])) + (uf(cv[2]) + uf(cv[3]))) + ((uf(cv[4]) + uf(cv[5])) + (uf(cv[6]) + uf(cv[7])))) + (uf(cv[8]) + uf(cv[9]));
        lst<float>(L_CS1 + j * 256 + lane * 4, c);
      }
      lds_inc(S_CS);
      WAITV(0);
      lds_inc(S_RT);
      if (j == 3) { lds_wait<0>(S_RT); flag_store(sws, tag, W_FB + bid * 32); }
    }
    STAMP(5);
    STAMP(25);
    u32 wos = W_WO + hq * 102400;
    OPS(wos);
    v4u wf[11][5], gf[8][4];
    const u32 wso = W_WD + (hq * 54 + 2 * j) * 5120;
    const u32 gso = W_G + (team * 8 + th * 4) * 54 * 1024 + 2 * j * 1024;
    auto ldw1 = [&](int i, int t) LAM { wf[i][t] = ld16c(ws, vl + t * 1024, wso + ((i >> 1) * 8 + (i & 1)) * 5120); };
    auto ldg1 = [&](int i, int r) LAM { gf[i][r] = ld16c(ws, vl, gso + r * 54 * 1024 + ((i >> 1) * 8 + (i & 1)) * 1024); };
    STAMP(2);
    lds_wait<2>(S_KVD);
#pragma unroll
    for (int n = 0; n < 3; ++n) dma5c(vl, sws, wos + (8 + 4 * n + j) * 5120, L_WO + (8 + 4 * n + j) * 5120);
    WAITV(15);
    STAMP(3);
    kv_job<true>(ws, j + 4, W_PART + (rank * 16 + team) * 8192);
    STAMP(4);
    bar_nw();
    dma5c(vl, sws, wos + j * 5120, L_WO + j * 5120);
    dma5c(vl, sws, wos + (4 + j) * 5120, L_WO + (4 + j) * 5120);
    if (j == 3) {
      const u32 pvo = (lane >> 5) * 8 * 8192 + (lane & 31) * 16, pso = W_PART + rank * 16 * 8192 + team * 512;
      v4u pv[8];
      __builtin_amdgcn_s_setprio(3);
      __builtin_amdgcn_s_sleep(HOPD);
#pragma unroll 1
      for (u32 s = 0; s < SPIN_CAP; ++s) {
#pragma unroll
        for (int n = 0; n < 8; ++n) pv[n] = ld16c(ws, pvo, pso + n * 8192);
        SB();
        u32 mx = 0;
#pragma unroll
        for (int n = 0; n < 8; ++n) mx = umax4(mx, pv[n]);
        if (wall(mx != SENT)) break;
        asm volatile("" ::: "memory");
      }
      float sm[4];
#pragma unroll
      for (int k = 0; k < 4; ++k) {
        const float a = ((uf(pv[0][k]) + uf(pv[1][k])) + (uf(pv[2][k]) + uf(pv[3][k]))) + ((uf(pv[4][k]) + uf(pv[5][k])) + (uf(pv[6][k]) + uf(pv[7][k])));
        auto r = __builtin_amdgcn_permlane32_swap(fu(a), fu(a), false, false);
        sm[k] = uf(r[0]) + uf(r[1]);
      }
      st8c(v2u{san(pk(sm[0], sm[1])), san(pk(sm[2], sm[3]))}, ws, lane < 32 ? lane * 8 : OOB, W_RED + rank * 4096 + team * 256);
      STAMP(6);
    } else if (j == 2) { poll256(ws, W_FB, 32, lane, tag); lds_inc(S_FB); }
    lds_wait(S_FB, 1);
    STAMP(22);
    sfor<0, 5 * NW1>([&](auto ic) LAM { ldw1(decltype(ic)::v / 5, decltype(ic)::v % 5); });
    SB();
    if (j == 3) {
      __builtin_amdgcn_s_sleep(HOPE);
      v4u kv[4];
#pragma unroll 1
      for (u32 s = 0; s < SPIN_CAP; ++s) {
#pragma unroll
        for (int n = 0; n < 4; ++n) kv[n] = ld16c(ws, vl, W_RED + rank * 4096 + n * 1024);
        SB();
        u32 mx = 0;
#pragma unroll
        for (int n = 0; n < 4; ++n) mx = umax4(mx, kv[n]);
        if (wall(mx != SENT)) break;
        asm volatile("" ::: "memory");
      }
#pragma unroll
      for (int n = 0; n < 4; ++n) lst<v4u>(L_KV + n * 1024 + vl, kv[n]);
    }
    WAITV(0);
    STAMP(7);
    bar();
    __builtin_amdgcn_s_setprio(HPRIO);
    STAMP(8);
    auto stage_u = [&](u32 hs) LAM {
      const u32 so = W_WGU + (16 * (hs >> 1) + rank) * 40960 + ((hs & 1) * 4 + j) * 5120, dst = L_HS + ((hs + 5) % 7) * HSLOT + j * 5120;
      if (hs >= 7) waitv<PACE>();
      if (!(ABL & 8) || hs < 7) dma5c(vl, sws, so, dst);
    };
    lds_wait(S_OP);
    stage_u(0); stage_u(1);
    {
      const u32 t = j * 64 + lane, u = 16 * (t >> 5) + rank;
      if (u < 108) {
        u32 cv[20];
#pragma unroll
        for (int k = 0; k < 20; ++k) cv[k] = ld4c(ws, u * 128 + (t & 31) * 4, W_CSP + k * 108 * 128);
        SB();
        float c = 0.f;
#pragma unroll
        for (int k = 0; k < 20; ++k) c += uf(cv[k]);
        lst<float>(L_CS3 + t * 4, c);
      }
    }
    u32 sdb[3] = {L_HS + j * 5120 + vl, L_HS + 3 * HSLOT + j * 5120 + vl, L_HS + 6 * HSLOT + j * 5120 + vl};
    OPQ(sdb[0]); OPQ(sdb[1]); OPQ(sdb[2]);
    auto spill = [&](int i) LAM {
#pragma unroll
      for (int t = 0; t < 5; ++t) lst<v4u>(sdb[((i + 1) % 7) / 3] + (((i + 1) % 7) % 3) * HSLOT + t * 1024, wf[i][t]);
      SB();
    };
    auto ldw = [&](auto lo, auto hi) LAM {
      sfor<decltype(lo)::v * 5, decltype(hi)::v * 5>([&](auto ic) LAM { if (decltype(ic)::v % 5 == 0) waitv<PACE>(); ldw1(decltype(ic)::v / 5, decltype(ic)::v % 5); });
      SB();
    };
    auto ldg = [&](int i) LAM {
      waitv<PACE>();
#pragma unroll
      for (int r = 0; r < 4; ++r) ldg1(i, r);
      SB();
    };
    STAMP(10);
    lds_wait(S_OL);
    ldw(IC<NW1>{}, IC<NW2>{});
    lds_wait<2>(S_WOC);
    STAMP(11);
    auto landed = [&](int k) LAM { lds_inc(L_LAND + 4 * k); };
    stage_u(2); stage_u(3); stage_u(4);
    WAITV(0); landed(0);
    STAMP(13);
    stage_u(5); stage_u(6);
    WAITV(0); landed(1);
    lds_wait<2>(S_HL);
    ldw(IC<NW2>{}, IC<NW3>{});
    lds_wait<1>(L_CONS + 4);
    stage_u(7); stage_u(8); stage_u(9);
    WAITV(5); landed(2);
    stage_u(10);
    ldw(IC<NW3>{}, IC<9>{});
    waitv<5 * (9 - NW3)>(); landed(3);
    lds_wait<2>(L_CONS + 8);
    stage_u(11);
    if (u7) stage_u(12);
    ldw(IC<9>{}, IC<11>{});
    WAITV(10); landed(4);
    const u32 fvo = W_FS4 + ((team * 16 + 4 * j + (lane >> 4)) * 4 + 2 * th + ((lane >> 3) & 1)) * 1024;
    lds_wait<2>(L_CONS + 12);
    if (u7) stage_u(13);
    spill(4);
    WAITV(0); landed(5);
    STAMP(26);
    lds_wait<2>(L_CONS + 16);
    STAMP(27);
    v2u fl = ld8c(ws, fvo, 2 * 128);
    spill(5); spill(6);
    if (!wall(fl.x == tag.x && fl.y == tag.y)) poll_level(ws, fvo, tag, 2);
    asm volatile("" ::: "memory");
    ldg(0); ldg(1); ldg(2);
    lds_wait<2>(L_CONS + 20);
    STAMP(28);
    fl = ld8c(ws, fvo, 3 * 128);
    spill(7); spill(8); ldg(3);
    if (!wall(fl.x == tag.x && fl.y == tag.y)) poll_level(ws, fvo, tag, 3);
    asm volatile("" ::: "memory");
    STAMP(29);
    __builtin_amdgcn_s_setprio(P4PRIO);
    lds_wait<1>(L_CONS + 24);
    STAMP(16);
    fl = ld8c(ws, fvo, 4 * 128);
    spill(9); spill(10);
    lds_inc(S_SP);
    STAMP(1);
    f4 da[4][5];
    sfor<0, 8>([&](auto ic) LAM {
      constexpr int i = decltype(ic)::v;
#pragma unroll
      for (int m = 0; m < 20; ++m) {
        const int r = m / 5, t = m % 5;
        da[r][t] = mfma16(as8(wf[i][t]), as8(gf[i][r]), i == 0 ? z4 : da[r][t]);
        if constexpr (i <= 3) { if (t == 4) ldg1(i + 4, r); }
        if constexpr (i >= 3 && i <= 6) { if (m >= 12 && m < 17) wf[i + 1][m - 12] = lld<v4u>(sdb[((i + 2) % 7) / 3] + (((i + 2) % 7) % 3) * HSLOT + (m - 12) * 1024); }
        if constexpr (i == 7) { if (m >= 6) lst<f4>(sdb[(((m - 6) / 5 + 5) % 7) / 3] + ((((m - 6) / 5 + 5) % 7) % 3) * HSLOT + ((m - 6) % 5) * 1024, da[(m - 6) / 5][(m - 6) % 5]); }
        SB();
      }
      if constexpr (i == 1) {
        if (!wall(fl.x == tag.x && fl.y == tag.y)) poll_level(ws, fvo, tag, 4);
        asm volatile("" ::: "memory");
      }
      STAMP(2 + i);
      SB();
    });
    STAMP(18);
#pragma unroll
    for (int m = 14; m < 20; ++m) lst<f4>(sdb[((m / 5 + 5) % 7) / 3] + (((m / 5 + 5) % 7) % 3) * HSLOT + (m % 5) * 1024, da[m / 5][m % 5]);
    lds_inc(S_HP);
    __builtin_amdgcn_s_setprio(0);
    lds_wait(S_HP);
    {
      const u32 pb = L_HS + ((j + 5) % 7) * HSLOT + vl;
      f4 r[5][4];
#pragma unroll
      for (int n = 0; n < 5; ++n)
#pragma unroll
        for (int k = 0; k < 4; ++k) r[n][k] = lld<f4>(pb + k * 5120 + n * 1024);
      const v4u h0 = lld<v4u>(L_HST + j * 2560 + vl), h1 = lld<v4u>(L_HST + j * 2560 + 1024 + vl);
      const v2u h2 = lld<v2u>(L_HST + j * 2560 + 2048 + lane * 8);
      const u32 hw[10] = {h0.x, h0.y, h0.z, h0.w, h1.x, h1.y, h1.z, h1.w, h2.x, h2.y};
#pragma unroll
      for (int n = 0; n < 5; ++n)
        lst<f4>(pb + n * 1024, ((r[n][0] + r[n][1]) + (r[n][2] + r[n][3])) + f4{blo(hw[2 * n]), bhi(hw[2 * n]), blo(hw[2 * n + 1]), bhi(hw[2 * n + 1])});
    }
    bar();
    DUMP();
  }
}

extern "C" void kernel_launch(void* const* d_in, const int* in_sizes, int n_in, void* d_out, int out_size, void* d_ws, size_t ws_size,
                              hipStream_t stream) {
  (void)hipFuncSetAttribute((const void*)fused_block, hipFuncAttributeMaxDynamicSharedMemorySize, L_BYTES);
  fused_block<<<256, 512, L_BYTES, stream>>>((const u16*)d_in[0], (const u16*)d_in[1], (const u16*)d_in[2], (const u16*)d_in[3],
                                             (const u16*)d_in[5], (const u16*)d_in[6], (const u16*)d_in[7], (u16*)d_out, (char*)d_ws);
}
```

```cpp
#include <hip/hip_runtime.h>
#include <hip/hip_bf16.h>

typedef unsigned short u16;
typedef unsigned int u32;
typedef unsigned long long u64;
typedef u32 v4u __attribute__((ext_vector_type(4)));
typedef u32 v2u __attribute__((ext_vector_type(2)));
typedef float f4 __attribute__((ext_vector_type(4)));
typedef float f2 __attribute__((ext_vector_type(2)));
typedef float f16v __attribute__((ext_vector_type(16)));
typedef short s4 __attribute__((ext_vector_type(4)));
typedef short s8 __attribute__((ext_vector_type(8)));
typedef __bf16 bf2 __attribute__((ext_vector_type(2)));
#define LAS __attribute__((address_space(3)))
#define CAS __attribute__((address_space(4)))
#if defined(__HIP_DEVICE_COMPILE__)
#define NOPK __attribute__((target("no-packed-fp32-ops")))
#else
#define NOPK
#endif
#define DEV __device__ __forceinline__ NOPK
#define SB() __builtin_amdgcn_sched_barrier(0)
#define OPQ(x) asm volatile("" : "+v"(x))
typedef __amdgpu_buffer_rsrc_t rsrc_t;

#define STMASK (1u << 0)
#define DBG 3
#define ROT ((rank + 3 * (team & 1)) % 10)
#define P1ST 0
#define HOPA0 28
#define HOPA1 56
#define HOPB 1
#define FAS 32
#define NRM1 3
#define NRM2 8
#define HOPA 2
#define NWPRE 2
#define HOPD 12
#define HOPE 4
#define FAD 0
#define FAR 4
#define PD2 14
#define KDEP 6
#define FBD 0
#define LVS 8
#define WD6 8
#define NW2 6
#define NW0 4
#define NW1 4
#define XD3 1
#define HPRIO 0
#define RDEP 4
#define CODEPF 1
#define CODE_BYTES 45056
#define HROT 0
#define OROT 0
#define P4PRIO 3
#define ABL 0
#define NW3 7
#define WLP 0
#define PACE 63
#define XMAP 4

constexpr int SEQ = 2048, DM = 640, FF = 1728;
constexpr u32 W_EPOCH = 0;
constexpr u32 W_FA = 4096;
constexpr u32 W_FB = W_FA + 256 * 128;
constexpr u32 W_FS2 = W_FB + 256 * 32;
constexpr u32 W_FS3 = W_FS2 + 256 * 4 * 128;
constexpr u32 W_FS4 = W_FS3 + 256 * 4 * 128;
constexpr u32 W_RED = W_FS4 + 256 * 4 * 8 * 128;
constexpr u32 W_STAT = W_RED + 65536;
constexpr u32 W_CSP = W_STAT + 131072;
constexpr u32 W_CS1 = W_CSP + 20 * 108 * 128;
constexpr u32 W_PART = W_CS1 + 20 * 7680;
constexpr u32 W_O = W_PART + 16 * 16 * 8192;
constexpr u32 W_H = W_O + 16 * 8 * 20 * 1024;
constexpr u32 W_G = W_H + 16 * 4 * 40 * 1024;
constexpr u32 W_WGU = W_G + 16 * 8 * 54 * 1024;
constexpr u32 W_WD = W_WGU + 108 * 40 * 1024;
constexpr u32 W_WO = W_WD + 8 * 54 * 5 * 1024;
constexpr u32 W_W1 = W_WO + 8 * 20 * 5 * 1024;
constexpr u32 W_DBG = W_W1 + 16 * 153600;
constexpr u32 W_END = W_DBG + 256 * 8 * 32 * 4;
constexpr u32 L_WPHI = 0;
constexpr u32 L_CS3 = 3200;
constexpr u32 L_SYNC = 3072;
constexpr u32 L_CS1 = 2048;
constexpr u32 S_PST = L_SYNC, S_FA = L_SYNC + 4, S_XD = L_SYNC + 8, S_CS = L_SYNC + 12, S_KVD = L_SYNC + 16, S_WOC = L_SYNC + 20, S_RT = L_SYNC + 24, S_FB = L_SYNC + 28, S_OP = L_SYNC + 52, S_OL = L_SYNC + 56, S_HL = L_SYNC + 60;
constexpr u32 S_L1 = L_SYNC + 32, S_SP = L_SYNC + 64, S_HP = L_SYNC + 68, L_LAND = L_SYNC + 80, L_CONS = L_SYNC + 100;
constexpr u32 L_KV = 4096;
constexpr u32 L_A = 8192;
constexpr u32 L_B = L_A + 102400;
constexpr u32 L_PH2 = L_B + 51200;
constexpr u32 L_IMG = L_A, L_WO = L_A;
constexpr u32 L_HS = L_A, HSLOT = 20480;
constexpr u32 L_HST = L_HS + 7 * HSLOT;
constexpr u32 L_BYTES = 163840;
constexpr u32 OOB = 0x80000000u;
constexpr u32 SENT = 0xFFFFFFFFu;
constexpr u32 SPIN_CAP = 1u << 18;

DEV v4u mk_srd(const void* p, u32 bytes) { return v4u{(u32)(size_t)p, (u32)((size_t)p >> 32), bytes, 0x00020000u}; }
DEV rsrc_t mk_rsrc(const void* p, u32 bytes) { return __builtin_amdgcn_make_buffer_rsrc((void*)p, 0, bytes, 0x00020000); }
DEV void dma16(u32 voff, v4u srd, u32 soff, u32 lds) {
  asm volatile("s_mov_b32 m0, %2\n\ts_nop 0\n\tbuffer_load_dwordx4 %0, %1, %3 offen lds" ::"v"(voff), "s"(srd), "s"(lds), "s"(soff) : "memory");
}
DEV void dma16c(u32 voff, v4u srd, u32 soff, u32 lds) {
  asm volatile("s_mov_b32 m0, %2\n\ts_nop 0\n\tbuffer_load_dwordx4 %0, %1, %3 offen sc1 lds" ::"v"(voff), "s"(srd), "s"(lds), "s"(soff) : "memory");
}
DEV void dma_a(u32 voff, v4u srd, u32 sb, u32 sa, u32 lb, u32 la) {
  u32 t;
  asm volatile("s_add_u32 m0, %5, %6\n\ts_add_u32 %0, %3, %4\n\tbuffer_load_dwordx4 %1, %2, %0 offen lds"
               : "=&s"(t) : "v"(voff), "s"(srd), "s"(sb), "s"(sa), "s"(lb), "s"(la) : "memory", "scc");
}
DEV void dma_ac(u32 voff, v4u srd, u32 sb, u32 sa, u32 lb, u32 la) {
  u32 t;
  asm volatile("s_add_u32 m0, %5, %6\n\ts_add_u32 %0, %3, %4\n\tbuffer_load_dwordx4 %1, %2, %0 offen sc1 lds"
               : "=&s"(t) : "v"(voff), "s"(srd), "s"(sb), "s"(sa), "s"(lb), "s"(la) : "memory", "scc");
}
DEV void dma_i(u32 voff, v4u srd, u32 sb, u32 sa, u32 lb, u32 la) {
  u32 t;
  asm volatile("s_add_u32 m0, %5, %6\n\ts_add_u32 %0, %3, %4\n\tbuffer_load_dwordx4 %1, %2, %0 offen lds"
               : "=&s"(t) : "v"(voff), "s"(srd), "s"(sb), "i"(sa), "s"(lb), "i"(la) : "memory", "scc");
}
DEV void dma5c(u32 voff, v4u srd, u32 so, u32 lds) {
  u32 t;
  asm volatile("s_mov_b32 m0, %4\n\ts_add_u32 %0, %3, 0x1000\n\t"
               "buffer_load_dwordx4 %1, %2, %3 offen sc1 lds\n\t"
               "buffer_load_dwordx4 %1, %2, %3 offen offset:1024 sc1 lds\n\t"
               "buffer_load_dwordx4 %1, %2, %3 offen offset:2048 sc1 lds\n\t"
               "buffer_load_dwordx4 %1, %2, %3 offen offset:3072 sc1 lds\n\t"
               "s_add_u32 m0, %4, 0x1000\n\ts_nop 0\n\t"
               "buffer_load_dwordx4 %1, %2, %0 offen sc1 lds"
               : "=&s"(t) : "v"(voff), "s"(srd), "s"(so), "s"(lds) : "memory", "scc");
}
DEV u32 sgpr(u32 x) {
  u32 o;
  asm volatile("s_mov_b32 %0, %1" : "=s"(o) : "s"(__builtin_amdgcn_readfirstlane(x)));
  return o;
}
#define OPS(x) x = sgpr(x)
template <int N> DEV void waitv() { asm volatile("s_waitcnt vmcnt(%0)" ::"n"(N) : "memory"); }
#define WAITV(n) asm volatile("s_waitcnt vmcnt(" #n ")" ::: "memory")
DEV void bar() {
  asm volatile("" ::: "memory");
  __builtin_amdgcn_s_waitcnt(0xC07F);
  __builtin_amdgcn_s_barrier();
  asm volatile("" ::: "memory");
  SB();
}
DEV void bar_nw() {
  asm volatile("" ::: "memory");
  __builtin_amdgcn_s_barrier();
  asm volatile("" ::: "memory");
  SB();
}
DEV s4 trd(u32 a) { return __builtin_amdgcn_ds_read_tr16_b64_v4i16((LAS s4*)a); }
DEV s8 cat(s4 a, s4 b) { return __builtin_shufflevector(a, b, 0, 1, 2, 3, 4, 5, 6, 7); }
DEV s8 as8(v4u a) { return __builtin_bit_cast(s8, a); }
DEV s4 as4(v2u a) { return __builtin_bit_cast(s4, a); }
DEV f4 mfma16(s8 a, s8 b, f4 c) { return __builtin_amdgcn_mfma_f32_16x16x32_bf16(a, b, c, 0, 0, 0); }
DEV f4 mfma16h(s4 a, s4 b, f4 c) { return __builtin_amdgcn_mfma_f32_16x16x16bf16_1k(a, b, c, 0, 0, 0); }
DEV f16v mfma32(s8 a, s8 b, f16v c) { return __builtin_amdgcn_mfma_f32_32x32x16_bf16(a, b, c, 0, 0, 0); }
DEV u32 pk(float a, float b) { return __builtin_bit_cast(u32, __builtin_convertvector(f2{a, b}, bf2)); }
DEV float blo(u32 x) { return __builtin_bit_cast(float, x << 16); }
DEV float bhi(u32 x) { return __builtin_bit_cast(float, x & 0xFFFF0000u); }
DEV float dot2(u32 a, u32 b, float c) { return __builtin_amdgcn_fdot2_f32_bf16(__builtin_bit_cast(bf2, a), __builtin_bit_cast(bf2, b), c, false); }
DEV u32 fu(float x) { return __builtin_bit_cast(u32, x); }
DEV float uf(u32 x) { return __builtin_bit_cast(float, x); }
template <class T> DEV T lld(u32 a) { return *(LAS T*)a; }
template <class T> DEV void lst(u32 a, T v) { *(LAS T*)a = v; }
DEV v4u ld16c(rsrc_t r, u32 vo, u32 so) { return __builtin_amdgcn_raw_buffer_load_b128(r, vo, so, 16); }
DEV v2u ld8c(rsrc_t r, u32 vo, u32 so) { return __builtin_amdgcn_raw_buffer_load_b64(r, vo, so, 16); }
DEV u32 ld4c(rsrc_t r, u32 vo, u32 so) { return __builtin_amdgcn_raw_buffer_load_b32(r, vo, so, 16); }
DEV void st16c(v4u v, rsrc_t r, u32 vo, u32 so) {
  __builtin_amdgcn_raw_buffer_store_b128(v, r, vo, so, 16);
  asm volatile("s_nop 1" ::"v"(v) : "memory");
}
DEV void st16p(v4u v, rsrc_t r, u32 vo, u32 so) {
  __builtin_amdgcn_raw_buffer_store_b128(v, r, vo, so, 0);
  asm volatile("s_nop 1" ::"v"(v) : "memory");
}
DEV void st8c(v2u v, rsrc_t r, u32 vo, u32 so) { __builtin_amdgcn_raw_buffer_store_b64(v, r, vo, so, 16); }
DEV void st4c(u32 v, rsrc_t r, u32 vo, u32 so) { __builtin_amdgcn_raw_buffer_store_b32(v, r, vo, so, 16); }
DEV bool wall(bool c) { return __builtin_amdgcn_ballot_w64(!c) == 0ull; }
DEV u32 san(u32 x) { return x < 0xFFFFFFFEu ? x : 0xFFFFFFFEu; }
DEV u32 swap23(u32 k) { return (k & ~12u) | ((k & 4u) << 1) | ((k & 8u) >> 1); }

template <bool MAXOP, bool RCP> DEV void red4(float& a, float& b, float& c, float& d) {
  auto r1 = __builtin_amdgcn_permlane16_swap(fu(a), fu(b), false, false);
  auto r2 = __builtin_amdgcn_permlane16_swap(fu(c), fu(d), false, false);
  float ab = MAXOP ? __builtin_elementwise_maximum(uf(r1[0]), uf(r1[1])) : uf(r1[0]) + uf(r1[1]);
  float cd = MAXOP ? __builtin_elementwise_maximum(uf(r2[0]), uf(r2[1])) : uf(r2[0]) + uf(r2[1]);
  auto r3 = __builtin_amdgcn_permlane32_swap(fu(ab), fu(cd), false, false);
  float z = MAXOP ? __builtin_elementwise_maximum(uf(r3[0]), uf(r3[1])) : uf(r3[0]) + uf(r3[1]);
  if (RCP) z = __builtin_amdgcn_rcpf(z);
  auto g1 = __builtin_amdgcn_permlane32_swap(fu(z), fu(z), false, false);
  auto g2 = __builtin_amdgcn_permlane16_swap(g1[0], g1[0], false, false);
  auto g3 = __builtin_amdgcn_permlane16_swap(g1[1], g1[1], false, false);
  a = uf(g2[0]); b = uf(g2[1]); c = uf(g3[0]); d = uf(g3[1]);
}
DEV void red2(float& a, float& b) {
  auto r1 = __builtin_amdgcn_permlane16_swap(fu(a), fu(b), false, false);
  float ab = uf(r1[0]) + uf(r1[1]);
  auto r3 = __builtin_amdgcn_permlane32_swap(fu(ab), fu(ab), false, false);
  float z = uf(r3[0]) + uf(r3[1]);
  auto g2 = __builtin_amdgcn_permlane16_swap(fu(z), fu(z), false, false);
  a = uf(g2[0]); b = uf(g2[1]);
}

#define LAM __attribute__((always_inline)) NOPK
template <int V> struct IC { static constexpr int v = V; };
template <int A, int B, class F> DEV void sfor(F&& f) {
  if constexpr (A < B) { f(IC<A>{}); sfor<A + 1, B>(f); }
}
struct Phi { v2u pl[2], mi[2], ex; };
DEV float fmx(float a, float b) { return __builtin_elementwise_maximum(a, b); }
DEV float fmn(float a, float b) { return __builtin_elementwise_minimum(a, b); }
DEV void phi_mm(s8 b0, s8 b1, const s8 (&A)[4], f4 (&p)[3]) {
  const f4 z = {0.f, 0.f, 0.f, 0.f};
  p[2] = mfma16(A[2], b0, z);
  p[0] = mfma16(A[0], b0, z);
  p[1] = mfma16(A[1], b1, z);
  p[2] = mfma16(A[3], b1, p[2]);
}
DEV Phi phi_sm(const f4 (&pp)[3]) {
  const float p[2][5] = {{pp[0][0], pp[0][1], pp[0][2], pp[0][3], pp[2][0]}, {pp[1][0], pp[1][1], pp[1][2], pp[1][3], pp[2][1]}};
  float mx[2], nm[2];
#pragma unroll
  for (int h = 0; h < 2; ++h) {
    mx[h] = fmx(fmx(fmx(fmx(p[h][0], p[h][1]), p[h][2]), p[h][3]), p[h][4]);
    nm[h] = fmx(fmx(fmx(fmx(-p[h][0], -p[h][1]), -p[h][2]), -p[h][3]), -p[h][4]);
  }
  red4<true, false>(mx[0], nm[0], mx[1], nm[1]);
  float ep[2][5], em[2][5], sp[2], sm[2];
#pragma unroll
  for (int h = 0; h < 2; ++h) {
#pragma unroll
    for (int j = 0; j < 5; ++j) {
      ep[h][j] = __builtin_amdgcn_exp2f(p[h][j] - mx[h]);
      em[h][j] = __builtin_amdgcn_exp2f(-p[h][j] - nm[h]);
    }
    sp[h] = ((ep[h][0] + ep[h][1]) + (ep[h][2] + ep[h][3])) + ep[h][4];
    sm[h] = ((em[h][0] + em[h][1]) + (em[h][2] + em[h][3])) + em[h][4];
  }
  red4<false, true>(sp[0], sm[0], sp[1], sm[1]);
  Phi o;
#pragma unroll
  for (int h = 0; h < 2; ++h) {
    o.pl[h] = v2u{pk(ep[h][0] * sp[h], ep[h][1] * sp[h]), pk(ep[h][2] * sp[h], ep[h][3] * sp[h])};
    o.mi[h] = v2u{pk(em[h][0] * sm[h], em[h][1] * sm[h]), pk(em[h][2] * sm[h], em[h][3] * sm[h])};
    o.ex[h] = pk(ep[h][4] * sp[h], em[h][4] * sm[h]);
  }
  return o;
}
DEV v2u lnpk(f4 a, f4 cs, float nmean, float sc) {
  return v2u{pk(__builtin_fmaf(nmean, cs[0], a[0]) * sc, __builtin_fmaf(nmean, cs[1], a[1]) * sc),
             pk(__builtin_fmaf(nmean, cs[2], a[2]) * sc, __builtin_fmaf(nmean, cs[3], a[3]) * sc)};
}
DEV f4 lcs(u32 a) { return lld<f4>(a) + lld<f4>(a + 512); }
DEV u32 bsel(u32 m, u32 a, u32 b) { return (a & m) | (b & ~m); }
DEV void poll_flags(rsrc_t ws, u32 voff, bool active, v2u tag) {
#pragma unroll 1
  for (u32 s = 0; s < SPIN_CAP; ++s) {
    v2u v = tag;
    if (active) v = ld8c(ws, voff, 0);
    if (wall(v.x == tag.x && v.y == tag.y)) break;
    asm volatile("" ::: "memory");
  }
  asm volatile("" ::: "memory");
}
template <int SL = 1> DEV void lds_check(u32 lv, u32 a, u32 n) {
  u32 sv, cnt;
  asm volatile(
      "s_nop 0\n\tv_readfirstlane_b32 %0, %2\n\ts_cmp_ge_u32 %0, %4\n\ts_cbranch_scc1 2f\n\ts_mov_b32 %1, 0x400000\n"
      "1:\n\tv_mov_b32 %2, %3\n\tds_read_b32 %2, %2\n\ts_waitcnt lgkmcnt(0)\n\tv_readfirstlane_b32 %0, %2\n\ts_cmp_ge_u32 %0, %4\n\ts_cbranch_scc1 2f\n\t"
      "s_sleep %5\n\ts_sub_u32 %1, %1, 1\n\ts_cbranch_scc0 1b\n2:"
      : "=&s"(sv), "=&s"(cnt), "+v"(lv) : "s"(a), "s"(n), "n"(SL) : "memory", "scc");
}
template <int SL = 1> DEV void lds_wait(u32 a, u32 n = 4) { lds_check<SL>(0u, a, n); }
DEV void lds_inc(u32 a) {
  u32 t0, t1;
  asm volatile("v_mov_b32 %0, %2\n\tv_mov_b32 %1, 1\n\ts_mov_b64 exec, 1\n\tds_add_u32 %0, %1\n\ts_mov_b64 exec, -1" : "=&v"(t0), "=&v"(t1) : "s"(a) : "memory");
}
DEV void flag_store(v4u srd, v2u tag, u32 so) {
  v2u t;
  asm volatile("v_mov_b64 %0, %2\n\ts_mov_b64 exec, 1\n\tbuffer_store_dwordx2 %0, off, %1, %3 sc1\n\ts_mov_b64 exec, -1"
               : "=&v"(t) : "s"(srd), "s"(tag), "s"(so) : "memory");
}
DEV void poll_mask(rsrc_t ws, u32 fv, u32 base, v2u tag, v2u v) {
#pragma unroll 1
  for (u32 s = 0; s < SPIN_CAP; ++s) {
    fv = (v.x == tag.x && v.y == tag.y) ? OOB : fv;
    if (wall(fv == OOB)) break;
    asm volatile("" ::: "memory");
    v = ld8c(ws, fv, base);
  }
  asm volatile("" ::: "memory");
}
DEV void poll256(rsrc_t ws, u32 base, u32 stride, u32 lane, v2u tag) {
#pragma unroll 1
  for (u32 s = 0; s < SPIN_CAP; ++s) {
    v2u f[4];
    bool ok = true;
#pragma unroll
    for (int n = 0; n < 4; ++n) f[n] = ld8c(ws, lane * stride, base + n * 64 * stride);
#pragma unroll
    for (int n = 0; n < 4; ++n) ok = ok && f[n].x == tag.x && f[n].y == tag.y;
    if (wall(ok)) break;
    asm volatile("" ::: "memory");
  }
  asm volatile("" ::: "memory");
}
DEV u32 lane_id() {
  u32 l;
  asm volatile("v_mbcnt_lo_u32_b32 %0, -1, 0\n\tv_mbcnt_hi_u32_b32 %0, -1, %0" : "=v"(l));
  return l;
}
DEV u32 umax(u32 a, u32 b) { return a > b ? a : b; }
DEV u32 umax4(u32 m, v4u v) { return umax(umax(umax(umax(m, v.x), v.y), v.z), v.w); }

#define STAMP(i) do { if ((i) == 0 || (i) == 19 || ((STMASK >> (i)) & 1u)) stamp[i] = (u32)__builtin_amdgcn_s_memrealtime(); } while (0)

DEV void poll_level(rsrc_t ws, u32 voff, v2u tag, u32 level) {
#pragma unroll 1
  for (u32 s = 0; s < SPIN_CAP; ++s) {
    const v2u v = ld8c(ws, voff, level * 128);
    voff = (v.x == tag.x && v.y == tag.y) ? OOB : voff;
    if (wall(voff == OOB)) break;
    __builtin_amdgcn_s_sleep(LVS);
    asm volatile("" ::: "memory");
  }
  asm volatile("" ::: "memory");
}

template <bool TWO> DEV void kv_job(rsrc_t ws, u32 jb, u32 so) {
  const u32 lane = lane_id();
  const u32 q = lane >> 4, tq = (lane >> 2) & 3, tp = lane & 3;
  const u32 a1c = !TWO ? jb * 32 : jb < 6 ? (jb - 4) * 32 : 128;
  const u32 b1c = !TWO ? 160 + (jb >> 1) * 32 : jb == 6 ? 160 : 224;
  const u32 a2c = jb < 6 ? a1c + 64 : a1c;
  const u32 b2c = jb == 6 ? 192 : b1c;
  const u32 rb = L_IMG + (2 * (q >> 1) + 16 * (q & 1) + 4 * tq) * 264 + 8 * tp;
  constexpr int NO = TWO ? 4 : 2;
  u32 ra[4] = {rb + a1c, rb + b1c, rb + a2c, rb + b2c};
  u32 mlo = (jb != 6 && (lane & 1)) ? ~0u : 0u, mhi = (jb == 6 || (lane & 1)) ? ~0u : 0u, vl = lane * 16;
#pragma unroll
  for (int n = 0; n < NO; ++n) OPQ(ra[n]);
  OPQ(vl);
  if (TWO) { OPQ(mlo); OPQ(mhi); }
  bar();
  s8 fr[4][NO];
#pragma unroll
  for (int ks = 0; ks < 4; ++ks)
#pragma unroll
    for (int n = 0; n < NO; ++n) fr[ks][n] = cat(trd(ra[n] + ks * 32 * 264), trd(ra[n] + ks * 32 * 264 + 264));
  SB();
  f4 d1 = {0.f, 0.f, 0.f, 0.f}, d2 = d1;
#pragma unroll
  for (int ks = 0; ks < 4; ++ks) {
    d1 = mfma16(fr[ks][0], fr[ks][1], d1);
    if (TWO) d2 = mfma16(fr[ks][NO - 2], fr[ks][NO - 1], d2);
  }
  if (TWO) d1 = f4{uf(bsel(mlo, fu(d2[0]), fu(d1[0]))), uf(bsel(mlo, fu(d2[1]), fu(d1[1]))), uf(bsel(mhi, fu(d2[2]), fu(d1[2]))), uf(bsel(mhi, fu(d2[3]), fu(d1[3])))};
  st16c(v4u{san(fu(d1[0])), san(fu(d1[1])), san(fu(d1[2])), san(fu(d1[3]))}, ws, vl, so + jb * 1024);
}

typedef u64 v8q __attribute__((ext_vector_type(8)));
__global__ __launch_bounds__(512, 2) NOPK void fused_block(const u16* x_, const u16* wqkv_, const u16* wphi_, const u16* wo_, const u16* wg_, const u16* wu_,
                                                           const u16* wd_, u16* out_, char* wsp_) {
  v8q ka;
  u64 kw;
  asm volatile("s_load_dwordx16 %0, %2, 0x0\n\ts_load_dwordx2 %1, %2, 0x40" : "=&s"(ka), "=&s"(kw) : "s"(__builtin_amdgcn_kernarg_segment_ptr()));
  u64 pc;
  asm volatile("s_getpc_b64 %0" : "=s"(pc));
  SB();
  u32 stamp[32];
#pragma unroll
  for (int i = 0; i < 32; ++i) stamp[i] = 0;
  STAMP(0);
  const u32 wv = __builtin_amdgcn_readfirstlane(threadIdx.x >> 6);
  const u32 b = blockIdx.x, xcd = b & 7, idx = b >> 3;
  const u32 team = (xcd >> 1) * 4 + (idx >> 3), rank = ((idx & 4) << 1) | ((xcd & 1) << 2) | (idx & 3);
  const u32 bid = team * 16 + rank, hq = rank & 7, th = rank >> 3;
  const bool u7 = rank < 12;
  const f4 z4 = {0.f, 0.f, 0.f, 0.f};
#define DUMP() STAMP(19); if (wv == 4) { const u32 d_ = stamp[0] - stamp[0]; while ((u32)__builtin_amdgcn_s_memrealtime() - stamp[19] < d_) __builtin_amdgcn_s_sleep(1); }
#define GET_ARGS()                                                                                         \
  SB();                                                                                                    \
  asm volatile("s_waitcnt lgkmcnt(0)" ::: "memory");                                                       \
  SB();                                                                                                    \
  asm volatile("" : "+s"(ka), "+s"(kw));                                                                   \
  char* const wsp = (char*)kw;                                                                             \
  const rsrc_t ws = mk_rsrc(wsp, W_END);                                                                   \
  const v4u sws = mk_srd(wsp, W_END);
#define GET_EPOCH()                                                                                        \
  asm volatile("s_waitcnt lgkmcnt(0)" ::: "memory");                                                       \
  SB();                                                                                                    \
  asm volatile("" : "+s"(ep));                                                                             \
  ep = (ep & ~15ull) + 16ull;                                                                              \
  const v2u tag = {(u32)ep, (u32)(ep >> 32)};

  if (wv >= 4) {
    const u32 w = wv - 4;
    const u32 lane = lane_id();
    const u32 q = lane >> 4, i16 = lane & 15;
    {
      const v4u sc = mk_srd((const void*)(pc & ~255ull), CODE_BYTES);
      if (w == 0) asm volatile("s_mov_b32 m0, %2\n\ts_nop 0\n\tbuffer_load_dword %0, %1, 0 offen lds" ::"v"(lane < 16 ? (lane * 32 + idx) * 128 : OOB), "s"(sc), "s"(L_KV) : "memory");
    }
    const u32 vx0 = (lane >> 3) * 1280 + (((lane & 7) ^ (lane >> 4)) << 4), vx1 = (vx0 ^ 64) + 8 * 1280;
    u32 xs0 = (team * 128 + w * 32) * 1280, xl0 = L_B + w * 12288;
    OPS(xs0); OPS(xl0);
    GET_ARGS();
    const u16* const x = (const u16*)ka[0];
    u16* const out = (u16*)ka[7];
    const v4u sx = mk_srd(x, SEQ * DM * 2);
    bar_nw();
    u64 ep;
    asm volatile("s_load_dwordx2 %0, %1, 0x0" : "=s"(ep) : "s"(wsp));
    auto stagex = [&](int c) LAM {
      dma_i(vx0, sx, xs0, c * 128, xl0, (c % 3) * 4096);
      dma_i(vx1, sx, xs0, c * 128, xl0, (c % 3) * 4096 + 1024);
      dma_i(vx0, sx, xs0, c * 128 + 16 * 1280, xl0, (c % 3) * 4096 + 2048);
      dma_i(vx1, sx, xs0, c * 128 + 16 * 1280, xl0, (c % 3) * 4096 + 3072);
    };
    stagex(0); stagex(1);
    stagex(2);
    v4u xa[2][20];
    float nm1[2], rs1[2];
    {
      v4u onesv = {0x3F803F80u, 0x3F803F80u, 0x3F803F80u, 0x3F803F80u};
      OPQ(onesv);
      const s8 ones = as8(onesv);
      f4 as[2] = {z4, z4}, ag[2] = {z4, z4};
      u32 ax0 = L_B + w * 12288 + i16 * 128 + ((q ^ ((i16 >> 1) & 7)) << 4), ax1 = ax0 ^ 64;
      OPQ(ax0); OPQ(ax1);
      sfor<0, 11>([&](auto cc_) LAM {
        constexpr int c = decltype(cc_)::v;
        if constexpr (c < 10) waitv<(c < 8 ? 8 : c == 8 ? 4 : 0)>();
        __builtin_amdgcn_s_waitcnt(0xC07F);
        SB();
        if constexpr (c < 10) {
#pragma unroll
          for (int t = 0; t < 2; ++t) { xa[t][2 * c] = lld<v4u>(ax0 + (c % 3) * 4096 + t * 2048); xa[t][2 * c + 1] = lld<v4u>(ax1 + (c % 3) * 4096 + t * 2048); }
        }
        if constexpr (c < 7) { __builtin_amdgcn_s_waitcnt(0xC07F); SB(); stagex(c + 3); }
        if constexpr (c == 10) lds_inc(S_XD);
        if constexpr (c >= 1) {
#pragma unroll
          for (int k = 2 * c - 2; k < 2 * c; ++k)
#pragma unroll
            for (int t = 0; t < 2; ++t) { as[t] = mfma16(ones, as8(xa[t][k]), as[t]); ag[t] = mfma16(as8(xa[t][k]), as8(xa[t][k]), ag[t]); }
        }
        SB();
      });
      STAMP(23);
      float g[2];
#pragma unroll
      for (int t = 0; t < 2; ++t) {
        u32 v = 0;
#pragma unroll
        for (int r = 0; r < 4; ++r) v |= fu(ag[t][r]) & (i16 == 4 * q + r ? ~0u : 0u);
        g[t] = uf(v);
      }
      red2(g[0], g[1]);
#pragma unroll
      for (int t = 0; t < 2; ++t) {
        nm1[t] = -as[t][0] * (1.0f / DM);
        rs1[t] = __builtin_amdgcn_rsqf(__builtin_fmaxf(__builtin_fmaf(-nm1[t], nm1[t], g[t] * (1.0f / DM)), 0.f) + 1e-5f);
        OPQ(nm1[t]); OPQ(rs1[t]);
      }
    }
    GET_EPOCH();
    __builtin_amdgcn_s_setprio(2);
    const float L2E = 1.4426950408889634f;
    {
      f4 acc[2][5] = {{z4, z4, z4, z4, z4}, {z4, z4, z4, z4, z4}};
      u32 bw[2] = {L_A + lane * 16, L_A + 51200 + lane * 16};
      OPQ(bw[0]); OPQ(bw[1]);
      constexpr int KD = KDEP;
      s8 kf[KD + 1];
      u32 lv;
      lds_wait(S_FA);
      { v2u tg = tag; OPQ(tg); st8c(tg, ws, lane_id() << 26, w == 0 ? W_FB + bid * 32 : OOB); }
      lds_wait(S_L1);
      STAMP(25);
#pragma unroll
      for (int f = 0; f < KD; ++f) kf[f] = lld<s8>(bw[0] + f * 1024);
      SB();
      sfor<0, 100>([&](auto gc) LAM {
        constexpr int g = decltype(gc)::v, gn = g + KD;
        if constexpr (gn < 100 && gn % 20 == 0) {
          lds_check(lv, S_L1 + 4 * (gn / 20), 4);

        }
        if constexpr (gn < 100) kf[gn % (KD + 1)] = lld<s8>(bw[gn / 50] + (gn % 50) * 1024);
        if constexpr (gn == 44) lds_inc(S_CS);
        if constexpr (gn % 20 == 14) lv = *(volatile LAS u32*)(gn < 80 ? S_L1 + 4 * (gn / 20 + 1) : S_CS);
        acc[0][g % 5] = mfma16(kf[g % (KD + 1)], as8(xa[0][g / 5]), acc[0][g % 5]);
        acc[1][g % 5] = mfma16(kf[g % (KD + 1)], as8(xa[1][g / 5]), acc[1][g % 5]);
        SB();
      });
      lds_inc(S_KVD);
      lds_check(lv, S_CS, 8);
      STAMP(2);
      const u32 cb = L_CS1 + 160 + q * 16;
      const u32 mq = q < 2 ? ~0u : 0u;
      f4 pp[2][3];
      {
        const f4 c0 = lcs(cb), c1 = lcs(cb + 80), c2 = lcs(L_CS1 + 224 + q * 80);
        const s8 A[4] = {lld<s8>(L_WPHI + lane * 16), lld<s8>(L_WPHI + 1024 + lane * 16), lld<s8>(L_PH2 + lane * 16), lld<s8>(L_PH2 + 1024 + lane * 16)};
#pragma unroll
        for (int t = 0; t < 2; ++t) {
          const float sk = rs1[t] * L2E;
          const v2u k0 = lnpk(acc[t][0], c0, nm1[t], sk), k1 = lnpk(acc[t][1], c1, nm1[t], sk), k2 = lnpk(acc[t][2], c2, nm1[t], uf(bsel(mq, fu(sk), fu(rs1[t]))));
          phi_mm(as8(v4u{k0.x, k0.y, k2.x, k2.y}), as8(v4u{k1.x, k1.y, k2.x, k2.y}), A, pp[t]);
          const u32 e0 = bsel(mq, 0x3F803F80u, k2.x), e1 = bsel(mq, 0x3F803F80u, k2.y), eb = L_IMG + (w * 32 + t * 16 + i16) * 264 + 224 + (q ^ 2) * 2;
          lst<u16>(eb, (u16)e0); lst<u16>(eb + 8, (u16)(e0 >> 16)); lst<u16>(eb + 16, (u16)e1); lst<u16>(eb + 24, (u16)(e1 >> 16));
          SB();
        }
      }
      {
        const f4 c3 = lcs(cb + 160), c4 = lcs(cb + 240);
#pragma unroll
        for (int t = 0; t < 2; ++t) {
          const u32 ib = L_IMG + (w * 32 + t * 16 + i16) * 264;
          lst<v2u>(ib + q * 8 + 160, lnpk(acc[t][3], c3, nm1[t], rs1[t]));
          lst<v2u>(ib + q * 8 + 192, lnpk(acc[t][4], c4, nm1[t], rs1[t]));
        }
      }
      SB();
#pragma unroll
      for (int t = 0; t < 2; ++t) {
        const Phi ph = phi_sm(pp[t]);
        const u32 ib = L_IMG + (w * 32 + t * 16 + i16) * 264 + q * 8;
        lst<v2u>(ib + 0, ph.pl[0]); lst<v2u>(ib + 32, ph.mi[0]); lst<v2u>(ib + 64, ph.pl[1]); lst<v2u>(ib + 96, ph.mi[1]);
        lst<v2u>(ib + 128, ph.ex);
      }
    }
    STAMP(3);
    kv_job<false>(ws, w, W_PART + (rank * 16 + team) * 8192);
    STAMP(4);
    bar_nw();
    {
      const u32 lane = lane_id(), q = lane >> 4, i16 = lane & 15;
      f4 qa[2][3] = {{z4, z4, z4}, {z4, z4, z4}};
      u32 bq = L_B + lane * 16, bq2 = L_B + 4096 + ((lane & 7) + 16 * q) * 16;
      OPQ(bq); OPQ(bq2);
      constexpr int QD = 4;
      s8 qf[QD + 1];
      auto ldq = [&](int f) LAM {
        const int t = f % 3, ks = f / 3;
        qf[f % (QD + 1)] = lld<s8>(t == 2 ? bq2 + (ks >> 1) * 5120 + (ks & 1) * 128 : bq + (ks >> 1) * 5120 + ((ks & 1) * 2 + t) * 1024);
      };
#pragma unroll
      for (int f = 0; f < QD; ++f) ldq(f);
      SB();
#pragma unroll
      for (int f = 0; f < 60; ++f) {
        if (f + QD < 60) ldq(f + QD);
        qa[0][f % 3] = mfma16(qf[f % (QD + 1)], as8(xa[0][f / 3]), qa[0][f % 3]);
        qa[1][f % 3] = mfma16(qf[f % (QD + 1)], as8(xa[1][f / 3]), qa[1][f % 3]);
        SB();
      }
      STAMP(5);
      const f4 c0 = lcs(L_CS1 + q * 16), c1 = lcs(L_CS1 + 80 + q * 16), c2 = lcs(L_CS1 + 64 + (q & 1) * 80);
      const s8 A[4] = {lld<s8>(L_WPHI + lane * 16), lld<s8>(L_WPHI + 1024 + lane * 16), lld<s8>(L_PH2 + lane * 16), lld<s8>(L_PH2 + 1024 + lane * 16)};
      f4 pp[2][3];
      Phi pq[2];
#pragma unroll
      for (int t = 0; t < 2; ++t) {
        const float sk = rs1[t] * L2E;
        const v2u k0 = lnpk(qa[t][0], c0, nm1[t], sk), k1 = lnpk(qa[t][1], c1, nm1[t], sk), k2 = lnpk(qa[t][2], c2, nm1[t], sk);
        phi_mm(as8(v4u{k0.x, k0.y, k2.x, k2.y}), as8(v4u{k1.x, k1.y, k2.x, k2.y}), A, pp[t]);
      }
      v4u b0[2], b1[2], bx[2];
#pragma unroll
      for (int t = 0; t < 2; ++t) {
        pq[t] = phi_sm(pp[t]);
        b0[t] = v4u{pq[t].pl[0].x, pq[t].pl[0].y, pq[t].mi[0].x, pq[t].mi[0].y};
        b1[t] = v4u{pq[t].pl[1].x, pq[t].pl[1].y, pq[t].mi[1].x, pq[t].mi[1].y};
        bx[t] = v4u{pq[t].ex.x, pq[t].ex.y, 0u, 0u};
        OPQ(b0[t]); OPQ(b1[t]); OPQ(bx[t]);
      }
      u32 mo = (lane & 1) ? ~0u : 0u, ve = i16 * 16 + q * 4;
      OPQ(mo); OPQ(ve);
      STAMP(7);
      bar_nw();
      STAMP(8);
      v2u it[8];
#pragma unroll
      for (int n = 0; n < 8; ++n) it[n] = lld<v2u>(L_KV + n * 512 + lane * 8);
      const s8 ae = as8(v4u{it[4].x & ~mo, it[4].y & ~mo, it[5].x & ~mo, it[5].y & ~mo}), ao = as8(v4u{it[4].x & mo, it[4].y & mo, it[5].x & mo, it[5].y & mo});
      const s8 ax = as8(v4u{it[7].x & ~mo, it[7].y & mo, 0u, 0u});
      f4 nx[2], n0[2], n1[2];
#pragma unroll
      for (int t = 0; t < 2; ++t) {
        nx[t] = mfma16(ae, as8(b0[t]), z4);
        nx[t] = mfma16(ao, as8(b1[t]), nx[t]);
        nx[t] = mfma16(ax, as8(bx[t]), nx[t]);
      }
#pragma unroll
      for (int t = 0; t < 2; ++t) {
        n0[t] = mfma16(as8(v4u{it[0].x, it[0].y, it[1].x, it[1].y}), as8(b0[t]), z4);
        n1[t] = mfma16(as8(v4u{it[2].x, it[2].y, it[3].x, it[3].y}), as8(b1[t]), z4);
        n0[t] = mfma16(as8(v4u{it[6].x, 0u, 0u, 0u}), as8(bx[t]), n0[t]);
        n1[t] = mfma16(as8(v4u{0u, it[6].y, 0u, 0u}), as8(bx[t]), n1[t]);
      }
#pragma unroll
      for (int t = 0; t < 2; ++t) {
        const float r0 = __builtin_amdgcn_rcpf(nx[t][2] + 1e-6f), r1 = __builtin_amdgcn_rcpf(nx[t][3] + 1e-6f);
        const u32 ob = W_O + (team * 8 + 2 * w + t) * 20480;
        st4c(pk(nx[t][0] * r0, nx[t][1] * r1), ws, ve, ob + 16384 + rank * 256);
        st16c(v4u{pk(n0[t][0] * r0, n0[t][1] * r0), pk(n0[t][2] * r0, n0[t][3] * r0), pk(n1[t][0] * r1, n1[t][1] * r1), pk(n1[t][2] * r1, n1[t][3] * r1)}, ws,
              lane * 16, ob + rank * 1024);
      }
    }
    {
      const u32 lane = lane_id(), q = lane >> 4, i16 = lane & 15;
      const u32 tk = th * 64 + w * 16 + i16;
      const u32 xo = tk * 1280 + q * 16, xso = team * 128 * 1280 + hq * 160;
      const rsrc_t rx = mk_rsrc(x, SEQ * DM * 2);
      const v4u xr0 = __builtin_amdgcn_raw_buffer_load_b128(rx, xo, xso, 0), xr1 = __builtin_amdgcn_raw_buffer_load_b128(rx, xo + 64, xso, 0);
      const v2u xr2 = __builtin_amdgcn_raw_buffer_load_b64(rx, tk * 1280 + 128 + q * 8, xso, 0);
      constexpr int OD = 6;
      s8 wf[OD][5];
      u32 bo[2] = {L_WO + lane * 16, L_WO + 51200 + lane * 16};
      OPQ(bo[0]); OPQ(bo[1]);
      const u32 oso = W_O + (team * 8 + th * 4 + w) * 20480;
      auto ldo1 = [&](int ks, int t) LAM { wf[ks % OD][t] = lld<s8>(bo[ks / 10] + (ks % 10) * 5120 + t * 1024); };
#pragma unroll
      for (int ks = 0; ks < OD; ++ks)
#pragma unroll
        for (int t = 0; t < 5; ++t) ldo1(ks, t);
      WAITV(3);
      flag_store(sws, tag, W_FS2 + (bid * 4 + w) * 128);
      lds_inc(S_OP);
      STAMP(9);
      f4 ha[5] = {f4{blo(xr0.x), bhi(xr0.x), blo(xr0.y), bhi(xr0.y)}, f4{blo(xr0.z), bhi(xr0.z), blo(xr0.w), bhi(xr0.w)}, f4{blo(xr1.x), bhi(xr1.x), blo(xr1.y), bhi(xr1.y)},
                  f4{blo(xr1.z), bhi(xr1.z), blo(xr1.w), bhi(xr1.w)}, f4{blo(xr2.x), bhi(xr2.x), blo(xr2.y), bhi(xr2.y)}};
#pragma unroll
      for (int t = 0; t < 5; ++t) OPQ(ha[t]);
      __builtin_amdgcn_s_waitcnt(0xC07F);
      __builtin_amdgcn_s_sleep(PD2);
      SB();
      poll_flags(ws, W_FS2 + ((team * 16 + lane) * 4 + th * 2 + (w >> 1)) * 128, lane < 16, tag);
      STAMP(10);
      v4u of[20];
      constexpr int PD = 12;
      auto ldof = [&](int ks) LAM { of[ks] = ld16c(ws, lane * 16 + (ks & 3) * 1024, oso + (ks >> 2) * 4096); };
#pragma unroll
      for (int ks = 0; ks < PD; ++ks) ldof(ks);
      SB();
#pragma unroll
      for (int ks = 0; ks < 20; ++ks) {
#pragma unroll
        for (int t = 0; t < 5; ++t) {
          ha[t] = mfma16(wf[ks % OD][t], as8(of[ks]), ha[t]);
          if (t == 0 && ks + PD < 20) ldof(ks + PD);
          if (t == 1 && ks + PD == 19) lds_inc(S_OL);
          if (ks + OD < 20) ldo1(ks + OD, t);
          SB();
        }
      }
      STAMP(11);
      v4u hc[2];
#pragma unroll
      for (int m = 0; m < 2; ++m)
        hc[m] = v4u{pk(ha[2 * m][0], ha[2 * m][1]), pk(ha[2 * m][2], ha[2 * m][3]), pk(ha[2 * m + 1][0], ha[2 * m + 1][1]), pk(ha[2 * m + 1][2], ha[2 * m + 1][3])};
      const v2u ht = {pk(ha[4][0], ha[4][1]), pk(ha[4][2], ha[4][3])};
      const u32 hb = W_H + (team * 4 + th * 2 + (w >> 1)) * 40960 + hq * 5120;
      const u32 t32 = (w & 1) * 16 + i16;
      st16c(hc[0], ws, (q >> 1) * 1024 + (t32 + 32 * (q & 1)) * 16, hb);
      st16c(hc[1], ws, (q >> 1) * 1024 + (t32 + 32 * (q & 1)) * 16, hb + 2048);
      st8c(ht, ws, (t32 + 32 * (q >> 1)) * 16 + 8 * (q & 1), hb + 4096);
      float a[2] = {0.f, 0.f}, c[2] = {0.f, 0.f};
#pragma unroll
      for (int m = 0; m < 2; ++m) {
#pragma unroll
        for (int j = 0; j < 4; ++j) { a[m] = dot2(hc[m][j], 0x3F803F80u, a[m]); c[m] = dot2(hc[m][j], hc[m][j], c[m]); }
        a[m] = dot2(ht[m], 0x3F803F80u, a[m]); c[m] = dot2(ht[m], ht[m], c[m]);
      }
      float a2 = a[0] + a[1], c2 = c[0] + c[1];
      red2(a2, c2);
      st8c(v2u{fu(a2), fu(c2)}, ws, q == 0 ? tk * 8 : OOB, W_STAT + (team * 8 + hq) * 1024);
      lst<v4u>(L_HST + w * 2560 + lane * 16, hc[0]);
      lst<v4u>(L_HST + w * 2560 + 1024 + lane * 16, hc[1]);
      lst<v2u>(L_HST + w * 2560 + 2048 + lane * 8, ht);
    }
    WAITV(0);
    flag_store(sws, tag, W_FS3 + (bid * 4 + w) * 128);
    lds_inc(S_WOC);
    STAMP(12);
    __builtin_amdgcn_s_sleep(PD2);
    const u32 lane3 = lane_id();
#define lane lane3
    poll_flags(ws, W_FS3 + ((team * 16 + (w >> 1) * 8 + (lane >> 1)) * 4 + 2 * (w & 1) + (lane & 1)) * 128, lane < 16, tag);
    STAMP(13);
    v4u wl[3][5];
    u32 wlv;
    auto ldwl = [&](int k) LAM {
      const int i = 11 + k / 5;
      if (k == 0) wlv = lane_id() * 16;
      wl[k / 5][k % 5] = ld16c(ws, ((i >= 12 && w == 3) ? OOB : wlv) + (k % 5) * 1024, W_WD + (hq * 54 + 2 * w + (i >> 1) * 8 + (i & 1)) * 5120);
    };
    {
      const u32 hh = lane >> 5, t32 = lane & 31;
      v2u sv[4];
#pragma unroll
      for (int n = 0; n < 4; ++n) sv[n] = ld8c(ws, (hh * 4 + n) * 1024 + t32 * 8, W_STAT + team * 8192 + w * 256);
      v4u hf[40];
      constexpr int HD = 12;
      const u32 hso = W_H + (team * 4 + w) * 40960;
      auto ldh = [&](int s) LAM { hf[s] = ld16c(ws, lane * 16 + (s & 3) * 1024, hso + (s >> 2) * 4096); };
#pragma unroll
      for (int s = 0; s < HD; ++s) ldh(s);
      SB();
      lds_wait(L_LAND, 4);
      u32 lb[3] = {L_HS + lane * 16, L_HS + 3 * HSLOT + lane * 16, L_HS + 6 * HSLOT + lane * 16};
      OPQ(lb[0]); OPQ(lb[1]); OPQ(lb[2]);
      auto ldw = [&](int n, int s) LAM -> s8 {
        const int hs = (2 * n + s / 20 + 5) % 7;
        return lld<s8>(lb[hs / 3] + (hs % 3) * HSLOT + (s % 20) * 1024);
      };
      auto ldcs = [&](int n) LAM -> f16v {
        const u32 csb = L_CS3 + (lane_id() & 32);
        const f4 g0 = lld<f4>(csb + n * 128), g1 = lld<f4>(csb + n * 128 + 16), u0 = lld<f4>(csb + n * 128 + 64), u1 = lld<f4>(csb + n * 128 + 80);
        return __builtin_shufflevector(__builtin_shufflevector(g0, g1, 0, 1, 2, 3, 4, 5, 6, 7), __builtin_shufflevector(u0, u1, 0, 1, 2, 3, 4, 5, 6, 7), 0, 1, 2, 3, 4,
                                       5, 6, 7, 8, 9, 10, 11, 12, 13, 14, 15);
      };
      f16v ac[3];
      ac[0] = ldcs(0); ac[1] = ldcs(1); ac[2] = ldcs(2);
      constexpr int RD = RDEP;
      s8 rg[RD];
#pragma unroll
      for (int f = 0; f < RD; ++f) rg[f] = ldw(f & 1, f >> 1);
      SB();
      float nmean, cK1, cK2, cR;
      {
        const float a = (uf(sv[0].x) + uf(sv[1].x)) + (uf(sv[2].x) + uf(sv[3].x)), c = (uf(sv[0].y) + uf(sv[1].y)) + (uf(sv[2].y) + uf(sv[3].y));
        auto r = __builtin_amdgcn_permlane32_swap(fu(a), fu(a), false, false);
        auto r2 = __builtin_amdgcn_permlane32_swap(fu(c), fu(c), false, false);
        nmean = -(uf(r[0]) + uf(r[1])) * (1.0f / DM);
        cR = __builtin_fmaxf(__builtin_fmaf(-nmean, nmean, (uf(r2[0]) + uf(r2[1])) * (1.0f / DM)), 0.f) + 1e-5f;
        const float rstd = __builtin_amdgcn_rsqf(cR), k1 = -2.0f * 0.7978845608028654f * 1.4426950408889634f;
        cK1 = k1 * rstd; cK2 = cK1 * 0.044715f * rstd * rstd;
      }
#pragma unroll
      for (int j = 0; j < 16; ++j) { ac[0][j] *= nmean; ac[1][j] *= nmean; }
      SB();
      u32 lv;
#pragma unroll
      for (int f = 0; f < 80; ++f) {
        ac[f & 1] = mfma32(rg[f % RD], as8(hf[f >> 1]), ac[f & 1]);
        const int fn = f + RD;
        rg[f % RD] = fn < 80 ? ldw(fn & 1, fn >> 1) : ldw(2, fn - 80);
        if (f >= 8 && f < 24) ac[2][f - 8] *= nmean;
        if ((f & 1) == 0 && (f >> 1) + HD < 40) ldh((f >> 1) + HD);
        if (f == 2 * (39 - HD) + 1) lds_inc(S_HL);
        if (f == 80 - RD) lds_inc(L_CONS + 4);
        SB();
      }
      OPQ(cK1); OPQ(cK2); OPQ(cR);
      STAMP(14);
      const u32 gvo = (t32 >> 4) * 54 * 1024 + ((t32 & 15) + 16 * hh) * 16 + (rank & 1) * 512;
      const u32 gso = W_G + (team * 8 + 2 * w) * 54 * 1024 + (rank >> 1) * 1024;
      const u32 fso = W_FS4 + (bid * 4 + w) * 1024;
      float tv, au, pu, pr, ov[2];
      v4u gp;
      auto gstep = [&](const f16v& a, int e) LAM {
        const int j = e / 3, m = e % 3;
        if (m == 0 && j < 8) { tv = a[j] * a[j]; tv = __builtin_fmaf(tv, cK2, cK1); tv = tv * a[j]; au = a[j] * a[8 + j]; }
        if (m == 1) tv = __builtin_amdgcn_exp2f(tv);
        if ((m == 1 && j > 0) || e == 24) { ov[(j - 1) & 1] = pu * pr; if (((j - 1) & 1) == 1) gp[(j - 1) >> 1] = pk(ov[0], ov[1]); }
        if (m == 2) { pr = __builtin_amdgcn_rcpf(__builtin_fmaf(tv, cR, cR)); pu = au; }
      };
      auto pass = [&](auto nc) LAM {
        constexpr int n = decltype(nc)::v;
        sfor<0, 40>([&](auto sc) LAM {
          constexpr int s = decltype(sc)::v, g = (n - 2) * 40 + s, sn = s + RD;
          constexpr int G[6] = {1, 31, 61, 91, 122, 161}, I[4] = {26, 56, 86, 150};
          ac[n % 3] = mfma32(rg[s % RD], as8(hf[s]), ac[n % 3]);
          if constexpr (sn < 40) rg[s % RD] = ldw(n, sn);
          else if constexpr (n < 6) rg[s % RD] = ldw(n + 1, sn - 40);
          sfor<0, 6>([&](auto uc) LAM {
            constexpr int u = decltype(uc)::v;
            if constexpr (g >= G[u] && g <= G[u] + 24 && (!(ABL & 1) || g - G[u] < 6 || g - G[u] == 24)) gstep(ac[u % 3], g - G[u]);
            if constexpr (g == G[u] + 24) {
              if constexpr (u >= 2) { if (!(ABL & 2)) WAITV(0); flag_store(sws, tag, fso + u * 128); }
              st16c(gp, ws, gvo, gso + u * 8192);
            }
          });
          sfor<0, 4>([&](auto kc) LAM {
            constexpr int k = decltype(kc)::v;
            if constexpr (g == I[k] - 3) ac[k % 3] = ldcs(3 + k);
            if constexpr (g > I[k] && g <= I[k] + 4) {
#pragma unroll
              for (int j = 0; j < 4; ++j) ac[k % 3][4 * (g - I[k] - 1) + j] *= nmean;
            }
          });
          if constexpr (WLP && n == 6 && s >= 25) ldwl(s - 25);
          if constexpr (s == 40 - RD) lds_inc(L_CONS + 4 * n);
          if constexpr (s == 8) lv = *(volatile LAS u32*)(L_LAND + 4 * (n - 1));
          if constexpr (s == 14 && !(ABL & 4)) lds_check(lv, L_LAND + 4 * (n - 1), 4);
          SB();
        });
      };
      pass(IC<2>{}); STAMP(26); pass(IC<3>{}); STAMP(27); pass(IC<4>{}); STAMP(28); pass(IC<5>{}); STAMP(29);
      pass(IC<6>{});
      STAMP(15);
#pragma unroll
      for (int e = 0; e < 25; ++e) gstep(ac[0], e);
      st16c(gp, ws, u7 ? gvo : OOB, gso + 6 * 8192);
      if (WLP) WAITV(16); else WAITV(1);
      flag_store(sws, tag, fso + 6 * 128);
    }
#undef lane
    STAMP(30);
    {
      const u32 ln = lane_id(), vl = ln * 16;
      const u32 fv = W_FS4 + ((team * 16 + 4 * w + (ln >> 4)) * 4 + 2 * th + ((ln >> 3) & 1)) * 1024;
      const u32 gso = W_G + (team * 8 + th * 4) * 54 * 1024 + 2 * w * 1024;
      const u32 vz = w < 3 ? vl : OOB;
      u32 gsr[4];
#pragma unroll
      for (int r = 0; r < 4; ++r) gsr[r] = sgpr(gso + ((r + w + 1) & 3) * (54 * 1024));
      v4u wf[6][5], gf[6][4];
      auto ldg = [&](int i) LAM {
#pragma unroll
        for (int r = 0; r < 4; ++r) gf[i - 8][r] = ld16c(ws, i >= 12 ? vz : vl, gsr[r] + ((i >> 1) * 8 + (i & 1)) * 1024);
        SB();
      };
      v2u fl = ld8c(ws, fv, 5 * 128);
      if (!WLP) sfor<0, 5>([&](auto kc) LAM { ldwl(decltype(kc)::v); });
      if (WLP) WAITV(2); else WAITV(7);
      flag_store(sws, tag, W_FS4 + (bid * 4 + w) * 1024 + 7 * 128);
      STAMP(16);
      if (!wall(fl.x == tag.x && fl.y == tag.y)) poll_level(ws, fv, tag, 5);
      asm volatile("" ::: "memory");
      ldg(8); ldg(9);
      fl = ld8c(ws, fv, 6 * 128);
      if (!WLP) sfor<5, 15>([&](auto kc) LAM { ldwl(decltype(kc)::v); });
#pragma unroll
      for (int i = 11; i < 14; ++i)
#pragma unroll
        for (int t = 0; t < 5; ++t) wf[i - 8][t] = wl[i - 11][t];
      u32 sb = L_HS + 2 * HSLOT + w * 5120 + vl;
      OPQ(sb);
      lds_wait(S_SP);
#pragma unroll
      for (int i = 8; i < 10; ++i)
#pragma unroll
        for (int t = 0; t < 5; ++t) wf[i - 8][t] = lld<v4u>(sb + (i - 8) * HSLOT + t * 1024);
      if (!wall(fl.x == tag.x && fl.y == tag.y)) poll_level(ws, fv, tag, 6);
      asm volatile("" ::: "memory");
      STAMP(31);
      ldg(10);
      f4 da[4][5];
      auto step = [&](int i) LAM {
#pragma unroll
        for (int m = 0; m < 20; ++m) da[m / 5][m % 5] = mfma16(as8(wf[i - 8][m % 5]), as8(gf[i - 8][m / 5]), i == 8 ? z4 : da[m / 5][m % 5]);
        SB();
      };
      step(8);
      ldg(11);
      fl = ld8c(ws, fv, 7 * 128);
#pragma unroll
      for (int t = 0; t < 5; ++t) wf[2][t] = lld<v4u>(sb + 2 * HSLOT + t * 1024);
      SB();
      step(9);
      if (w < 3 && !wall(fl.x == tag.x && fl.y == tag.y)) poll_level(ws, fv, tag, 7);
      asm volatile("" ::: "memory");
      STAMP(1);
      ldg(12); ldg(13);
      step(10); step(11);
      STAMP(6);
      step(12);
#pragma unroll
      for (int m = 0; m < 21; ++m) {
        if (m < 20) da[m / 5][m % 5] = mfma16(as8(wf[5][m % 5]), as8(gf[5][m / 5]), da[m / 5][m % 5]);
        if (m >= 6) lst<f4>(sb + ((m - 6) / 5) * HSLOT + ((m - 6) % 5) * 1024, da[(m - 6) / 5][(m - 6) % 5]);
        SB();
      }
      u32 rb[4];
#pragma unroll
      for (int r = 0; r < 3; ++r) { rb[r] = L_HS + (2 + r) * HSLOT + ((w - r - 1) & 3) * 5120 + vl; OPQ(rb[r]); }
      rb[3] = L_HS + ((w + 5) % 7) * HSLOT + vl;
      OPQ(rb[3]);
      const rsrc_t ro = mk_rsrc(out, SEQ * DM * 2);
      const u32 oso = (team * 128 + th * 64 + w * 16) * 1280 + hq * 160;
      u32 ovo = (ln & 15) * 1280 + (ln >> 4) * 16, ov2 = (ln & 15) * 1280 + 128 + (ln >> 4) * 8;
      OPQ(ovo); OPQ(ov2);
      bar();
      STAMP(17);
      f4 r[5][4], rr[5];
#pragma unroll
      for (int n = 0; n < 5; ++n)
#pragma unroll
        for (int j = 0; j < 4; ++j) r[n][j] = lld<f4>(rb[j] + n * 1024);
      SB();
#pragma unroll
      for (int n = 0; n < 5; ++n) rr[n] = ((da[3][n] + r[n][0]) + (r[n][1] + r[n][2])) + r[n][3];
#pragma unroll
      for (int m = 0; m < 2; ++m)
        st16p(v4u{pk(rr[2 * m][0], rr[2 * m][1]), pk(rr[2 * m][2], rr[2 * m][3]), pk(rr[2 * m + 1][0], rr[2 * m + 1][1]), pk(rr[2 * m + 1][2], rr[2 * m + 1][3])}, ro,
              ovo + 64 * m, oso);
      __builtin_amdgcn_raw_buffer_store_b64(v2u{pk(rr[4][0], rr[4][1]), pk(rr[4][2], rr[4][3])}, ro, ov2, oso, 0);
      if (b == 0 && w == 0) flag_store(sws, tag, W_EPOCH);
    }
    STAMP(18);
    DUMP();
  } else {
    const u32 j = wv;
    __builtin_amdgcn_s_setprio(HPRIO);
    const u32 lane = lane_id();
    const u32 q = lane >> 4, i16 = lane & 15, tq = (lane >> 2) & 3, tp = lane & 3, vl = lane * 16;
    const u32 p1 = j * 256 + b;
    const bool isq = p1 < 600;
    const u32 pp = isq ? p1 : p1 - 600, pks = (pp * (isq ? 2185u : 6554u)) >> 16, pcb1 = pp - pks * (isq ? 30 : 10);
    u32 pv0, ps0, psn, pl0 = L_A + j * 4096;
    {
      const u32 jr = lane >> 3;
      const bool ex = pks >= 16;
      const u32 ra = 20 * (jr & 1) + 16 + (jr >> 1), rb = 20 * (jr >> 2) + (jr & 3);
      const u32 m1 = isq ? 3840 : 1280, m2 = isq ? 8 : ex ? 40 : 4, m3 = isq ? 32 : ex ? 160 : 40, m4 = (!isq && ex) ? 2560 : 0;
      pv0 = (isq ? jr : ex ? ra : rb) * m1 + (lane & 7) * 16;
      psn = m2 * m1;
      ps0 = (m3 * pks - m4) * m1 + pcb1 * 128;
    }
    OPS(ps0); OPS(psn); OPS(pl0); OPQ(pv0);
    if (lane < 32) lst<u32>(L_SYNC + lane * 4, 0u);
    GET_ARGS();
    {
      const v4u sp = mk_srd((const void*)(isq ? ka[1] : ka[3]), pks >= 20 ? 0u : isq ? DM * 3 * DM * 2 : DM * DM * 2);
#pragma unroll
      for (int n = 0; n < 4; ++n) dma16(pv0, sp, ps0 + n * psn, pl0 + n * 1024);
    }
    bar();
    u64 ep;
    asm volatile("s_load_dwordx2 %0, %1, 0x0" : "=s"(ep) : "s"(wsp));
    if (j == 3) {
      const v4u sp = mk_srd((const void*)ka[2], 32 * 400 * 2);
      dma16(vl, sp, rank * 1600, L_WPHI); dma16(vl, sp, rank * 1600 + 1024, L_WPHI + 1024);
    }
    u32 selv = i16 == q ? 0x3F803F80u : 0u;
    OPQ(selv);
    const s8 sel = as8(v4u{selv, selv, selv, selv});
    const u32 pi4 = ((tp & 1) << 1) | (tp >> 1);
    {
      u32 dv, cv;
      if (isq) {
        const u32 sec = (pcb1 * 26) >> 8, cn = (pcb1 - sec * 10) * 64 + lane, head = (cn * 3277) >> 16, d = cn - head * 20, hh = head & 1;
        const u32 par = pks & 1;
        const u32 tb = sec == 0 ? par * 2048 : sec * 3072 - 3072, xb = sec == 0 ? 4096 - 256 + par * 128 : 2048 - 256 - 128 + sec * 128;
        const u32 ds = W_W1 + (sec == 0 ? 102400 + (pks >> 1) * 5120 : pks * 5120);
        dv = (head >> 1) * 153600 + d * 16 + (d < 16 ? tb + hh * 1024 : xb + hh * 64) + ds;
        cv = i16 < 4 ? W_CS1 + pks * 7680 + pcb1 * 256 + i16 * 64 + q * 16 : OOB;
      } else {
        const u32 sgi = 4 * pcb1 + q, cc = (sgi * 52) >> 8, pos = sgi - cc * 5;
        const u32 o1 = ((pos & 2) + (i16 >> 3)) * 1024 + (8 * (pos & 1) + (i16 & 7)) * 16, o2 = 4096 + swap23(i16) * 16;
        dv = pks >= 20 ? OOB : cc * 102400 + (pos < 4 ? o1 : o2) + W_WO + pks * 5120;
        cv = OOB;
      }
      u32 rb = L_A + j * 4096 + tq * 128 + q * 32 + (isq ? tp : pi4) * 8;
      OPQ(dv); OPQ(rb); OPQ(cv);
      WAITV(0);
      v4u f[4];
#pragma unroll
      for (int n = 0; n < 4; ++n) f[n] = __builtin_bit_cast(v4u, cat(trd(rb + n * 1024), trd(rb + n * 1024 + 512)));
      f4 cs = z4;
#pragma unroll
      for (int n = 0; n < 4; ++n) { st16c(f[n], ws, dv + n * 256, 0); cs = mfma16(as8(f[n]), sel, cs); }
      st16c(__builtin_bit_cast(v4u, cs), ws, cv, 0);
      v4u sent = {SENT, SENT, SENT, SENT};
      OPQ(sent);
      st16c(sent, ws, vl, W_PART + (rank * 16 + team) * 8192 + j * 2048);
      st16c(sent, ws, vl, W_PART + (rank * 16 + team) * 8192 + j * 2048 + 1024);
      st16c(sent, ws, lane < 4 ? vl : OOB, W_RED + rank * 4096 + team * 256 + j * 64);
      SB();
    }
    STAMP(1);
    if (j == 3) {
      v2u pl[2][4];
      const int src = ((lane & ~3u) | tq) * 4;
#pragma unroll
      for (int h = 0; h < 2; ++h) {
        const u32 pb = L_WPHI + h * 800;
        pl[h][0] = __builtin_bit_cast(v2u, trd(pb + (4 * q + tq) * 40 + 8 * tp)); pl[h][1] = __builtin_bit_cast(v2u, trd(pb + (16 + tq) * 40 + 8 * tp));
        pl[h][2] = __builtin_bit_cast(v2u, trd(pb + (4 * q + tq) * 40 + 32)); pl[h][3] = __builtin_bit_cast(v2u, trd(pb + (16 + tq) * 40 + 32));
      }
      asm volatile("" ::: "memory");
#pragma unroll
      for (int h = 0; h < 2; ++h) {
        const u32 qh = q == (u32)h ? ~0u : 0u, rh = tp == (u32)h ? ~0u : 0u;
        lst<v4u>(L_WPHI + h * 1024 + vl, v4u{pl[h][0].x, pl[h][0].y, pl[h][1].x & qh, pl[h][1].y & qh});
        lst<v4u>(L_PH2 + h * 1024 + vl, v4u{(u32)__builtin_amdgcn_ds_bpermute(src, pl[h][2].x) & rh, (u32)__builtin_amdgcn_ds_bpermute(src, pl[h][2].y) & rh,
                                            (u32)__builtin_amdgcn_ds_bpermute(src, pl[h][3].x) & rh & qh, (u32)__builtin_amdgcn_ds_bpermute(src, pl[h][3].y) & rh & qh});
      }
    }
    GET_EPOCH();
    WAITV(0);
    lds_inc(S_PST);
    if (j == 0) { lds_wait<0>(S_PST); flag_store(sws, tag, W_FA + bid * FAS); }
    STAMP(9);
    u32 rdv[2], rdn[2], rcv[2], rvo[2], rs0[2], rsn[2];
    v4u rsp[2];
#pragma unroll
    for (int e = 0; e < 2; ++e) {
      const u32 piece = p1 + e * 1024;
      const bool isg = piece < 1080;
      const u32 p2 = isg ? piece : piece - 1080;
      const u32 pk_ = (p2 * (isg ? 1214u : 6554u)) >> 16, pcb = p2 - pk_ * (isg ? 54 : 10);
      const u32 mat = pcb >= 27 ? 1 : 0, rs = isg ? 3456 : 1280, u0 = 4 * (pcb - mat * 27);
      rsp[e] = mk_srd((const void*)(!isg ? ka[6] : mat ? ka[5] : ka[4]), piece < 1620 ? DM * FF * 2 : 0u);
      rs0[e] = pk_ * 32 * rs + u0 * 32; rsn[e] = 8 * rs; rvo[e] = (lane >> 3) * rs + (lane & 7) * 16;
      const u32 sgi = u0 + q, cc = (sgi * 52) >> 8, pos = sgi - cc * 5;
      const u32 o1 = ((pos & 2) + (i16 >> 3)) * 1024 + (8 * (pos & 1) + (i16 & 7)) * 16, o2 = 4096 + swap23(i16) * 16;
      const u32 dg = W_WGU + sgi * 40960 + i16 * 16 + pk_ * 2048 + mat * 256, dd = W_WD + cc * (54 * 5120) + (pos < 4 ? o1 : o2) + pk_ * 5120;
      rdv[e] = isg ? dg : piece < 1620 ? dd : OOB;
      rdn[e] = isg ? 512 : 256;
      rcv[e] = (isg && i16 < 4) ? W_CSP + (pk_ * 108 + u0 + i16) * 128 + mat * 64 + (((q & 1) << 1) | (q >> 1)) * 16 : OOB;
      OPQ(rdv[e]); OPQ(rcv[e]); OPQ(rvo[e]); OPS(rs0[e]); OPS(rsn[e]); OPS(rdn[e]);
      rsp[e].x = sgpr(rsp[e].x); rsp[e].y = sgpr(rsp[e].y); rsp[e].z = sgpr(rsp[e].z);
    }
    u32 c1v;
    { const u32 t = (j & 1) * 64 + lane, sec = (t * 1639) >> 16; c1v = t < 120 ? (sec * 600 + t) * 4 + (j >> 1) * (10 * 7680) : OOB; OPQ(c1v); }
    v4u rf[10];
    {
      v4u rt[2][4];
#pragma unroll
      for (int e = 0; e < 2; ++e) {
        const rsrc_t sp = mk_rsrc((const void*)(((u64)rsp[e].y << 32) | rsp[e].x), rsp[e].z);
#pragma unroll
        for (int n = 0; n < 4; ++n) rt[e][n] = __builtin_amdgcn_raw_buffer_load_b128(sp, rvo[e], rs0[e] + n * rsn[e], 0);
      }
      SB();
      const u32 rb = L_KV + j * 1024 + tq * 128 + q * 32 + pi4 * 8;
#pragma unroll
      for (int e = 0; e < 2; ++e) {
        f4 cs = z4;
#pragma unroll
        for (int n = 0; n < 4; ++n) {
          lst<v4u>(L_KV + j * 1024 + vl, rt[e][n]);
          asm volatile("" ::: "memory");
          rf[5 * e + n] = __builtin_bit_cast(v4u, cat(trd(rb), trd(rb + 512)));
          asm volatile("" ::: "memory");
        }
#pragma unroll
        for (int n = 0; n < 4; ++n) cs = mfma16(as8(rf[5 * e + n]), sel, cs);
        rf[5 * e + 4] = __builtin_bit_cast(v4u, cs);
        OPQ(rf[5 * e + 4]);
      }
    }
    auto rst = [&](int k) LAM { if (k % 5 < 4) st16c(rf[k], ws, rdv[k / 5], (k % 5) * rdn[k / 5]); else st16c(rf[k], ws, rcv[k / 5], 0); };
    STAMP(23);
    __builtin_amdgcn_s_sleep(FAD);
    {
      u32 fv = (j * 64 + lane) * FAS;
#pragma unroll 1
      for (u32 s = 0; s < SPIN_CAP; ++s) {
        const v2u v = ld8c(ws, fv, W_FA);
        fv = (v.x == tag.x && v.y == tag.y) ? OOB : fv;
        if (wall(fv == OOB)) break;
        __builtin_amdgcn_s_sleep(FAR);
        asm volatile("" ::: "memory");
      }
    }
    WAITV(0);
    lds_inc(S_FA);
    lds_wait<0>(S_FA);
    STAMP(24);
    {
      u32 w1s = W_W1 + rank * 153600 + j * 5120, w1l = L_A + j * 5120;
      OPS(w1s); OPS(w1l);
      auto dd = [&](int m) LAM { dma5c(vl, sws, w1s + m * 20480, w1l + m * 20480); };
      u32 cv[10];
      dd(0); dd(1); rst(0);
      WAITV(6); lds_inc(S_L1);
      dd(2); rst(1); dd(3); rst(2);
      WAITV(13); lds_inc(S_L1 + 4);
      dd(4); rst(3); rst(4);
      WAITV(14); lds_inc(S_L1 + 8);
#pragma unroll
      for (int k = 0; k < 10; ++k) cv[k] = ld4c(ws, c1v, W_CS1 + k * 7680 + rank * 160);
      rst(5);
      WAITV(19); lds_inc(S_L1 + 12);
      lds_wait(S_XD);
      dd(5); rst(6);
      WAITV(19); lds_inc(S_L1 + 16);
      dd(6); rst(7);
      if (j < 2) dd(7);
      rst(8); rst(9);
      {
        const float c = (((uf(cv[0]) + uf(cv[1])) + (uf(cv[2]) + uf(cv[3]))) + ((uf(cv[4]) + uf(cv[5])) + (uf(cv[6]) + uf(cv[7])))) + (uf(cv[8]) + uf(cv[9]));
        lst<float>(L_CS1 + j * 256 + lane * 4, c);
      }
      lds_inc(S_CS);
      WAITV(0);
      lds_inc(S_RT);
      if (j == 3) { lds_wait<0>(S_RT); flag_store(sws, tag, W_FB + bid * 32); }
    }
    STAMP(5);
    STAMP(25);
    u32 wos = W_WO + hq * 102400;
    OPS(wos);
    v4u wf[11][5], gf[8][4];
    const u32 wso = W_WD + (hq * 54 + 2 * j) * 5120;
    const u32 gso = W_G + (team * 8 + th * 4) * 54 * 1024 + 2 * j * 1024;
    auto ldw1 = [&](int i, int t) LAM { wf[i][t] = ld16c(ws, vl + t * 1024, wso + ((i >> 1) * 8 + (i & 1)) * 5120); };
    auto ldg1 = [&](int i, int r) LAM { gf[i][r] = ld16c(ws, vl, gso + r * 54 * 1024 + ((i >> 1) * 8 + (i & 1)) * 1024); };
    STAMP(2);
    lds_wait<2>(S_KVD);
#pragma unroll
    for (int n = 0; n < 3; ++n) dma5c(vl, sws, wos + (8 + 4 * n + j) * 5120, L_WO + (8 + 4 * n + j) * 5120);
    WAITV(15);
    STAMP(3);
    kv_job<true>(ws, j + 4, W_PART + (rank * 16 + team) * 8192);
    STAMP(4);
    bar_nw();
    dma5c(vl, sws, wos + j * 5120, L_WO + j * 5120);
    dma5c(vl, sws, wos + (4 + j) * 5120, L_WO + (4 + j) * 5120);
    if (j == 3) {
      const u32 pvo = (lane >> 5) * 8 * 8192 + (lane & 31) * 16, pso = W_PART + rank * 16 * 8192 + team * 512;
      v4u pv[8];
      __builtin_amdgcn_s_setprio(3);
      __builtin_amdgcn_s_sleep(HOPD);
#pragma unroll 1
      for (u32 s = 0; s < SPIN_CAP; ++s) {
#pragma unroll
        for (int n = 0; n < 8; ++n) pv[n] = ld16c(ws, pvo, pso + n * 8192);
        SB();
        u32 mx = 0;
#pragma unroll
        for (int n = 0; n < 8; ++n) mx = umax4(mx, pv[n]);
        if (wall(mx != SENT)) break;
        asm volatile("" ::: "memory");
      }
      float sm[4];
#pragma unroll
      for (int k = 0; k < 4; ++k) {
        const float a = ((uf(pv[0][k]) + uf(pv[1][k])) + (uf(pv[2][k]) + uf(pv[3][k]))) + ((uf(pv[4][k]) + uf(pv[5][k])) + (uf(pv[6][k]) + uf(pv[7][k])));
        auto r = __builtin_amdgcn_permlane32_swap(fu(a), fu(a), false, false);
        sm[k] = uf(r[0]) + uf(r[1]);
      }
      st8c(v2u{san(pk(sm[0], sm[1])), san(pk(sm[2], sm[3]))}, ws, lane < 32 ? lane * 8 : OOB, W_RED + rank * 4096 + team * 256);
      STAMP(6);
    } else if (j == 2) { poll256(ws, W_FB, 32, lane, tag); lds_inc(S_FB); }
    lds_wait(S_FB, 1);
    STAMP(22);
    sfor<0, 5 * NW1>([&](auto ic) LAM { ldw1(decltype(ic)::v / 5, decltype(ic)::v % 5); });
    SB();
    if (j == 3) {
      __builtin_amdgcn_s_sleep(HOPE);
      v4u kv[4];
#pragma unroll 1
      for (u32 s = 0; s < SPIN_CAP; ++s) {
#pragma unroll
        for (int n = 0; n < 4; ++n) kv[n] = ld16c(ws, vl, W_RED + rank * 4096 + n * 1024);
        SB();
        u32 mx = 0;
#pragma unroll
        for (int n = 0; n < 4; ++n) mx = umax4(mx, kv[n]);
        if (wall(mx != SENT)) break;
        asm volatile("" ::: "memory");
      }
#pragma unroll
      for (int n = 0; n < 4; ++n) lst<v4u>(L_KV + n * 1024 + vl, kv[n]);
    }
    WAITV(0);
    STAMP(7);
    bar();
    __builtin_amdgcn_s_setprio(HPRIO);
    STAMP(8);
    auto stage_u = [&](u32 hs) LAM {
      const u32 so = W_WGU + (16 * (hs >> 1) + rank) * 40960 + ((hs & 1) * 4 + j) * 5120, dst = L_HS + ((hs + 5) % 7) * HSLOT + j * 5120;
      if (hs >= 7) waitv<PACE>();
      if (!(ABL & 8) || hs < 7) dma5c(vl, sws, so, dst);
    };
    lds_wait(S_OP);
    stage_u(0); stage_u(1);
    {
      const u32 t = j * 64 + lane, u = 16 * (t >> 5) + rank;
      if (u < 108) {
        u32 cv[20];
#pragma unroll
        for (int k = 0; k < 20; ++k) cv[k] = ld4c(ws, u * 128 + (t & 31) * 4, W_CSP + k * 108 * 128);
        SB();
        float c = 0.f;
#pragma unroll
        for (int k = 0; k < 20; ++k) c += uf(cv[k]);
        lst<float>(L_CS3 + t * 4, c);
      }
    }
    u32 sdb[3] = {L_HS + j * 5120 + vl, L_HS + 3 * HSLOT + j * 5120 + vl, L_HS + 6 * HSLOT + j * 5120 + vl};
    OPQ(sdb[0]); OPQ(sdb[1]); OPQ(sdb[2]);
    auto spill = [&](int i) LAM {
#pragma unroll
      for (int t = 0; t < 5; ++t) lst<v4u>(sdb[((i + 1) % 7) / 3] + (((i + 1) % 7) % 3) * HSLOT + t * 1024, wf[i][t]);
      SB();
    };
    auto ldw = [&](auto lo, auto hi) LAM {
      sfor<decltype(lo)::v * 5, decltype(hi)::v * 5>([&](auto ic) LAM { if (decltype(ic)::v % 5 == 0) waitv<PACE>(); ldw1(decltype(ic)::v / 5, decltype(ic)::v % 5); });
      SB();
    };
    auto ldg = [&](int i) LAM {
      waitv<PACE>();
#pragma unroll
      for (int r = 0; r < 4; ++r) ldg1(i, r);
      SB();
    };
    STAMP(10);
    lds_wait(S_OL);
    ldw(IC<NW1>{}, IC<NW2>{});
    lds_wait<2>(S_WOC);
    STAMP(11);
    auto landed = [&](int k) LAM { lds_inc(L_LAND + 4 * k); };
    stage_u(2); stage_u(3); stage_u(4);
    WAITV(0); landed(0);
    STAMP(13);
    stage_u(5); stage_u(6);
    WAITV(0); landed(1);
    lds_wait<2>(S_HL);
    ldw(IC<NW2>{}, IC<NW3>{});
    lds_wait<1>(L_CONS + 4);
    stage_u(7); stage_u(8); stage_u(9);
    WAITV(5); landed(2);
    stage_u(10);
    ldw(IC<NW3>{}, IC<9>{});
    waitv<5 * (9 - NW3)>(); landed(3);
    lds_wait<2>(L_CONS + 8);
    stage_u(11);
    if (u7) stage_u(12);
    ldw(IC<9>{}, IC<11>{});
    WAITV(10); landed(4);
    const u32 fvo = W_FS4 + ((team * 16 + 4 * j + (lane >> 4)) * 4 + 2 * th + ((lane >> 3) & 1)) * 1024;
    lds_wait<2>(L_CONS + 12);
    if (u7) stage_u(13);
    spill(4);
    WAITV(0); landed(5);
    STAMP(26);
    lds_wait<2>(L_CONS + 16);
    STAMP(27);
    v2u fl = ld8c(ws, fvo, 2 * 128);
    spill(5); spill(6);
    if (!wall(fl.x == tag.x && fl.y == tag.y)) poll_level(ws, fvo, tag, 2);
    asm volatile("" ::: "memory");
    ldg(0); ldg(1); ldg(2);
    lds_wait<2>(L_CONS + 20);
    STAMP(28);
    fl = ld8c(ws, fvo, 3 * 128);
    spill(7); spill(8); ldg(3);
    if (!wall(fl.x == tag.x && fl.y == tag.y)) poll_level(ws, fvo, tag, 3);
    asm volatile("" ::: "memory");
    STAMP(29);
    __builtin_amdgcn_s_setprio(P4PRIO);
    lds_wait<1>(L_CONS + 24);
    STAMP(16);
    fl = ld8c(ws, fvo, 4 * 128);
    spill(9); spill(10);
    lds_inc(S_SP);
    f4 da[4][5];
    sfor<0, 8>([&](auto ic) LAM {
      constexpr int i = decltype(ic)::v;
#pragma unroll
      for (int m = 0; m < 20; ++m) {
        const int r = m / 5, t = m % 5;
        da[r][t] = mfma16(as8(wf[i][t]), as8(gf[i][r]), i == 0 ? z4 : da[r][t]);
        if constexpr (i <= 3) { if (t == 4) ldg1(i + 4, r); }
        if constexpr (i >= 3 && i <= 6) { if (m >= 12 && m < 17) wf[i + 1][m - 12] = lld<v4u>(sdb[((i + 2) % 7) / 3] + (((i + 2) % 7) % 3) * HSLOT + (m - 12) * 1024); }
        if constexpr (i == 7) { if (m >= 6) lst<f4>(sdb[(((m - 6) / 5 + 5) % 7) / 3] + ((((m - 6) / 5 + 5) % 7) % 3) * HSLOT + ((m - 6) % 5) * 1024, da[(m - 6) / 5][(m - 6) % 5]); }
        SB();
      }
      if constexpr (i == 1) {
        if (!wall(fl.x == tag.x && fl.y == tag.y)) poll_level(ws, fvo, tag, 4);
        asm volatile("" ::: "memory");
      }
      SB();
    });
    STAMP(18);
#pragma unroll
    for (int m = 14; m < 20; ++m) lst<f4>(sdb[((m / 5 + 5) % 7) / 3] + (((m / 5 + 5) % 7) % 3) * HSLOT + (m % 5) * 1024, da[m / 5][m % 5]);
    lds_inc(S_HP);
    __builtin_amdgcn_s_setprio(0);
    lds_wait(S_HP);
    {
      const u32 pb = L_HS + ((j + 5) % 7) * HSLOT + vl;
      f4 r[5][4];
#pragma unroll
      for (int n = 0; n < 5; ++n)
#pragma unroll
        for (int k = 0; k < 4; ++k) r[n][k] = lld<f4>(pb + k * 5120 + n * 1024);
      const v4u h0 = lld<v4u>(L_HST + j * 2560 + vl), h1 = lld<v4u>(L_HST + j * 2560 + 1024 + vl);
      const v2u h2 = lld<v2u>(L_HST + j * 2560 + 2048 + lane * 8);
      const u32 hw[10] = {h0.x, h0.y, h0.z, h0.w, h1.x, h1.y, h1.z, h1.w, h2.x, h2.y};
#pragma unroll
      for (int n = 0; n < 5; ++n)
        lst<f4>(pb + n * 1024, ((r[n][0] + r[n][1]) + (r[n][2] + r[n][3])) + f4{blo(hw[2 * n]), bhi(hw[2 * n]), blo(hw[2 * n + 1]), bhi(hw[2 * n + 1])});
    }
    bar();
    DUMP();
  }
}

extern "C" void kernel_launch(void* const* d_in, const int* in_sizes, int n_in, void* d_out, int out_size, void* d_ws, size_t ws_size,
                              hipStream_t stream) {
  (void)hipFuncSetAttribute((const void*)fused_block, hipFuncAttributeMaxDynamicSharedMemorySize, L_BYTES);
  fused_block<<<256, 512, L_BYTES, stream>>>((const u16*)d_in[0], (const u16*)d_in[1], (const u16*)d_in[2], (const u16*)d_in[3],
                                             (const u16*)d_in[5], (const u16*)d_in[6], (const u16*)d_in[7], (u16*)d_out, (char*)d_ws);
}
```

```cpp
#include <hip/hip_runtime.h>
#include <hip/hip_bf16.h>

typedef unsigned short u16;
typedef unsigned int u32;
typedef unsigned long long u64;
typedef u32 v4u __attribute__((ext_vector_type(4)));
typedef u32 v2u __attribute__((ext_vector_type(2)));
typedef float f4 __attribute__((ext_vector_type(4)));
typedef float f2 __attribute__((ext_vector_type(2)));
typedef float f16v __attribute__((ext_vector_type(16)));
typedef short s4 __attribute__((ext_vector_type(4)));
typedef short s8 __attribute__((ext_vector_type(8)));
typedef __bf16 bf2 __attribute__((ext_vector_type(2)));
#define LAS __attribute__((address_space(3)))
#define CAS __attribute__((address_space(4)))
#if defined(__HIP_DEVICE_COMPILE__)
#define NOPK __attribute__((target("no-packed-fp32-ops")))
#else
#define NOPK
#endif
#define DEV __device__ __forceinline__ NOPK
#define SB() __builtin_amdgcn_sched_barrier(0)
#define OPQ(x) asm volatile("" : "+v"(x))
typedef __amdgpu_buffer_rsrc_t rsrc_t;

#define DBG 0
#define ROT ((rank + 3 * (team & 1)) % 10)
#define P1ST 0
#define HOPA0 28
#define HOPA1 56
#define HOPB 1
#define FAS 32
#define NRM1 3
#define NRM2 8
#define HOPA 2
#define NWPRE 2
#define HOPD 12
#define HOPE 4
#define FAD 0
#define FAR 4
#define PD2 14
#define KDEP 6
#define FBD 0
#define LVS 8
#define WD6 8
#define NW2 6
#define NW0 4
#define NW1 4
#define XD3 1
#define HPRIO 0
#define RDEP 4
#define CODEPF 1
#define CODE_BYTES 45056
#define HROT 0
#define OROT 0
#define P4PRIO 3
#define ABL 0
#define NW3 7
#define WLP 0
#define PACE 63
#define WS_OFF(n) (((n) / 2) & ~(size_t)0x1FFFFF)
#define XMAP 4

constexpr int SEQ = 2048, DM = 640, FF = 1728;
constexpr u32 W_EPOCH = 0;
constexpr u32 W_FA = 4096;
constexpr u32 W_FB = W_FA + 256 * 128;
constexpr u32 W_FS2 = W_FB + 256 * 32;
constexpr u32 W_FS3 = W_FS2 + 256 * 4 * 128;
constexpr u32 W_FS4 = W_FS3 + 256 * 4 * 128;
constexpr u32 W_RED = W_FS4 + 256 * 4 * 8 * 128;
constexpr u32 W_STAT = W_RED + 65536;
constexpr u32 W_CSP = W_STAT + 131072;
constexpr u32 W_CS1 = W_CSP + 20 * 108 * 128;
constexpr u32 W_PART = W_CS1 + 20 * 7680;
constexpr u32 W_O = W_PART + 16 * 16 * 8192;
constexpr u32 W_H = W_O + 16 * 8 * 20 * 1024;
constexpr u32 W_G = W_H + 16 * 4 * 40 * 1024;
constexpr u32 W_WGU = W_G + 16 * 8 * 54 * 1024;
constexpr u32 W_WD = W_WGU + 108 * 40 * 1024;
constexpr u32 W_WO = W_WD + 8 * 54 * 5 * 1024;
constexpr u32 W_W1 = W_WO + 8 * 20 * 5 * 1024;
constexpr u32 W_DBG = W_W1 + 16 * 153600;
constexpr u32 W_END = W_DBG + 256 * 8 * 32 * 4;
constexpr u32 L_WPHI = 0;
constexpr u32 L_CS3 = 3200;
constexpr u32 L_SYNC = 3072;
constexpr u32 L_CS1 = 2048;
constexpr u32 S_PST = L_SYNC, S_FA = L_SYNC + 4, S_XD = L_SYNC + 8, S_CS = L_SYNC + 12, S_KVD = L_SYNC + 16, S_WOC = L_SYNC + 20, S_RT = L_SYNC + 24, S_FB = L_SYNC + 28, S_OP = L_SYNC + 52, S_OL = L_SYNC + 56, S_HL = L_SYNC + 60;
constexpr u32 S_L1 = L_SYNC + 32, S_SP = L_SYNC + 64, S_HP = L_SYNC + 68, L_LAND = L_SYNC + 80, L_CONS = L_SYNC + 100;
constexpr u32 L_KV = 4096;
constexpr u32 L_A = 8192;
constexpr u32 L_B = L_A + 102400;
constexpr u32 L_PH2 = L_B + 51200;
constexpr u32 L_IMG = L_A, L_WO = L_A;
constexpr u32 L_HS = L_A, HSLOT = 20480;
constexpr u32 L_HST = L_HS + 7 * HSLOT;
constexpr u32 L_BYTES = 163840;
constexpr u32 OOB = 0x80000000u;
constexpr u32 SENT = 0xFFFFFFFFu;
constexpr u32 SPIN_CAP = 1u << 18;

DEV v4u mk_srd(const void* p, u32 bytes) { return v4u{(u32)(size_t)p, (u32)((size_t)p >> 32), bytes, 0x00020000u}; }
DEV rsrc_t mk_rsrc(const void* p, u32 bytes) { return __builtin_amdgcn_make_buffer_rsrc((void*)p, 0, bytes, 0x00020000); }
DEV void dma16(u32 voff, v4u srd, u32 soff, u32 lds) {
  asm volatile("s_mov_b32 m0, %2\n\ts_nop 0\n\tbuffer_load_dwordx4 %0, %1, %3 offen lds" ::"v"(voff), "s"(srd), "s"(lds), "s"(soff) : "memory");
}
DEV void dma16c(u32 voff, v4u srd, u32 soff, u32 lds) {
  asm volatile("s_mov_b32 m0, %2\n\ts_nop 0\n\tbuffer_load_dwordx4 %0, %1, %3 offen sc1 lds" ::"v"(voff), "s"(srd), "s"(lds), "s"(soff) : "memory");
}
DEV void dma_a(u32 voff, v4u srd, u32 sb, u32 sa, u32 lb, u32 la) {
  u32 t;
  asm volatile("s_add_u32 m0, %5, %6\n\ts_add_u32 %0, %3, %4\n\tbuffer_load_dwordx4 %1, %2, %0 offen lds"
               : "=&s"(t) : "v"(voff), "s"(srd), "s"(sb), "s"(sa), "s"(lb), "s"(la) : "memory", "scc");
}
DEV void dma_ac(u32 voff, v4u srd, u32 sb, u32 sa, u32 lb, u32 la) {
  u32 t;
  asm volatile("s_add_u32 m0, %5, %6\n\ts_add_u32 %0, %3, %4\n\tbuffer_load_dwordx4 %1, %2, %0 offen sc1 lds"
               : "=&s"(t) : "v"(voff), "s"(srd), "s"(sb), "s"(sa), "s"(lb), "s"(la) : "memory", "scc");
}
DEV void dma_i(u32 voff, v4u srd, u32 sb, u32 sa, u32 lb, u32 la) {
  u32 t;
  asm volatile("s_add_u32 m0, %5, %6\n\ts_add_u32 %0, %3, %4\n\tbuffer_load_dwordx4 %1, %2, %0 offen lds"
               : "=&s"(t) : "v"(voff), "s"(srd), "s"(sb), "i"(sa), "s"(lb), "i"(la) : "memory", "scc");
}
DEV void dma5c(u32 voff, v4u srd, u32 so, u32 lds) {
  u32 t;
  asm volatile("s_mov_b32 m0, %4\n\ts_add_u32 %0, %3, 0x1000\n\t"
               "buffer_load_dwordx4 %1, %2, %3 offen sc1 lds\n\t"
               "buffer_load_dwordx4 %1, %2, %3 offen offset:1024 sc1 lds\n\t"
               "buffer_load_dwordx4 %1, %2, %3 offen offset:2048 sc1 lds\n\t"
               "buffer_load_dwordx4 %1, %2, %3 offen offset:3072 sc1 lds\n\t"
               "s_add_u32 m0, %4, 0x1000\n\ts_nop 0\n\t"
               "buffer_load_dwordx4 %1, %2, %0 offen sc1 lds"
               : "=&s"(t) : "v"(voff), "s"(srd), "s"(so), "s"(lds) : "memory", "scc");
}
DEV u32 sgpr(u32 x) {
  u32 o;
  asm volatile("s_mov_b32 %0, %1" : "=s"(o) : "s"(__builtin_amdgcn_readfirstlane(x)));
  return o;
}
#define OPS(x) x = sgpr(x)
template <int N> DEV void waitv() { asm volatile("s_waitcnt vmcnt(%0)" ::"n"(N) : "memory"); }
#define WAITV(n) asm volatile("s_waitcnt vmcnt(" #n ")" ::: "memory")
DEV void bar() {
  asm volatile("" ::: "memory");
  __builtin_amdgcn_s_waitcnt(0xC07F);
  __builtin_amdgcn_s_barrier();
  asm volatile("" ::: "memory");
  SB();
}
DEV void bar_nw() {
  asm volatile("" ::: "memory");
  __builtin_amdgcn_s_barrier();
  asm volatile("" ::: "memory");
  SB();
}
DEV s4 trd(u32 a) { return __builtin_amdgcn_ds_read_tr16_b64_v4i16((LAS s4*)a); }
DEV s8 cat(s4 a, s4 b) { return __builtin_shufflevector(a, b, 0, 1, 2, 3, 4, 5, 6, 7); }
DEV s8 as8(v4u a) { return __builtin_bit_cast(s8, a); }
DEV s4 as4(v2u a) { return __builtin_bit_cast(s4, a); }
DEV f4 mfma16(s8 a, s8 b, f4 c) { return __builtin_amdgcn_mfma_f32_16x16x32_bf16(a, b, c, 0, 0, 0); }
DEV f4 mfma16h(s4 a, s4 b, f4 c) { return __builtin_amdgcn_mfma_f32_16x16x16bf16_1k(a, b, c, 0, 0, 0); }
DEV f16v mfma32(s8 a, s8 b, f16v c) { return __builtin_amdgcn_mfma_f32_32x32x16_bf16(a, b, c, 0, 0, 0); }
DEV u32 pk(float a, float b) { return __builtin_bit_cast(u32, __builtin_convertvector(f2{a, b}, bf2)); }
DEV float blo(u32 x) { return __builtin_bit_cast(float, x << 16); }
DEV float bhi(u32 x) { return __builtin_bit_cast(float, x & 0xFFFF0000u); }
DEV float dot2(u32 a, u32 b, float c) { return __builtin_amdgcn_fdot2_f32_bf16(__builtin_bit_cast(bf2, a), __builtin_bit_cast(bf2, b), c, false); }
DEV u32 fu(float x) { return __builtin_bit_cast(u32, x); }
DEV float uf(u32 x) { return __builtin_bit_cast(float, x); }
template <class T> DEV T lld(u32 a) { return *(LAS T*)a; }
template <class T> DEV void lst(u32 a, T v) { *(LAS T*)a = v; }
DEV v4u ld16c(rsrc_t r, u32 vo, u32 so) { return __builtin_amdgcn_raw_buffer_load_b128(r, vo, so, 16); }
DEV v2u ld8c(rsrc_t r, u32 vo, u32 so) { return __builtin_amdgcn_raw_buffer_load_b64(r, vo, so, 16); }
DEV u32 ld4c(rsrc_t r, u32 vo, u32 so) { return __builtin_amdgcn_raw_buffer_load_b32(r, vo, so, 16); }
DEV void st16c(v4u v, rsrc_t r, u32 vo, u32 so) {
  __builtin_amdgcn_raw_buffer_store_b128(v, r, vo, so, 16);
  asm volatile("s_nop 1" ::"v"(v) : "memory");
}
DEV void st16p(v4u v, rsrc_t r, u32 vo, u32 so) {
  __builtin_amdgcn_raw_buffer_store_b128(v, r, vo, so, 0);
  asm volatile("s_nop 1" ::"v"(v) : "memory");
}
DEV void st8c(v2u v, rsrc_t r, u32 vo, u32 so) { __builtin_amdgcn_raw_buffer_store_b64(v, r, vo, so, 16); }
DEV void st4c(u32 v, rsrc_t r, u32 vo, u32 so) { __builtin_amdgcn_raw_buffer_store_b32(v, r, vo, so, 16); }
DEV bool wall(bool c) { return __builtin_amdgcn_ballot_w64(!c) == 0ull; }
DEV u32 san(u32 x) { return x < 0xFFFFFFFEu ? x : 0xFFFFFFFEu; }
DEV u32 swap23(u32 k) { return (k & ~12u) | ((k & 4u) << 1) | ((k & 8u) >> 1); }

template <bool MAXOP, bool RCP> DEV void red4(float& a, float& b, float& c, float& d) {
  auto r1 = __builtin_amdgcn_permlane16_swap(fu(a), fu(b), false, false);
  auto r2 = __builtin_amdgcn_permlane16_swap(fu(c), fu(d), false, false);
  float ab = MAXOP ? __builtin_elementwise_maximum(uf(r1[0]), uf(r1[1])) : uf(r1[0]) + uf(r1[1]);
  float cd = MAXOP ? __builtin_elementwise_maximum(uf(r2[0]), uf(r2[1])) : uf(r2[0]) + uf(r2[1]);
  auto r3 = __builtin_amdgcn_permlane32_swap(fu(ab), fu(cd), false, false);
  float z = MAXOP ? __builtin_elementwise_maximum(uf(r3[0]), uf(r3[1])) : uf(r3[0]) + uf(r3[1]);
  if (RCP) z = __builtin_amdgcn_rcpf(z);
  auto g1 = __builtin_amdgcn_permlane32_swap(fu(z), fu(z), false, false);
  auto g2 = __builtin_amdgcn_permlane16_swap(g1[0], g1[0], false, false);
  auto g3 = __builtin_amdgcn_permlane16_swap(g1[1], g1[1], false, false);
  a = uf(g2[0]); b = uf(g2[1]); c = uf(g3[0]); d = uf(g3[1]);
}
DEV void red2(float& a, float& b) {
  auto r1 = __builtin_amdgcn_permlane16_swap(fu(a), fu(b), false, false);
  float ab = uf(r1[0]) + uf(r1[1]);
  auto r3 = __builtin_amdgcn_permlane32_swap(fu(ab), fu(ab), false, false);
  float z = uf(r3[0]) + uf(r3[1]);
  auto g2 = __builtin_amdgcn_permlane16_swap(fu(z), fu(z), false, false);
  a = uf(g2[0]); b = uf(g2[1]);
}

#define LAM __attribute__((always_inline)) NOPK
template <int V> struct IC { static constexpr int v = V; };
template <int A, int B, class F> DEV void sfor(F&& f) {
  if constexpr (A < B) { f(IC<A>{}); sfor<A + 1, B>(f); }
}
struct Phi { v2u pl[2], mi[2], ex; };
DEV float fmx(float a, float b) { return __builtin_elementwise_maximum(a, b); }
DEV float fmn(float a, float b) { return __builtin_elementwise_minimum(a, b); }
DEV void phi_mm(s8 b0, s8 b1, const s8 (&A)[4], f4 (&p)[3]) {
  const f4 z = {0.f, 0.f, 0.f, 0.f};
  p[2] = mfma16(A[2], b0, z);
  p[0] = mfma16(A[0], b0, z);
  p[1] = mfma16(A[1], b1, z);
  p[2] = mfma16(A[3], b1, p[2]);
}
DEV Phi phi_sm(const f4 (&pp)[3]) {
  const float p[2][5] = {{pp[0][0], pp[0][1], pp[0][2], pp[0][3], pp[2][0]}, {pp[1][0], pp[1][1], pp[1][2], pp[1][3], pp[2][1]}};
  float mx[2], nm[2];
#pragma unroll
  for (int h = 0; h < 2; ++h) {
    mx[h] = fmx(fmx(fmx(fmx(p[h][0], p[h][1]), p[h][2]), p[h][3]), p[h][4]);
    nm[h] = fmx(fmx(fmx(fmx(-p[h][0], -p[h][1]), -p[h][2]), -p[h][3]), -p[h][4]);
  }
  red4<true, false>(mx[0], nm[0], mx[1], nm[1]);
  float ep[2][5], em[2][5], sp[2], sm[2];
#pragma unroll
  for (int h = 0; h < 2; ++h) {
#pragma unroll
    for (int j = 0; j < 5; ++j) {
      ep[h][j] = __builtin_amdgcn_exp2f(p[h][j] - mx[h]);
      em[h][j] = __builtin_amdgcn_exp2f(-p[h][j] - nm[h]);
    }
    sp[h] = ((ep[h][0] + ep[h][1]) + (ep[h][2] + ep[h][3])) + ep[h][4];
    sm[h] = ((em[h][0] + em[h][1]) + (em[h][2] + em[h][3])) + em[h][4];
  }
  red4<false, true>(sp[0], sm[0], sp[1], sm[1]);
  Phi o;
#pragma unroll
  for (int h = 0; h < 2; ++h) {
    o.pl[h] = v2u{pk(ep[h][0] * sp[h], ep[h][1] * sp[h]), pk(ep[h][2] * sp[h], ep[h][3] * sp[h])};
    o.mi[h] = v2u{pk(em[h][0] * sm[h], em[h][1] * sm[h]), pk(em[h][2] * sm[h], em[h][3] * sm[h])};
    o.ex[h] = pk(ep[h][4] * sp[h], em[h][4] * sm[h]);
  }
  return o;
}
DEV v2u lnpk(f4 a, f4 cs, float nmean, float sc) {
  return v2u{pk(__builtin_fmaf(nmean, cs[0], a[0]) * sc, __builtin_fmaf(nmean, cs[1], a[1]) * sc),
             pk(__builtin_fmaf(nmean, cs[2], a[2]) * sc, __builtin_fmaf(nmean, cs[3], a[3]) * sc)};
}
DEV f4 lcs(u32 a) { return lld<f4>(a) + lld<f4>(a + 512); }
DEV u32 bsel(u32 m, u32 a, u32 b) { return (a & m) | (b & ~m); }
DEV void poll_flags(rsrc_t ws, u32 voff, bool active, v2u tag) {
#pragma unroll 1
  for (u32 s = 0; s < SPIN_CAP; ++s) {
    v2u v = tag;
    if (active) v = ld8c(ws, voff, 0);
    if (wall(v.x == tag.x && v.y == tag.y)) break;
    asm volatile("" ::: "memory");
  }
  asm volatile("" ::: "memory");
}
template <int SL = 1> DEV void lds_check(u32 lv, u32 a, u32 n) {
  u32 sv, cnt;
  asm volatile(
      "s_nop 0\n\tv_readfirstlane_b32 %0, %2\n\ts_cmp_ge_u32 %0, %4\n\ts_cbranch_scc1 2f\n\ts_mov_b32 %1, 0x400000\n"
      "1:\n\tv_mov_b32 %2, %3\n\tds_read_b32 %2, %2\n\ts_waitcnt lgkmcnt(0)\n\tv_readfirstlane_b32 %0, %2\n\ts_cmp_ge_u32 %0, %4\n\ts_cbranch_scc1 2f\n\t"
      "s_sleep %5\n\ts_sub_u32 %1, %1, 1\n\ts_cbranch_scc0 1b\n2:"
      : "=&s"(sv), "=&s"(cnt), "+v"(lv) : "s"(a), "s"(n), "n"(SL) : "memory", "scc");
}
template <int SL = 1> DEV void lds_wait(u32 a, u32 n = 4) { lds_check<SL>(0u, a, n); }
DEV void lds_inc(u32 a) {
  u32 t0, t1;
  asm volatile("v_mov_b32 %0, %2\n\tv_mov_b32 %1, 1\n\ts_mov_b64 exec, 1\n\tds_add_u32 %0, %1\n\ts_mov_b64 exec, -1" : "=&v"(t0), "=&v"(t1) : "s"(a) : "memory");
}
DEV void flag_store(v4u srd, v2u tag, u32 so) {
  v2u t;
  asm volatile("v_mov_b64 %0, %2\n\ts_mov_b64 exec, 1\n\tbuffer_store_dwordx2 %0, off, %1, %3 sc1\n\ts_mov_b64 exec, -1"
               : "=&v"(t) : "s"(srd), "s"(tag), "s"(so) : "memory");
}
DEV void poll_mask(rsrc_t ws, u32 fv, u32 base, v2u tag, v2u v) {
#pragma unroll 1
  for (u32 s = 0; s < SPIN_CAP; ++s) {
    fv = (v.x == tag.x && v.y == tag.y) ? OOB : fv;
    if (wall(fv == OOB)) break;
    asm volatile("" ::: "memory");
    v = ld8c(ws, fv, base);
  }
  asm volatile("" ::: "memory");
}
DEV void poll256(rsrc_t ws, u32 base, u32 stride, u32 lane, v2u tag) {
#pragma unroll 1
  for (u32 s = 0; s < SPIN_CAP; ++s) {
    v2u f[4];
    bool ok = true;
#pragma unroll
    for (int n = 0; n < 4; ++n) f[n] = ld8c(ws, lane * stride, base + n * 64 * stride);
#pragma unroll
    for (int n = 0; n < 4; ++n) ok = ok && f[n].x == tag.x && f[n].y == tag.y;
    if (wall(ok)) break;
    asm volatile("" ::: "memory");
  }
  asm volatile("" ::: "memory");
}
DEV u32 lane_id() {
  u32 l;
  asm volatile("v_mbcnt_lo_u32_b32 %0, -1, 0\n\tv_mbcnt_hi_u32_b32 %0, -1, %0" : "=v"(l));
  return l;
}
DEV u32 umax(u32 a, u32 b) { return a > b ? a : b; }
DEV u32 umax4(u32 m, v4u v) { return umax(umax(umax(umax(m, v.x), v.y), v.z), v.w); }

#define STAMP(i) do {} while (0)

DEV void poll_level(rsrc_t ws, u32 voff, v2u tag, u32 level) {
#pragma unroll 1
  for (u32 s = 0; s < SPIN_CAP; ++s) {
    const v2u v = ld8c(ws, voff, level * 128);
    voff = (v.x == tag.x && v.y == tag.y) ? OOB : voff;
    if (wall(voff == OOB)) break;
    __builtin_amdgcn_s_sleep(LVS);
    asm volatile("" ::: "memory");
  }
  asm volatile("" ::: "memory");
}

template <bool TWO> DEV void kv_job(rsrc_t ws, u32 jb, u32 so) {
  const u32 lane = lane_id();
  const u32 q = lane >> 4, tq = (lane >> 2) & 3, tp = lane & 3;
  const u32 a1c = !TWO ? jb * 32 : jb < 6 ? (jb - 4) * 32 : 128;
  const u32 b1c = !TWO ? 160 + (jb >> 1) * 32 : jb == 6 ? 160 : 224;
  const u32 a2c = jb < 6 ? a1c + 64 : a1c;
  const u32 b2c = jb == 6 ? 192 : b1c;
  const u32 rb = L_IMG + (2 * (q >> 1) + 16 * (q & 1) + 4 * tq) * 264 + 8 * tp;
  constexpr int NO = TWO ? 4 : 2;
  u32 ra[4] = {rb + a1c, rb + b1c, rb + a2c, rb + b2c};
  u32 mlo = (jb != 6 && (lane & 1)) ? ~0u : 0u, mhi = (jb == 6 || (lane & 1)) ? ~0u : 0u, vl = lane * 16;
#pragma unroll
  for (int n = 0; n < NO; ++n) OPQ(ra[n]);
  OPQ(vl);
  if (TWO) { OPQ(mlo); OPQ(mhi); }
  bar();
  s8 fr[4][NO];
#pragma unroll
  for (int ks = 0; ks < 4; ++ks)
#pragma unroll
    for (int n = 0; n < NO; ++n) fr[ks][n] = cat(trd(ra[n] + ks * 32 * 264), trd(ra[n] + ks * 32 * 264 + 264));
  SB();
  f4 d1 = {0.f, 0.f, 0.f, 0.f}, d2 = d1;
#pragma unroll
  for (int ks = 0; ks < 4; ++ks) {
    d1 = mfma16(fr[ks][0], fr[ks][1], d1);
    if (TWO) d2 = mfma16(fr[ks][NO - 2], fr[ks][NO - 1], d2);
  }
  if (TWO) d1 = f4{uf(bsel(mlo, fu(d2[0]), fu(d1[0]))), uf(bsel(mlo, fu(d2[1]), fu(d1[1]))), uf(bsel(mhi, fu(d2[2]), fu(d1[2]))), uf(bsel(mhi, fu(d2[3]), fu(d1[3])))};
  st16c(v4u{san(fu(d1[0])), san(fu(d1[1])), san(fu(d1[2])), san(fu(d1[3]))}, ws, vl, so + jb * 1024);
}

typedef u64 v8q __attribute__((ext_vector_type(8)));
__global__ __launch_bounds__(512, 2) NOPK void fused_block(const u16* x_, const u16* wqkv_, const u16* wphi_, const u16* wo_, const u16* wg_, const u16* wu_,
                                                           const u16* wd_, u16* out_, char* wsp_) {
  v8q ka;
  u64 kw;
  asm volatile("s_load_dwordx16 %0, %2, 0x0\n\ts_load_dwordx2 %1, %2, 0x40" : "=&s"(ka), "=&s"(kw) : "s"(__builtin_amdgcn_kernarg_segment_ptr()));
  u64 pc;
  asm volatile("s_getpc_b64 %0" : "=s"(pc));
  SB();
  STAMP(0);
  const u32 wv = __builtin_amdgcn_readfirstlane(threadIdx.x >> 6);
  const u32 b = blockIdx.x, xcd = b & 7, idx = b >> 3;
  const u32 team = (xcd >> 1) * 4 + (idx >> 3), rank = ((idx & 4) << 1) | ((xcd & 1) << 2) | (idx & 3);
  const u32 bid = team * 16 + rank, hq = rank & 7, th = rank >> 3;
  const bool u7 = rank < 12;
  const f4 z4 = {0.f, 0.f, 0.f, 0.f};
#define DUMP()
#define GET_ARGS()                                                                                         \
  SB();                                                                                                    \
  asm volatile("s_waitcnt lgkmcnt(0)" ::: "memory");                                                       \
  SB();                                                                                                    \
  asm volatile("" : "+s"(ka), "+s"(kw));                                                                   \
  char* const wsp = (char*)kw;                                                                             \
  const rsrc_t ws = mk_rsrc(wsp, W_END);                                                                   \
  const v4u sws = mk_srd(wsp, W_END);
#define GET_EPOCH()                                                                                        \
  asm volatile("s_waitcnt lgkmcnt(0)" ::: "memory");                                                       \
  SB();                                                                                                    \
  asm volatile("" : "+s"(ep));                                                                             \
  ep = (ep & ~15ull) + 16ull;                                                                              \
  const v2u tag = {(u32)ep, (u32)(ep >> 32)};

  if (wv >= 4) {
    const u32 w = wv - 4;
    const u32 lane = lane_id();
    const u32 q = lane >> 4, i16 = lane & 15;
    {
      const v4u sc = mk_srd((const void*)(pc & ~255ull), CODE_BYTES);
      if (w == 0) asm volatile("s_mov_b32 m0, %2\n\ts_nop 0\n\tbuffer_load_dword %0, %1, 0 offen lds" ::"v"(lane < 16 ? (lane * 32 + idx) * 128 : OOB), "s"(sc), "s"(L_KV) : "memory");
    }
    const u32 vx0 = (lane >> 3) * 1280 + (((lane & 7) ^ (lane >> 4)) << 4), vx1 = (vx0 ^ 64) + 8 * 1280;
    u32 xs0 = (team * 128 + w * 32) * 1280, xl0 = L_B + w * 12288;
    OPS(xs0); OPS(xl0);
    GET_ARGS();
    const u16* const x = (const u16*)ka[0];
    u16* const out = (u16*)ka[7];
    const v4u sx = mk_srd(x, SEQ * DM * 2);
    bar_nw();
    u64 ep;
    asm volatile("s_load_dwordx2 %0, %1, 0x0" : "=s"(ep) : "s"(wsp));
    auto stagex = [&](int c) LAM {
      dma_i(vx0, sx, xs0, c * 128, xl0, (c % 3) * 4096);
      dma_i(vx1, sx, xs0, c * 128, xl0, (c % 3) * 4096 + 1024);
      dma_i(vx0, sx, xs0, c * 128 + 16 * 1280, xl0, (c % 3) * 4096 + 2048);
      dma_i(vx1, sx, xs0, c * 128 + 16 * 1280, xl0, (c % 3) * 4096 + 3072);
    };
    stagex(0); stagex(1);
    stagex(2);
    v4u xa[2][20];
    float nm1[2], rs1[2];
    {
      v4u onesv = {0x3F803F80u, 0x3F803F80u, 0x3F803F80u, 0x3F803F80u};
      OPQ(onesv);
      const s8 ones = as8(onesv);
      f4 as[2] = {z4, z4}, ag[2] = {z4, z4};
      u32 ax0 = L_B + w * 12288 + i16 * 128 + ((q ^ ((i16 >> 1) & 7)) << 4), ax1 = ax0 ^ 64;
      OPQ(ax0); OPQ(ax1);
      sfor<0, 11>([&](auto cc_) LAM {
        constexpr int c = decltype(cc_)::v;
        if constexpr (c < 10) waitv<(c < 8 ? 8 : c == 8 ? 4 : 0)>();
        __builtin_amdgcn_s_waitcnt(0xC07F);
        SB();
        if constexpr (c < 10) {
#pragma unroll
          for (int t = 0; t < 2; ++t) { xa[t][2 * c] = lld<v4u>(ax0 + (c % 3) * 4096 + t * 2048); xa[t][2 * c + 1] = lld<v4u>(ax1 + (c % 3) * 4096 + t * 2048); }
        }
        if constexpr (c < 7) { __builtin_amdgcn_s_waitcnt(0xC07F); SB(); stagex(c + 3); }
        if constexpr (c == 10) lds_inc(S_XD);
        if constexpr (c >= 1) {
#pragma unroll
          for (int k = 2 * c - 2; k < 2 * c; ++k)
#pragma unroll
            for (int t = 0; t < 2; ++t) { as[t] = mfma16(ones, as8(xa[t][k]), as[t]); ag[t] = mfma16(as8(xa[t][k]), as8(xa[t][k]), ag[t]); }
        }
        SB();
      });
      STAMP(23);
      float g[2];
#pragma unroll
      for (int t = 0; t < 2; ++t) {
        u32 v = 0;
#pragma unroll
        for (int r = 0; r < 4; ++r) v |= fu(ag[t][r]) & (i16 == 4 * q + r ? ~0u : 0u);
        g[t] = uf(v);
      }
      red2(g[0], g[1]);
#pragma unroll
      for (int t = 0; t < 2; ++t) {
        nm1[t] = -as[t][0] * (1.0f / DM);
        rs1[t] = __builtin_amdgcn_rsqf(__builtin_fmaxf(__builtin_fmaf(-nm1[t], nm1[t], g[t] * (1.0f / DM)), 0.f) + 1e-5f);
        OPQ(nm1[t]); OPQ(rs1[t]);
      }
    }
    GET_EPOCH();
    __builtin_amdgcn_s_setprio(2);
    const float L2E = 1.4426950408889634f;
    {
      f4 acc[2][5] = {{z4, z4, z4, z4, z4}, {z4, z4, z4, z4, z4}};
      u32 bw[2] = {L_A + lane * 16, L_A + 51200 + lane * 16};
      OPQ(bw[0]); OPQ(bw[1]);
      constexpr int KD = KDEP;
      s8 kf[KD + 1];
      u32 lv;
      lds_wait(S_FA);
      { v2u tg = tag; OPQ(tg); st8c(tg, ws, lane_id() << 26, w == 0 ? W_FB + bid * 32 : OOB); }
      lds_wait(S_L1);
      STAMP(25);
#pragma unroll
      for (int f = 0; f < KD; ++f) kf[f] = lld<s8>(bw[0] + f * 1024);
      SB();
      sfor<0, 100>([&](auto gc) LAM {
        constexpr int g = decltype(gc)::v, gn = g + KD;
        if constexpr (gn < 100 && gn % 20 == 0) {
          lds_check(lv, S_L1 + 4 * (gn / 20), 4);

        }
        if constexpr (gn < 100) kf[gn % (KD + 1)] = lld<s8>(bw[gn / 50] + (gn % 50) * 1024);
        if constexpr (gn == 44) lds_inc(S_CS);
        if constexpr (gn % 20 == 14) lv = *(volatile LAS u32*)(gn < 80 ? S_L1 + 4 * (gn / 20 + 1) : S_CS);
        acc[0][g % 5] = mfma16(kf[g % (KD + 1)], as8(xa[0][g / 5]), acc[0][g % 5]);
        acc[1][g % 5] = mfma16(kf[g % (KD + 1)], as8(xa[1][g / 5]), acc[1][g % 5]);
        SB();
      });
      lds_inc(S_KVD);
      lds_check(lv, S_CS, 8);
      STAMP(2);
      const u32 cb = L_CS1 + 160 + q * 16;
      const u32 mq = q < 2 ? ~0u : 0u;
      f4 pp[2][3];
      {
        const f4 c0 = lcs(cb), c1 = lcs(cb + 80), c2 = lcs(L_CS1 + 224 + q * 80);
        const s8 A[4] = {lld<s8>(L_WPHI + lane * 16), lld<s8>(L_WPHI + 1024 + lane * 16), lld<s8>(L_PH2 + lane * 16), lld<s8>(L_PH2 + 1024 + lane * 16)};
#pragma unroll
        for (int t = 0; t < 2; ++t) {
          const float sk = rs1[t] * L2E;
          const v2u k0 = lnpk(acc[t][0], c0, nm1[t], sk), k1 = lnpk(acc[t][1], c1, nm1[t], sk), k2 = lnpk(acc[t][2], c2, nm1[t], uf(bsel(mq, fu(sk), fu(rs1[t]))));
          phi_mm(as8(v4u{k0.x, k0.y, k2.x, k2.y}), as8(v4u{k1.x, k1.y, k2.x, k2.y}), A, pp[t]);
          const u32 e0 = bsel(mq, 0x3F803F80u, k2.x), e1 = bsel(mq, 0x3F803F80u, k2.y), eb = L_IMG + (w * 32 + t * 16 + i16) * 264 + 224 + (q ^ 2) * 2;
          lst<u16>(eb, (u16)e0); lst<u16>(eb + 8, (u16)(e0 >> 16)); lst<u16>(eb + 16, (u16)e1); lst<u16>(eb + 24, (u16)(e1 >> 16));
          SB();
        }
      }
      {
        const f4 c3 = lcs(cb + 160), c4 = lcs(cb + 240);
#pragma unroll
        for (int t = 0; t < 2; ++t) {
          const u32 ib = L_IMG + (w * 32 + t * 16 + i16) * 264;
          lst<v2u>(ib + q * 8 + 160, lnpk(acc[t][3], c3, nm1[t], rs1[t]));
          lst<v2u>(ib + q * 8 + 192, lnpk(acc[t][4], c4, nm1[t], rs1[t]));
        }
      }
      SB();
#pragma unroll
      for (int t = 0; t < 2; ++t) {
        const Phi ph = phi_sm(pp[t]);
        const u32 ib = L_IMG + (w * 32 + t * 16 + i16) * 264 + q * 8;
        lst<v2u>(ib + 0, ph.pl[0]); lst<v2u>(ib + 32, ph.mi[0]); lst<v2u>(ib + 64, ph.pl[1]); lst<v2u>(ib + 96, ph.mi[1]);
        lst<v2u>(ib + 128, ph.ex);
      }
    }
    STAMP(3);
    kv_job<false>(ws, w, W_PART + (rank * 16 + team) * 8192);
    STAMP(4);
    bar_nw();
    {
      const u32 lane = lane_id(), q = lane >> 4, i16 = lane & 15;
      f4 qa[2][3] = {{z4, z4, z4}, {z4, z4, z4}};
      u32 bq = L_B + lane * 16, bq2 = L_B + 4096 + ((lane & 7) + 16 * q) * 16;
      OPQ(bq); OPQ(bq2);
      constexpr int QD = 4;
      s8 qf[QD + 1];
      auto ldq = [&](int f) LAM {
        const int t = f % 3, ks = f / 3;
        qf[f % (QD + 1)] = lld<s8>(t == 2 ? bq2 + (ks >> 1) * 5120 + (ks & 1) * 128 : bq + (ks >> 1) * 5120 + ((ks & 1) * 2 + t) * 1024);
      };
#pragma unroll
      for (int f = 0; f < QD; ++f) ldq(f);
      SB();
#pragma unroll
      for (int f = 0; f < 60; ++f) {
        if (f + QD < 60) ldq(f + QD);
        qa[0][f % 3] = mfma16(qf[f % (QD + 1)], as8(xa[0][f / 3]), qa[0][f % 3]);
        qa[1][f % 3] = mfma16(qf[f % (QD + 1)], as8(xa[1][f / 3]), qa[1][f % 3]);
        SB();
      }
      STAMP(5);
      const f4 c0 = lcs(L_CS1 + q * 16), c1 = lcs(L_CS1 + 80 + q * 16), c2 = lcs(L_CS1 + 64 + (q & 1) * 80);
      const s8 A[4] = {lld<s8>(L_WPHI + lane * 16), lld<s8>(L_WPHI + 1024 + lane * 16), lld<s8>(L_PH2 + lane * 16), lld<s8>(L_PH2 + 1024 + lane * 16)};
      f4 pp[2][3];
      Phi pq[2];
#pragma unroll
      for (int t = 0; t < 2; ++t) {
        const float sk = rs1[t] * L2E;
        const v2u k0 = lnpk(qa[t][0], c0, nm1[t], sk), k1 = lnpk(qa[t][1], c1, nm1[t], sk), k2 = lnpk(qa[t][2], c2, nm1[t], sk);
        phi_mm(as8(v4u{k0.x, k0.y, k2.x, k2.y}), as8(v4u{k1.x, k1.y, k2.x, k2.y}), A, pp[t]);
      }
      v4u b0[2], b1[2], bx[2];
#pragma unroll
      for (int t = 0; t < 2; ++t) {
        pq[t] = phi_sm(pp[t]);
        b0[t] = v4u{pq[t].pl[0].x, pq[t].pl[0].y, pq[t].mi[0].x, pq[t].mi[0].y};
        b1[t] = v4u{pq[t].pl[1].x, pq[t].pl[1].y, pq[t].mi[1].x, pq[t].mi[1].y};
        bx[t] = v4u{pq[t].ex.x, pq[t].ex.y, 0u, 0u};
        OPQ(b0[t]); OPQ(b1[t]); OPQ(bx[t]);
      }
      u32 mo = (lane & 1) ? ~0u : 0u, ve = i16 * 16 + q * 4;
      OPQ(mo); OPQ(ve);
      STAMP(7);
      bar_nw();
      STAMP(8);
      v2u it[8];
#pragma unroll
      for (int n = 0; n < 8; ++n) it[n] = lld<v2u>(L_KV + n * 512 + lane * 8);
      const s8 ae = as8(v4u{it[4].x & ~mo, it[4].y & ~mo, it[5].x & ~mo, it[5].y & ~mo}), ao = as8(v4u{it[4].x & mo, it[4].y & mo, it[5].x & mo, it[5].y & mo});
      const s8 ax = as8(v4u{it[7].x & ~mo, it[7].y & mo, 0u, 0u});
      f4 nx[2], n0[2], n1[2];
#pragma unroll
      for (int t = 0; t < 2; ++t) {
        nx[t] = mfma16(ae, as8(b0[t]), z4);
        nx[t] = mfma16(ao, as8(b1[t]), nx[t]);
        nx[t] = mfma16(ax, as8(bx[t]), nx[t]);
      }
#pragma unroll
      for (int t = 0; t < 2; ++t) {
        n0[t] = mfma16(as8(v4u{it[0].x, it[0].y, it[1].x, it[1].y}), as8(b0[t]), z4);
        n1[t] = mfma16(as8(v4u{it[2].x, it[2].y, it[3].x, it[3].y}), as8(b1[t]), z4);
        n0[t] = mfma16(as8(v4u{it[6].x, 0u, 0u, 0u}), as8(bx[t]), n0[t]);
        n1[t] = mfma16(as8(v4u{0u, it[6].y, 0u, 0u}), as8(bx[t]), n1[t]);
      }
#pragma unroll
      for (int t = 0; t < 2; ++t) {
        const float r0 = __builtin_amdgcn_rcpf(nx[t][2] + 1e-6f), r1 = __builtin_amdgcn_rcpf(nx[t][3] + 1e-6f);
        const u32 ob = W_O + (team * 8 + 2 * w + t) * 20480;
        st4c(pk(nx[t][0] * r0, nx[t][1] * r1), ws, ve, ob + 16384 + rank * 256);
        st16c(v4u{pk(n0[t][0] * r0, n0[t][1] * r0), pk(n0[t][2] * r0, n0[t][3] * r0), pk(n1[t][0] * r1, n1[t][1] * r1), pk(n1[t][2] * r1, n1[t][3] * r1)}, ws,
              lane * 16, ob + rank * 1024);
      }
    }
    {
      const u32 lane = lane_id(), q = lane >> 4, i16 = lane & 15;
      const u32 tk = th * 64 + w * 16 + i16;
      const u32 xo = tk * 1280 + q * 16, xso = team * 128 * 1280 + hq * 160;
      const rsrc_t rx = mk_rsrc(x, SEQ * DM * 2);
      const v4u xr0 = __builtin_amdgcn_raw_buffer_load_b128(rx, xo, xso, 0), xr1 = __builtin_amdgcn_raw_buffer_load_b128(rx, xo + 64, xso, 0);
      const v2u xr2 = __builtin_amdgcn_raw_buffer_load_b64(rx, tk * 1280 + 128 + q * 8, xso, 0);
      constexpr int OD = 6;
      s8 wf[OD][5];
      u32 bo[2] = {L_WO + lane * 16, L_WO + 51200 + lane * 16};
      OPQ(bo[0]); OPQ(bo[1]);
      const u32 oso = W_O + (team * 8 + th * 4 + w) * 20480;
      auto ldo1 = [&](int ks, int t) LAM { wf[ks % OD][t] = lld<s8>(bo[ks / 10] + (ks % 10) * 5120 + t * 1024); };
#pragma unroll
      for (int ks = 0; ks < OD; ++ks)
#pragma unroll
        for (int t = 0; t < 5; ++t) ldo1(ks, t);
      WAITV(3);
      flag_store(sws, tag, W_FS2 + (bid * 4 + w) * 128);
      lds_inc(S_OP);
      STAMP(9);
      f4 ha[5] = {f4{blo(xr0.x), bhi(xr0.x), blo(xr0.y), bhi(xr0.y)}, f4{blo(xr0.z), bhi(xr0.z), blo(xr0.w), bhi(xr0.w)}, f4{blo(xr1.x), bhi(xr1.x), blo(xr1.y), bhi(xr1.y)},
                  f4{blo(xr1.z), bhi(xr1.z), blo(xr1.w), bhi(xr1.w)}, f4{blo(xr2.x), bhi(xr2.x), blo(xr2.y), bhi(xr2.y)}};
#pragma unroll
      for (int t = 0; t < 5; ++t) OPQ(ha[t]);
      __builtin_amdgcn_s_waitcnt(0xC07F);
      __builtin_amdgcn_s_sleep(PD2);
      SB();
      poll_flags(ws, W_FS2 + ((team * 16 + lane) * 4 + th * 2 + (w >> 1)) * 128, lane < 16, tag);
      STAMP(10);
      v4u of[20];
      constexpr int PD = 12;
      auto ldof = [&](int ks) LAM { of[ks] = ld16c(ws, lane * 16 + (ks & 3) * 1024, oso + (ks >> 2) * 4096); };
#pragma unroll
      for (int ks = 0; ks < PD; ++ks) ldof(ks);
      SB();
#pragma unroll
      for (int ks = 0; ks < 20; ++ks) {
#pragma unroll
        for (int t = 0; t < 5; ++t) {
          ha[t] = mfma16(wf[ks % OD][t], as8(of[ks]), ha[t]);
          if (t == 0 && ks + PD < 20) ldof(ks + PD);
          if (t == 1 && ks + PD == 19) lds_inc(S_OL);
          if (ks + OD < 20) ldo1(ks + OD, t);
          SB();
        }
      }
      STAMP(11);
      v4u hc[2];
#pragma unroll
      for (int m = 0; m < 2; ++m)
        hc[m] = v4u{pk(ha[2 * m][0], ha[2 * m][1]), pk(ha[2 * m][2], ha[2 * m][3]), pk(ha[2 * m + 1][0], ha[2 * m + 1][1]), pk(ha[2 * m + 1][2], ha[2 * m + 1][3])};
      const v2u ht = {pk(ha[4][0], ha[4][1]), pk(ha[4][2], ha[4][3])};
      const u32 hb = W_H + (team * 4 + th * 2 + (w >> 1)) * 40960 + hq * 5120;
      const u32 t32 = (w & 1) * 16 + i16;
      st16c(hc[0], ws, (q >> 1) * 1024 + (t32 + 32 * (q & 1)) * 16, hb);
      st16c(hc[1], ws, (q >> 1) * 1024 + (t32 + 32 * (q & 1)) * 16, hb + 2048);
      st8c(ht, ws, (t32 + 32 * (q >> 1)) * 16 + 8 * (q & 1), hb + 4096);
      float a[2] = {0.f, 0.f}, c[2] = {0.f, 0.f};
#pragma unroll
      for (int m = 0; m < 2; ++m) {
#pragma unroll
        for (int j = 0; j < 4; ++j) { a[m] = dot2(hc[m][j], 0x3F803F80u, a[m]); c[m] = dot2(hc[m][j], hc[m][j], c[m]); }
        a[m] = dot2(ht[m], 0x3F803F80u, a[m]); c[m] = dot2(ht[m], ht[m], c[m]);
      }
      float a2 = a[0] + a[1], c2 = c[0] + c[1];
      red2(a2, c2);
      st8c(v2u{fu(a2), fu(c2)}, ws, q == 0 ? tk * 8 : OOB, W_STAT + (team * 8 + hq) * 1024);
      lst<v4u>(L_HST + w * 2560 + lane * 16, hc[0]);
      lst<v4u>(L_HST + w * 2560 + 1024 + lane * 16, hc[1]);
      lst<v2u>(L_HST + w * 2560 + 2048 + lane * 8, ht);
    }
    WAITV(0);
    flag_store(sws, tag, W_FS3 + (bid * 4 + w) * 128);
    lds_inc(S_WOC);
    STAMP(12);
    __builtin_amdgcn_s_sleep(PD2);
    const u32 lane3 = lane_id();
#define lane lane3
    poll_flags(ws, W_FS3 + ((team * 16 + (w >> 1) * 8 + (lane >> 1)) * 4 + 2 * (w & 1) + (lane & 1)) * 128, lane < 16, tag);
    STAMP(13);
    v4u wl[3][5];
    u32 wlv;
    auto ldwl = [&](int k) LAM {
      const int i = 11 + k / 5;
      if (k == 0) wlv = lane_id() * 16;
      wl[k / 5][k % 5] = ld16c(ws, ((i >= 12 && w == 3) ? OOB : wlv) + (k % 5) * 1024, W_WD + (hq * 54 + 2 * w + (i >> 1) * 8 + (i & 1)) * 5120);
    };
    {
      const u32 hh = lane >> 5, t32 = lane & 31;
      v2u sv[4];
#pragma unroll
      for (int n = 0; n < 4; ++n) sv[n] = ld8c(ws, (hh * 4 + n) * 1024 + t32 * 8, W_STAT + team * 8192 + w * 256);
      v4u hf[40];
      constexpr int HD = 12;
      const u32 hso = W_H + (team * 4 + w) * 40960;
      auto ldh = [&](int s) LAM { hf[s] = ld16c(ws, lane * 16 + (s & 3) * 1024, hso + (s >> 2) * 4096); };
#pragma unroll
      for (int s = 0; s < HD; ++s) ldh(s);
      SB();
      lds_wait(L_LAND, 4);
      u32 lb[3] = {L_HS + lane * 16, L_HS + 3 * HSLOT + lane * 16, L_HS + 6 * HSLOT + lane * 16};
      OPQ(lb[0]); OPQ(lb[1]); OPQ(lb[2]);
      auto ldw = [&](int n, int s) LAM -> s8 {
        const int hs = (2 * n + s / 20 + 5) % 7;
        return lld<s8>(lb[hs / 3] + (hs % 3) * HSLOT + (s % 20) * 1024);
      };
      auto ldcs = [&](int n) LAM -> f16v {
        const u32 csb = L_CS3 + (lane_id() & 32);
        const f4 g0 = lld<f4>(csb + n * 128), g1 = lld<f4>(csb + n * 128 + 16), u0 = lld<f4>(csb + n * 128 + 64), u1 = lld<f4>(csb + n * 128 + 80);
        return __builtin_shufflevector(__builtin_shufflevector(g0, g1, 0, 1, 2, 3, 4, 5, 6, 7), __builtin_shufflevector(u0, u1, 0, 1, 2, 3, 4, 5, 6, 7), 0, 1, 2, 3, 4,
                                       5, 6, 7, 8, 9, 10, 11, 12, 13, 14, 15);
      };
      f16v ac[3];
      ac[0] = ldcs(0); ac[1] = ldcs(1); ac[2] = ldcs(2);
      constexpr int RD = RDEP;
      s8 rg[RD];
#pragma unroll
      for (int f = 0; f < RD; ++f) rg[f] = ldw(f & 1, f >> 1);
      SB();
      float nmean, cK1, cK2, cR;
      {
        const float a = (uf(sv[0].x) + uf(sv[1].x)) + (uf(sv[2].x) + uf(sv[3].x)), c = (uf(sv[0].y) + uf(sv[1].y)) + (uf(sv[2].y) + uf(sv[3].y));
        auto r = __builtin_amdgcn_permlane32_swap(fu(a), fu(a), false, false);
        auto r2 = __builtin_amdgcn_permlane32_swap(fu(c), fu(c), false, false);
        nmean = -(uf(r[0]) + uf(r[1])) * (1.0f / DM);
        cR = __builtin_fmaxf(__builtin_fmaf(-nmean, nmean, (uf(r2[0]) + uf(r2[1])) * (1.0f / DM)), 0.f) + 1e-5f;
        const float rstd = __builtin_amdgcn_rsqf(cR), k1 = -2.0f * 0.7978845608028654f * 1.4426950408889634f;
        cK1 = k1 * rstd; cK2 = cK1 * 0.044715f * rstd * rstd;
      }
#pragma unroll
      for (int j = 0; j < 16; ++j) { ac[0][j] *= nmean; ac[1][j] *= nmean; }
      SB();
      u32 lv;
#pragma unroll
      for (int f = 0; f < 80; ++f) {
        ac[f & 1] = mfma32(rg[f % RD], as8(hf[f >> 1]), ac[f & 1]);
        const int fn = f + RD;
        rg[f % RD] = fn < 80 ? ldw(fn & 1, fn >> 1) : ldw(2, fn - 80);
        if (f >= 8 && f < 24) ac[2][f - 8] *= nmean;
        if ((f & 1) == 0 && (f >> 1) + HD < 40) ldh((f >> 1) + HD);
        if (f == 2 * (39 - HD) + 1) lds_inc(S_HL);
        if (f == 80 - RD) lds_inc(L_CONS + 4);
        SB();
      }
      OPQ(cK1); OPQ(cK2); OPQ(cR);
      STAMP(14);
      const u32 gvo = (t32 >> 4) * 54 * 1024 + ((t32 & 15) + 16 * hh) * 16 + (rank & 1) * 512;
      const u32 gso = W_G + (team * 8 + 2 * w) * 54 * 1024 + (rank >> 1) * 1024;
      const u32 fso = W_FS4 + (bid * 4 + w) * 1024;
      float tv, au, pu, pr, ov[2];
      v4u gp;
      auto gstep = [&](const f16v& a, int e) LAM {
        const int j = e / 3, m = e % 3;
        if (m == 0 && j < 8) { tv = a[j] * a[j]; tv = __builtin_fmaf(tv, cK2, cK1); tv = tv * a[j]; au = a[j] * a[8 + j]; }
        if (m == 1) tv = __builtin_amdgcn_exp2f(tv);
        if ((m == 1 && j > 0) || e == 24) { ov[(j - 1) & 1] = pu * pr; if (((j - 1) & 1) == 1) gp[(j - 1) >> 1] = pk(ov[0], ov[1]); }
        if (m == 2) { pr = __builtin_amdgcn_rcpf(__builtin_fmaf(tv, cR, cR)); pu = au; }
      };
      auto pass = [&](auto nc) LAM {
        constexpr int n = decltype(nc)::v;
        sfor<0, 40>([&](auto sc) LAM {
          constexpr int s = decltype(sc)::v, g = (n - 2) * 40 + s, sn = s + RD;
          constexpr int G[6] = {1, 31, 61, 91, 122, 161}, I[4] = {26, 56, 86, 150};
          ac[n % 3] = mfma32(rg[s % RD], as8(hf[s]), ac[n % 3]);
          if constexpr (sn < 40) rg[s % RD] = ldw(n, sn);
          else if constexpr (n < 6) rg[s % RD] = ldw(n + 1, sn - 40);
          sfor<0, 6>([&](auto uc) LAM {
            constexpr int u = decltype(uc)::v;
            if constexpr (g >= G[u] && g <= G[u] + 24 && (!(ABL & 1) || g - G[u] < 6 || g - G[u] == 24)) gstep(ac[u % 3], g - G[u]);
            if constexpr (g == G[u] + 24) {
              if constexpr (u >= 2) { if (!(ABL & 2)) WAITV(0); flag_store(sws, tag, fso + u * 128); }
              st16c(gp, ws, gvo, gso + u * 8192);
            }
          });
          sfor<0, 4>([&](auto kc) LAM {
            constexpr int k = decltype(kc)::v;
            if constexpr (g == I[k] - 3) ac[k % 3] = ldcs(3 + k);
            if constexpr (g > I[k] && g <= I[k] + 4) {
#pragma unroll
              for (int j = 0; j < 4; ++j) ac[k % 3][4 * (g - I[k] - 1) + j] *= nmean;
            }
          });
          if constexpr (WLP && n == 6 && s >= 25) ldwl(s - 25);
          if constexpr (s == 40 - RD) lds_inc(L_CONS + 4 * n);
          if constexpr (s == 8) lv = *(volatile LAS u32*)(L_LAND + 4 * (n - 1));
          if constexpr (s == 14 && !(ABL & 4)) lds_check(lv, L_LAND + 4 * (n - 1), 4);
          SB();
        });
      };
      pass(IC<2>{}); STAMP(26); pass(IC<3>{}); STAMP(27); pass(IC<4>{}); STAMP(28); pass(IC<5>{}); STAMP(29);
      pass(IC<6>{});
      STAMP(15);
#pragma unroll
      for (int e = 0; e < 25; ++e) gstep(ac[0], e);
      st16c(gp, ws, u7 ? gvo : OOB, gso + 6 * 8192);
      if (WLP) WAITV(16); else WAITV(1);
      flag_store(sws, tag, fso + 6 * 128);
    }
#undef lane
    STAMP(30);
    {
      const u32 ln = lane_id(), vl = ln * 16;
      const u32 fv = W_FS4 + ((team * 16 + 4 * w + (ln >> 4)) * 4 + 2 * th + ((ln >> 3) & 1)) * 1024;
      const u32 gso = W_G + (team * 8 + th * 4) * 54 * 1024 + 2 * w * 1024;
      const u32 vz = w < 3 ? vl : OOB;
      u32 gsr[4];
#pragma unroll
      for (int r = 0; r < 4; ++r) gsr[r] = sgpr(gso + ((r + w + 1) & 3) * (54 * 1024));
      v4u wf[6][5], gf[6][4];
      auto ldg = [&](int i) LAM {
#pragma unroll
        for (int r = 0; r < 4; ++r) gf[i - 8][r] = ld16c(ws, i >= 12 ? vz : vl, gsr[r] + ((i >> 1) * 8 + (i & 1)) * 1024);
        SB();
      };
      v2u fl = ld8c(ws, fv, 5 * 128);
      if (!WLP) sfor<0, 5>([&](auto kc) LAM { ldwl(decltype(kc)::v); });
      if (WLP) WAITV(2); else WAITV(7);
      flag_store(sws, tag, W_FS4 + (bid * 4 + w) * 1024 + 7 * 128);
      STAMP(16);
      if (!wall(fl.x == tag.x && fl.y == tag.y)) poll_level(ws, fv, tag, 5);
      asm volatile("" ::: "memory");
      ldg(8); ldg(9);
      fl = ld8c(ws, fv, 6 * 128);
      if (!WLP) sfor<5, 15>([&](auto kc) LAM { ldwl(decltype(kc)::v); });
#pragma unroll
      for (int i = 11; i < 14; ++i)
#pragma unroll
        for (int t = 0; t < 5; ++t) wf[i - 8][t] = wl[i - 11][t];
      u32 sb = L_HS + 2 * HSLOT + w * 5120 + vl;
      OPQ(sb);
      lds_wait(S_SP);
#pragma unroll
      for (int i = 8; i < 10; ++i)
#pragma unroll
        for (int t = 0; t < 5; ++t) wf[i - 8][t] = lld<v4u>(sb + (i - 8) * HSLOT + t * 1024);
      if (!wall(fl.x == tag.x && fl.y == tag.y)) poll_level(ws, fv, tag, 6);
      asm volatile("" ::: "memory");
      STAMP(31);
      ldg(10);
      f4 da[4][5];
      auto step = [&](int i) LAM {
#pragma unroll
        for (int m = 0; m < 20; ++m) da[m / 5][m % 5] = mfma16(as8(wf[i - 8][m % 5]), as8(gf[i - 8][m / 5]), i == 8 ? z4 : da[m / 5][m % 5]);
        SB();
      };
      step(8);
      ldg(11);
      fl = ld8c(ws, fv, 7 * 128);
#pragma unroll
      for (int t = 0; t < 5; ++t) wf[2][t] = lld<v4u>(sb + 2 * HSLOT + t * 1024);
      SB();
      step(9);
      if (w < 3 && !wall(fl.x == tag.x && fl.y == tag.y)) poll_level(ws, fv, tag, 7);
      asm volatile("" ::: "memory");
      STAMP(1);
      ldg(12); ldg(13);
      step(10); step(11);
      STAMP(6);
      step(12);
#pragma unroll
      for (int m = 0; m < 21; ++m) {
        if (m < 20) da[m / 5][m % 5] = mfma16(as8(wf[5][m % 5]), as8(gf[5][m / 5]), da[m / 5][m % 5]);
        if (m >= 6) lst<f4>(sb + ((m - 6) / 5) * HSLOT + ((m - 6) % 5) * 1024, da[(m - 6) / 5][(m - 6) % 5]);
        SB();
      }
      u32 rb[4];
#pragma unroll
      for (int r = 0; r < 3; ++r) { rb[r] = L_HS + (2 + r) * HSLOT + ((w - r - 1) & 3) * 5120 + vl; OPQ(rb[r]); }
      rb[3] = L_HS + ((w + 5) % 7) * HSLOT + vl;
      OPQ(rb[3]);
      const rsrc_t ro = mk_rsrc(out, SEQ * DM * 2);
      const u32 oso = (team * 128 + th * 64 + w * 16) * 1280 + hq * 160;
      u32 ovo = (ln & 15) * 1280 + (ln >> 4) * 16, ov2 = (ln & 15) * 1280 + 128 + (ln >> 4) * 8;
      OPQ(ovo); OPQ(ov2);
      bar();
      STAMP(17);
      f4 r[5][4], rr[5];
#pragma unroll
      for (int n = 0; n < 5; ++n)
#pragma unroll
        for (int j = 0; j < 4; ++j) r[n][j] = lld<f4>(rb[j] + n * 1024);
      SB();
#pragma unroll
      for (int n = 0; n < 5; ++n) rr[n] = ((da[3][n] + r[n][0]) + (r[n][1] + r[n][2])) + r[n][3];
#pragma unroll
      for (int m = 0; m < 2; ++m)
        st16p(v4u{pk(rr[2 * m][0], rr[2 * m][1]), pk(rr[2 * m][2], rr[2 * m][3]), pk(rr[2 * m + 1][0], rr[2 * m + 1][1]), pk(rr[2 * m + 1][2], rr[2 * m + 1][3])}, ro,
              ovo + 64 * m, oso);
      __builtin_amdgcn_raw_buffer_store_b64(v2u{pk(rr[4][0], rr[4][1]), pk(rr[4][2], rr[4][3])}, ro, ov2, oso, 0);
      if (b == 0 && w == 0) flag_store(sws, tag, W_EPOCH);
    }
    STAMP(18);
    DUMP();
  } else {
    const u32 j = wv;
    __builtin_amdgcn_s_setprio(HPRIO);
    const u32 lane = lane_id();
    const u32 q = lane >> 4, i16 = lane & 15, tq = (lane >> 2) & 3, tp = lane & 3, vl = lane * 16;
    const u32 p1 = j * 256 + b;
    const bool isq = p1 < 600;
    const u32 pp = isq ? p1 : p1 - 600, pks = (pp * (isq ? 2185u : 6554u)) >> 16, pcb1 = pp - pks * (isq ? 30 : 10);
    u32 pv0, ps0, psn, pl0 = L_A + j * 4096;
    {
      const u32 jr = lane >> 3;
      const bool ex = pks >= 16;
      const u32 ra = 20 * (jr & 1) + 16 + (jr >> 1), rb = 20 * (jr >> 2) + (jr & 3);
      const u32 m1 = isq ? 3840 : 1280, m2 = isq ? 8 : ex ? 40 : 4, m3 = isq ? 32 : ex ? 160 : 40, m4 = (!isq && ex) ? 2560 : 0;
      pv0 = (isq ? jr : ex ? ra : rb) * m1 + (lane & 7) * 16;
      psn = m2 * m1;
      ps0 = (m3 * pks - m4) * m1 + pcb1 * 128;
    }
    OPS(ps0); OPS(psn); OPS(pl0); OPQ(pv0);
    if (lane < 32) lst<u32>(L_SYNC + lane * 4, 0u);
    GET_ARGS();
    {
      const v4u sp = mk_srd((const void*)(isq ? ka[1] : ka[3]), pks >= 20 ? 0u : isq ? DM * 3 * DM * 2 : DM * DM * 2);
#pragma unroll
      for (int n = 0; n < 4; ++n) dma16(pv0, sp, ps0 + n * psn, pl0 + n * 1024);
    }
    bar();
    u64 ep;
    asm volatile("s_load_dwordx2 %0, %1, 0x0" : "=s"(ep) : "s"(wsp));
    if (j == 3) {
      const v4u sp = mk_srd((const void*)ka[2], 32 * 400 * 2);
      dma16(vl, sp, rank * 1600, L_WPHI); dma16(vl, sp, rank * 1600 + 1024, L_WPHI + 1024);
    }
    u32 selv = i16 == q ? 0x3F803F80u : 0u;
    OPQ(selv);
    const s8 sel = as8(v4u{selv, selv, selv, selv});
    const u32 pi4 = ((tp & 1) << 1) | (tp >> 1);
    {
      u32 dv, cv;
      if (isq) {
        const u32 sec = (pcb1 * 26) >> 8, cn = (pcb1 - sec * 10) * 64 + lane, head = (cn * 3277) >> 16, d = cn - head * 20, hh = head & 1;
        const u32 par = pks & 1;
        const u32 tb = sec == 0 ? par * 2048 : sec * 3072 - 3072, xb = sec == 0 ? 4096 - 256 + par * 128 : 2048 - 256 - 128 + sec * 128;
        const u32 ds = W_W1 + (sec == 0 ? 102400 + (pks >> 1) * 5120 : pks * 5120);
        dv = (head >> 1) * 153600 + d * 16 + (d < 16 ? tb + hh * 1024 : xb + hh * 64) + ds;
        cv = i16 < 4 ? W_CS1 + pks * 7680 + pcb1 * 256 + i16 * 64 + q * 16 : OOB;
      } else {
        const u32 sgi = 4 * pcb1 + q, cc = (sgi * 52) >> 8, pos = sgi - cc * 5;
        const u32 o1 = ((pos & 2) + (i16 >> 3)) * 1024 + (8 * (pos & 1) + (i16 & 7)) * 16, o2 = 4096 + swap23(i16) * 16;
        dv = pks >= 20 ? OOB : cc * 102400 + (pos < 4 ? o1 : o2) + W_WO + pks * 5120;
        cv = OOB;
      }
      u32 rb = L_A + j * 4096 + tq * 128 + q * 32 + (isq ? tp : pi4) * 8;
      OPQ(dv); OPQ(rb); OPQ(cv);
      WAITV(0);
      v4u f[4];
#pragma unroll
      for (int n = 0; n < 4; ++n) f[n] = __builtin_bit_cast(v4u, cat(trd(rb + n * 1024), trd(rb + n * 1024 + 512)));
      f4 cs = z4;
#pragma unroll
      for (int n = 0; n < 4; ++n) { st16c(f[n], ws, dv + n * 256, 0); cs = mfma16(as8(f[n]), sel, cs); }
      st16c(__builtin_bit_cast(v4u, cs), ws, cv, 0);
      v4u sent = {SENT, SENT, SENT, SENT};
      OPQ(sent);
      st16c(sent, ws, vl, W_PART + (rank * 16 + team) * 8192 + j * 2048);
      st16c(sent, ws, vl, W_PART + (rank * 16 + team) * 8192 + j * 2048 + 1024);
      st16c(sent, ws, lane < 4 ? vl : OOB, W_RED + rank * 4096 + team * 256 + j * 64);
      SB();
    }
    STAMP(1);
    if (j == 3) {
      v2u pl[2][4];
      const int src = ((lane & ~3u) | tq) * 4;
#pragma unroll
      for (int h = 0; h < 2; ++h) {
        const u32 pb = L_WPHI + h * 800;
        pl[h][0] = __builtin_bit_cast(v2u, trd(pb + (4 * q + tq) * 40 + 8 * tp)); pl[h][1] = __builtin_bit_cast(v2u, trd(pb + (16 + tq) * 40 + 8 * tp));
        pl[h][2] = __builtin_bit_cast(v2u, trd(pb + (4 * q + tq) * 40 + 32)); pl[h][3] = __builtin_bit_cast(v2u, trd(pb + (16 + tq) * 40 + 32));
      }
      asm volatile("" ::: "memory");
#pragma unroll
      for (int h = 0; h < 2; ++h) {
        const u32 qh = q == (u32)h ? ~0u : 0u, rh = tp == (u32)h ? ~0u : 0u;
        lst<v4u>(L_WPHI + h * 1024 + vl, v4u{pl[h][0].x, pl[h][0].y, pl[h][1].x & qh, pl[h][1].y & qh});
        lst<v4u>(L_PH2 + h * 1024 + vl, v4u{(u32)__builtin_amdgcn_ds_bpermute(src, pl[h][2].x) & rh, (u32)__builtin_amdgcn_ds_bpermute(src, pl[h][2].y) & rh,
                                            (u32)__builtin_amdgcn_ds_bpermute(src, pl[h][3].x) & rh & qh, (u32)__builtin_amdgcn_ds_bpermute(src, pl[h][3].y) & rh & qh});
      }
    }
    GET_EPOCH();
    WAITV(0);
    lds_inc(S_PST);
    if (j == 0) { lds_wait<0>(S_PST); flag_store(sws, tag, W_FA + bid * FAS); }
    STAMP(9);
    u32 rdv[2], rdn[2], rcv[2], rvo[2], rs0[2], rsn[2];
    v4u rsp[2];
#pragma unroll
    for (int e = 0; e < 2; ++e) {
      const u32 piece = p1 + e * 1024;
      const bool isg = piece < 1080;
      const u32 p2 = isg ? piece : piece - 1080;
      const u32 pk_ = (p2 * (isg ? 1214u : 6554u)) >> 16, pcb = p2 - pk_ * (isg ? 54 : 10);
      const u32 mat = pcb >= 27 ? 1 : 0, rs = isg ? 3456 : 1280, u0 = 4 * (pcb - mat * 27);
      rsp[e] = mk_srd((const void*)(!isg ? ka[6] : mat ? ka[5] : ka[4]), piece < 1620 ? DM * FF * 2 : 0u);
      rs0[e] = pk_ * 32 * rs + u0 * 32; rsn[e] = 8 * rs; rvo[e] = (lane >> 3) * rs + (lane & 7) * 16;
      const u32 sgi = u0 + q, cc = (sgi * 52) >> 8, pos = sgi - cc * 5;
      const u32 o1 = ((pos & 2) + (i16 >> 3)) * 1024 + (8 * (pos & 1) + (i16 & 7)) * 16, o2 = 4096 + swap23(i16) * 16;
      const u32 dg = W_WGU + sgi * 40960 + i16 * 16 + pk_ * 2048 + mat * 256, dd = W_WD + cc * (54 * 5120) + (pos < 4 ? o1 : o2) + pk_ * 5120;
      rdv[e] = isg ? dg : piece < 1620 ? dd : OOB;
      rdn[e] = isg ? 512 : 256;
      rcv[e] = (isg && i16 < 4) ? W_CSP + (pk_ * 108 + u0 + i16) * 128 + mat * 64 + (((q & 1) << 1) | (q >> 1)) * 16 : OOB;
      OPQ(rdv[e]); OPQ(rcv[e]); OPQ(rvo[e]); OPS(rs0[e]); OPS(rsn[e]); OPS(rdn[e]);
      rsp[e].x = sgpr(rsp[e].x); rsp[e].y = sgpr(rsp[e].y); rsp[e].z = sgpr(rsp[e].z);
    }
    u32 c1v;
    { const u32 t = (j & 1) * 64 + lane, sec = (t * 1639) >> 16; c1v = t < 120 ? (sec * 600 + t) * 4 + (j >> 1) * (10 * 7680) : OOB; OPQ(c1v); }
    v4u rf[10];
    {
      v4u rt[2][4];
#pragma unroll
      for (int e = 0; e < 2; ++e) {
        const rsrc_t sp = mk_rsrc((const void*)(((u64)rsp[e].y << 32) | rsp[e].x), rsp[e].z);
#pragma unroll
        for (int n = 0; n < 4; ++n) rt[e][n] = __builtin_amdgcn_raw_buffer_load_b128(sp, rvo[e], rs0[e] + n * rsn[e], 0);
      }
      SB();
      const u32 rb = L_KV + j * 1024 + tq * 128 + q * 32 + pi4 * 8;
#pragma unroll
      for (int e = 0; e < 2; ++e) {
        f4 cs = z4;
#pragma unroll
        for (int n = 0; n < 4; ++n) {
          lst<v4u>(L_KV + j * 1024 + vl, rt[e][n]);
          asm volatile("" ::: "memory");
          rf[5 * e + n] = __builtin_bit_cast(v4u, cat(trd(rb), trd(rb + 512)));
          asm volatile("" ::: "memory");
        }
#pragma unroll
        for (int n = 0; n < 4; ++n) cs = mfma16(as8(rf[5 * e + n]), sel, cs);
        rf[5 * e + 4] = __builtin_bit_cast(v4u, cs);
        OPQ(rf[5 * e + 4]);
      }
    }
    auto rst = [&](int k) LAM { if (k % 5 < 4) st16c(rf[k], ws, rdv[k / 5], (k % 5) * rdn[k / 5]); else st16c(rf[k], ws, rcv[k / 5], 0); };
    STAMP(23);
    __builtin_amdgcn_s_sleep(FAD);
    {
      u32 fv = (j * 64 + lane) * FAS;
#pragma unroll 1
      for (u32 s = 0; s < SPIN_CAP; ++s) {
        const v2u v = ld8c(ws, fv, W_FA);
        fv = (v.x == tag.x && v.y == tag.y) ? OOB : fv;
        if (wall(fv == OOB)) break;
        __builtin_amdgcn_s_sleep(FAR);
        asm volatile("" ::: "memory");
      }
    }
    WAITV(0);
    lds_inc(S_FA);
    lds_wait<0>(S_FA);
    STAMP(24);
    {
      u32 w1s = W_W1 + rank * 153600 + j * 5120, w1l = L_A + j * 5120;
      OPS(w1s); OPS(w1l);
      auto dd = [&](int m) LAM { dma5c(vl, sws, w1s + m * 20480, w1l + m * 20480); };
      u32 cv[10];
      dd(0); dd(1); rst(0);
      WAITV(6); lds_inc(S_L1);
      dd(2); rst(1); dd(3); rst(2);
      WAITV(13); lds_inc(S_L1 + 4);
      dd(4); rst(3); rst(4);
      WAITV(14); lds_inc(S_L1 + 8);
#pragma unroll
      for (int k = 0; k < 10; ++k) cv[k] = ld4c(ws, c1v, W_CS1 + k * 7680 + rank * 160);
      rst(5);
      WAITV(19); lds_inc(S_L1 + 12);
      lds_wait(S_XD);
      dd(5); rst(6);
      WAITV(19); lds_inc(S_L1 + 16);
      dd(6); rst(7);
      if (j < 2) dd(7);
      rst(8); rst(9);
      {
        const float c = (((uf(cv[0]) + uf(cv[1])) + (uf(cv[2]) + uf(cv[3]))) + ((uf(cv[4]) + uf(cv[5])) + (uf(cv[6]) + uf(cv[7])))) + (uf(cv[8]) + uf(cv[9]));
        lst<float>(L_CS1 + j * 256 + lane * 4, c);
      }
      lds_inc(S_CS);
      WAITV(0);
      lds_inc(S_RT);
      if (j == 3) { lds_wait<0>(S_RT); flag_store(sws, tag, W_FB + bid * 32); }
    }
    STAMP(5);
    STAMP(25);
    u32 wos = W_WO + hq * 102400;
    OPS(wos);
    v4u wf[11][5], gf[8][4];
    const u32 wso = W_WD + (hq * 54 + 2 * j) * 5120;
    const u32 gso = W_G + (team * 8 + th * 4) * 54 * 1024 + 2 * j * 1024;
    auto ldw1 = [&](int i, int t) LAM { wf[i][t] = ld16c(ws, vl + t * 1024, wso + ((i >> 1) * 8 + (i & 1)) * 5120); };
    auto ldg1 = [&](int i, int r) LAM { gf[i][r] = ld16c(ws, vl, gso + r * 54 * 1024 + ((i >> 1) * 8 + (i & 1)) * 1024); };
    STAMP(2);
    lds_wait<2>(S_KVD);
#pragma unroll
    for (int n = 0; n < 3; ++n) dma5c(vl, sws, wos + (8 + 4 * n + j) * 5120, L_WO + (8 + 4 * n + j) * 5120);
    WAITV(15);
    STAMP(3);
    kv_job<true>(ws, j + 4, W_PART + (rank * 16 + team) * 8192);
    STAMP(4);
    bar_nw();
    dma5c(vl, sws, wos + j * 5120, L_WO + j * 5120);
    dma5c(vl, sws, wos + (4 + j) * 5120, L_WO + (4 + j) * 5120);
    if (j == 3) {
      const u32 pvo = (lane >> 5) * 8 * 8192 + (lane & 31) * 16, pso = W_PART + rank * 16 * 8192 + team * 512;
      v4u pv[8];
      __builtin_amdgcn_s_setprio(3);
      __builtin_amdgcn_s_sleep(HOPD);
#pragma unroll 1
      for (u32 s = 0; s < SPIN_CAP; ++s) {
#pragma unroll
        for (int n = 0; n < 8; ++n) pv[n] = ld16c(ws, pvo, pso + n * 8192);
        SB();
        u32 mx = 0;
#pragma unroll
        for (int n = 0; n < 8; ++n) mx = umax4(mx, pv[n]);
        if (wall(mx != SENT)) break;
        asm volatile("" ::: "memory");
      }
      float sm[4];
#pragma unroll
      for (int k = 0; k < 4; ++k) {
        const float a = ((uf(pv[0][k]) + uf(pv[1][k])) + (uf(pv[2][k]) + uf(pv[3][k]))) + ((uf(pv[4][k]) + uf(pv[5][k])) + (uf(pv[6][k]) + uf(pv[7][k])));
        auto r = __builtin_amdgcn_permlane32_swap(fu(a), fu(a), false, false);
        sm[k] = uf(r[0]) + uf(r[1]);
      }
      st8c(v2u{san(pk(sm[0], sm[1])), san(pk(sm[2], sm[3]))}, ws, lane < 32 ? lane * 8 : OOB, W_RED + rank * 4096 + team * 256);
      STAMP(6);
    } else if (j == 2) { poll256(ws, W_FB, 32, lane, tag); lds_inc(S_FB); }
    lds_wait(S_FB, 1);
    STAMP(22);
    sfor<0, 5 * NW1>([&](auto ic) LAM { ldw1(decltype(ic)::v / 5, decltype(ic)::v % 5); });
    SB();
    if (j == 3) {
      __builtin_amdgcn_s_sleep(HOPE);
      v4u kv[4];
#pragma unroll 1
      for (u32 s = 0; s < SPIN_CAP; ++s) {
#pragma unroll
        for (int n = 0; n < 4; ++n) kv[n] = ld16c(ws, vl, W_RED + rank * 4096 + n * 1024);
        SB();
        u32 mx = 0;
#pragma unroll
        for (int n = 0; n < 4; ++n) mx = umax4(mx, kv[n]);
        if (wall(mx != SENT)) break;
        asm volatile("" ::: "memory");
      }
#pragma unroll
      for (int n = 0; n < 4; ++n) lst<v4u>(L_KV + n * 1024 + vl, kv[n]);
    }
    WAITV(0);
    STAMP(7);
    bar();
    __builtin_amdgcn_s_setprio(HPRIO);
    STAMP(8);
    auto stage_u = [&](u32 hs) LAM {
      const u32 so = W_WGU + (16 * (hs >> 1) + rank) * 40960 + ((hs & 1) * 4 + j) * 5120, dst = L_HS + ((hs + 5) % 7) * HSLOT + j * 5120;
      if (hs >= 7) waitv<PACE>();
      if (!(ABL & 8) || hs < 7) dma5c(vl, sws, so, dst);
    };
    lds_wait(S_OP);
    stage_u(0); stage_u(1);
    {
      const u32 t = j * 64 + lane, u = 16 * (t >> 5) + rank;
      if (u < 108) {
        u32 cv[20];
#pragma unroll
        for (int k = 0; k < 20; ++k) cv[k] = ld4c(ws, u * 128 + (t & 31) * 4, W_CSP + k * 108 * 128);
        SB();
        float c = 0.f;
#pragma unroll
        for (int k = 0; k < 20; ++k) c += uf(cv[k]);
        lst<float>(L_CS3 + t * 4, c);
      }
    }
    u32 sdb[3] = {L_HS + j * 5120 + vl, L_HS + 3 * HSLOT + j * 5120 + vl, L_HS + 6 * HSLOT + j * 5120 + vl};
    OPQ(sdb[0]); OPQ(sdb[1]); OPQ(sdb[2]);
    auto spill = [&](int i) LAM {
#pragma unroll
      for (int t = 0; t < 5; ++t) lst<v4u>(sdb[((i + 1) % 7) / 3] + (((i + 1) % 7) % 3) * HSLOT + t * 1024, wf[i][t]);
      SB();
    };
    auto ldw = [&](auto lo, auto hi) LAM {
      sfor<decltype(lo)::v * 5, decltype(hi)::v * 5>([&](auto ic) LAM { if (decltype(ic)::v % 5 == 0) waitv<PACE>(); ldw1(decltype(ic)::v / 5, decltype(ic)::v % 5); });
      SB();
    };
    auto ldg = [&](int i) LAM {
      waitv<PACE>();
#pragma unroll
      for (int r = 0; r < 4; ++r) ldg1(i, r);
      SB();
    };
    STAMP(10);
    lds_wait(S_OL);
    ldw(IC<NW1>{}, IC<NW2>{});
    lds_wait<2>(S_WOC);
    STAMP(11);
    auto landed = [&](int k) LAM { lds_inc(L_LAND + 4 * k); };
    stage_u(2); stage_u(3); stage_u(4);
    WAITV(0); landed(0);
    STAMP(13);
    stage_u(5); stage_u(6);
    WAITV(0); landed(1);
    lds_wait<2>(S_HL);
    ldw(IC<NW2>{}, IC<NW3>{});
    lds_wait<1>(L_CONS + 4);
    stage_u(7); stage_u(8); stage_u(9);
    WAITV(5); landed(2);
    stage_u(10);
    ldw(IC<NW3>{}, IC<9>{});
    waitv<5 * (9 - NW3)>(); landed(3);
    lds_wait<2>(L_CONS + 8);
    stage_u(11);
    if (u7) stage_u(12);
    ldw(IC<9>{}, IC<11>{});
    WAITV(10); landed(4);
    const u32 fvo = W_FS4 + ((team * 16 + 4 * j + (lane >> 4)) * 4 + 2 * th + ((lane >> 3) & 1)) * 1024;
    lds_wait<2>(L_CONS + 12);
    if (u7) stage_u(13);
    spill(4);
    WAITV(0); landed(5);
    STAMP(26);
    lds_wait<2>(L_CONS + 16);
    STAMP(27);
    v2u fl = ld8c(ws, fvo, 2 * 128);
    spill(5); spill(6);
    if (!wall(fl.x == tag.x && fl.y == tag.y)) poll_level(ws, fvo, tag, 2);
    asm volatile("" ::: "memory");
    ldg(0); ldg(1); ldg(2);
    lds_wait<2>(L_CONS + 20);
    STAMP(28);
    fl = ld8c(ws, fvo, 3 * 128);
    spill(7); spill(8); ldg(3);
    if (!wall(fl.x == tag.x && fl.y == tag.y)) poll_level(ws, fvo, tag, 3);
    asm volatile("" ::: "memory");
    STAMP(29);
    __builtin_amdgcn_s_setprio(P4PRIO);
    lds_wait<1>(L_CONS + 24);
    STAMP(16);
    fl = ld8c(ws, fvo, 4 * 128);
    spill(9); spill(10);
    lds_inc(S_SP);
    STAMP(1);
    f4 da[4][5];
    sfor<0, 8>([&](auto ic) LAM {
      constexpr int i = decltype(ic)::v;
#pragma unroll
      for (int m = 0; m < 20; ++m) {
        const int r = m / 5, t = m % 5;
        da[r][t] = mfma16(as8(wf[i][t]), as8(gf[i][r]), i == 0 ? z4 : da[r][t]);
        if constexpr (i <= 3) { if (t == 4) ldg1(i + 4, r); }
        if constexpr (i >= 3 && i <= 6) { if (m >= 12 && m < 17) wf[i + 1][m - 12] = lld<v4u>(sdb[((i + 2) % 7) / 3] + (((i + 2) % 7) % 3) * HSLOT + (m - 12) * 1024); }
        if constexpr (i == 7) { if (m >= 6) lst<f4>(sdb[(((m - 6) / 5 + 5) % 7) / 3] + ((((m - 6) / 5 + 5) % 7) % 3) * HSLOT + ((m - 6) % 5) * 1024, da[(m - 6) / 5][(m - 6) % 5]); }
        SB();
      }
      if constexpr (i == 1) {
        if (!wall(fl.x == tag.x && fl.y == tag.y)) poll_level(ws, fvo, tag, 4);
        asm volatile("" ::: "memory");
      }
      STAMP(2 + i);
      SB();
    });
    STAMP(18);
#pragma unroll
    for (int m = 14; m < 20; ++m) lst<f4>(sdb[((m / 5 + 5) % 7) / 3] + (((m / 5 + 5) % 7) % 3) * HSLOT + (m % 5) * 1024, da[m / 5][m % 5]);
    lds_inc(S_HP);
    __builtin_amdgcn_s_setprio(0);
    lds_wait(S_HP);
    {
      const u32 pb = L_HS + ((j + 5) % 7) * HSLOT + vl;
      f4 r[5][4];
#pragma unroll
      for (int n = 0; n < 5; ++n)
#pragma unroll
        for (int k = 0; k < 4; ++k) r[n][k] = lld<f4>(pb + k * 5120 + n * 1024);
      const v4u h0 = lld<v4u>(L_HST + j * 2560 + vl), h1 = lld<v4u>(L_HST + j * 2560 + 1024 + vl);
      const v2u h2 = lld<v2u>(L_HST + j * 2560 + 2048 + lane * 8);
      const u32 hw[10] = {h0.x, h0.y, h0.z, h0.w, h1.x, h1.y, h1.z, h1.w, h2.x, h2.y};
#pragma unroll
      for (int n = 0; n < 5; ++n)
        lst<f4>(pb + n * 1024, ((r[n][0] + r[n][1]) + (r[n][2] + r[n][3])) + f4{blo(hw[2 * n]), bhi(hw[2 * n]), blo(hw[2 * n + 1]), bhi(hw[2 * n + 1])});
    }
    bar();
    DUMP();
  }
}

extern "C" void kernel_launch(void* const* d_in, const int* in_sizes, int n_in, void* d_out, int out_size, void* d_ws, size_t ws_size,
                              hipStream_t stream) {
  (void)hipFuncSetAttribute((const void*)fused_block, hipFuncAttributeMaxDynamicSharedMemorySize, L_BYTES);
  fused_block<<<256, 512, L_BYTES, stream>>>((const u16*)d_in[0], (const u16*)d_in[1], (const u16*)d_in[2], (const u16*)d_in[3],
                                             (const u16*)d_in[5], (const u16*)d_in[6], (const u16*)d_in[7], (u16*)d_out, (char*)d_ws + WS_OFF(ws_size));
}
```

```cpp
#include <hip/hip_runtime.h>
#include <hip/hip_bf16.h>

typedef unsigned short u16;
typedef unsigned int u32;
typedef unsigned long long u64;
typedef u32 v4u __attribute__((ext_vector_type(4)));
typedef u32 v2u __attribute__((ext_vector_type(2)));
typedef float f4 __attribute__((ext_vector_type(4)));
typedef float f2 __attribute__((ext_vector_type(2)));
typedef float f16v __attribute__((ext_vector_type(16)));
typedef short s4 __attribute__((ext_vector_type(4)));
typedef short s8 __attribute__((ext_vector_type(8)));
typedef __bf16 bf2 __attribute__((ext_vector_type(2)));
#define LAS __attribute__((address_space(3)))
#define CAS __attribute__((address_space(4)))
#if defined(__HIP_DEVICE_COMPILE__)
#define NOPK __attribute__((target("no-packed-fp32-ops")))
#else
#define NOPK
#endif
#define DEV __device__ __forceinline__ NOPK
#define SB() __builtin_amdgcn_sched_barrier(0)
#define OPQ(x) asm volatile("" : "+v"(x))
typedef __amdgpu_buffer_rsrc_t rsrc_t;

#define DBG 0
#define ROT ((rank + 3 * (team & 1)) % 10)
#define P1ST 0
#define HOPA0 28
#define HOPA1 56
#define HOPB 1
#define FAS 32
#define NRM1 3
#define NRM2 8
#define HOPA 2
#define NWPRE 2
#define HOPD 12
#define HOPE 4
#define FAD 0
#define FAR 4
#define PD2 14
#define KDEP 6
#define FBD 0
#define LVS 8
#define WD6 8
#define NW2 6
#define NW0 4
#define NW1 4
#define XD3 1
#define HPRIO 0
#define RDEP 4
#define CODEPF 1
#define CODE_BYTES 45056
#define HROT 0
#define OROT 0
#define P4PRIO 3
#define ABL 0
#define NW3 7
#define WLP 0
#define PACE 63
#define XT 1
#define RT 0
#define RLATE 0
#define RSP 2
#define NT 0
#define XMAP 4

constexpr int SEQ = 2048, DM = 640, FF = 1728;
constexpr u32 W_EPOCH = 0;
constexpr u32 W_FA = 4096;
constexpr u32 W_FB = W_FA + 256 * 128;
constexpr u32 W_FS2 = W_FB + 256 * 32;
constexpr u32 W_FS3 = W_FS2 + 256 * 4 * 128;
constexpr u32 W_FS4 = W_FS3 + 256 * 4 * 128;
constexpr u32 W_RED = W_FS4 + 256 * 4 * 8 * 128;
constexpr u32 W_STAT = W_RED + 65536;
constexpr u32 W_CSP = W_STAT + 131072;
constexpr u32 W_CS1 = W_CSP + 20 * 108 * 128;
constexpr u32 W_PART = W_CS1 + 20 * 7680;
constexpr u32 W_O = W_PART + 16 * 16 * 8192;
constexpr u32 W_H = W_O + 16 * 8 * 20 * 1024;
constexpr u32 W_G = W_H + 16 * 4 * 40 * 1024;
constexpr u32 W_WGU = W_G + 16 * 8 * 54 * 1024;
constexpr u32 W_WD = W_WGU + 108 * 40 * 1024;
constexpr u32 W_WO = W_WD + 8 * 54 * 5 * 1024;
constexpr u32 W_W1 = W_WO + 8 * 20 * 5 * 1024;
constexpr u32 W_DBG = W_W1 + 16 * 153600;
constexpr u32 W_END = W_DBG + 256 * 8 * 32 * 4;
constexpr u32 L_WPHI = 0;
constexpr u32 L_CS3 = 3200;
constexpr u32 L_SYNC = 3072;
constexpr u32 L_CS1 = 2048;
constexpr u32 S_PST = L_SYNC, S_FA = L_SYNC + 4, S_XD = L_SYNC + 8, S_CS = L_SYNC + 12, S_KVD = L_SYNC + 16, S_WOC = L_SYNC + 20, S_RT = L_SYNC + 24, S_FB = L_SYNC + 28, S_OP = L_SYNC + 52, S_OL = L_SYNC + 56, S_HL = L_SYNC + 60;
constexpr u32 S_L1 = L_SYNC + 32, S_SP = L_SYNC + 64, S_HP = L_SYNC + 68, L_LAND = L_SYNC + 80, L_CONS = L_SYNC + 100;
constexpr u32 L_KV = 4096;
constexpr u32 L_A = 8192;
constexpr u32 L_B = L_A + 102400;
constexpr u32 L_PH2 = L_B + 51200;
constexpr u32 L_IMG = L_A, L_WO = L_A;
constexpr u32 L_HS = L_A, HSLOT = 20480;
constexpr u32 L_HST = L_HS + 7 * HSLOT;
constexpr u32 L_BYTES = 163840;
constexpr u32 OOB = 0x80000000u;
constexpr u32 SENT = 0xFFFFFFFFu;
constexpr u32 SPIN_CAP = 1u << 18;

DEV v4u mk_srd(const void* p, u32 bytes) { return v4u{(u32)(size_t)p, (u32)((size_t)p >> 32), bytes, 0x00020000u}; }
DEV rsrc_t mk_rsrc(const void* p, u32 bytes) { return __builtin_amdgcn_make_buffer_rsrc((void*)p, 0, bytes, 0x00020000); }
DEV void dma16(u32 voff, v4u srd, u32 soff, u32 lds) {
  asm volatile("s_mov_b32 m0, %2\n\ts_nop 0\n\tbuffer_load_dwordx4 %0, %1, %3 offen lds" ::"v"(voff), "s"(srd), "s"(lds), "s"(soff) : "memory");
}
DEV void dma16c(u32 voff, v4u srd, u32 soff, u32 lds) {
  asm volatile("s_mov_b32 m0, %2\n\ts_nop 0\n\tbuffer_load_dwordx4 %0, %1, %3 offen sc1 lds" ::"v"(voff), "s"(srd), "s"(lds), "s"(soff) : "memory");
}
DEV void dma16n(u32 voff, v4u srd, u32 soff, u32 lds) {
  asm volatile("s_mov_b32 m0, %2\n\ts_nop 0\n\tbuffer_load_dwordx4 %0, %1, %3 offen nt lds" ::"v"(voff), "s"(srd), "s"(lds), "s"(soff) : "memory");
}
DEV void dma_a(u32 voff, v4u srd, u32 sb, u32 sa, u32 lb, u32 la) {
  u32 t;
  asm volatile("s_add_u32 m0, %5, %6\n\ts_add_u32 %0, %3, %4\n\tbuffer_load_dwordx4 %1, %2, %0 offen lds"
               : "=&s"(t) : "v"(voff), "s"(srd), "s"(sb), "s"(sa), "s"(lb), "s"(la) : "memory", "scc");
}
DEV void dma_ac(u32 voff, v4u srd, u32 sb, u32 sa, u32 lb, u32 la) {
  u32 t;
  asm volatile("s_add_u32 m0, %5, %6\n\ts_add_u32 %0, %3, %4\n\tbuffer_load_dwordx4 %1, %2, %0 offen sc1 lds"
               : "=&s"(t) : "v"(voff), "s"(srd), "s"(sb), "s"(sa), "s"(lb), "s"(la) : "memory", "scc");
}
DEV void dma_i(u32 voff, v4u srd, u32 sb, u32 sa, u32 lb, u32 la) {
  u32 t;
  asm volatile("s_add_u32 m0, %5, %6\n\ts_add_u32 %0, %3, %4\n\tbuffer_load_dwordx4 %1, %2, %0 offen lds"
               : "=&s"(t) : "v"(voff), "s"(srd), "s"(sb), "i"(sa), "s"(lb), "i"(la) : "memory", "scc");
}
DEV void touch(u32 voff, v4u srd, u32 soff, u32 lds) {
  asm volatile("s_mov_b32 m0, %2\n\ts_nop 0\n\tbuffer_load_dword %0, %1, %3 offen lds" ::"v"(voff), "s"(srd), "s"(lds), "s"(soff) : "memory");
}
DEV void dma5c(u32 voff, v4u srd, u32 so, u32 lds) {
  u32 t;
  asm volatile("s_mov_b32 m0, %4\n\ts_add_u32 %0, %3, 0x1000\n\t"
               "buffer_load_dwordx4 %1, %2, %3 offen sc1 lds\n\t"
               "buffer_load_dwordx4 %1, %2, %3 offen offset:1024 sc1 lds\n\t"
               "buffer_load_dwordx4 %1, %2, %3 offen offset:2048 sc1 lds\n\t"
               "buffer_load_dwordx4 %1, %2, %3 offen offset:3072 sc1 lds\n\t"
               "s_add_u32 m0, %4, 0x1000\n\ts_nop 0\n\t"
               "buffer_load_dwordx4 %1, %2, %0 offen sc1 lds"
               : "=&s"(t) : "v"(voff), "s"(srd), "s"(so), "s"(lds) : "memory", "scc");
}
DEV u32 sgpr(u32 x) {
  u32 o;
  asm volatile("s_mov_b32 %0, %1" : "=s"(o) : "s"(__builtin_amdgcn_readfirstlane(x)));
  return o;
}
#define OPS(x) x = sgpr(x)
template <int N> DEV void waitv() { asm volatile("s_waitcnt vmcnt(%0)" ::"n"(N) : "memory"); }
#define WAITV(n) asm volatile("s_waitcnt vmcnt(" #n ")" ::: "memory")
DEV void bar() {
  asm volatile("" ::: "memory");
  __builtin_amdgcn_s_waitcnt(0xC07F);
  __builtin_amdgcn_s_barrier();
  asm volatile("" ::: "memory");
  SB();
}
DEV void bar_nw() {
  asm volatile("" ::: "memory");
  __builtin_amdgcn_s_barrier();
  asm volatile("" ::: "memory");
  SB();
}
DEV s4 trd(u32 a) { return __builtin_amdgcn_ds_read_tr16_b64_v4i16((LAS s4*)a); }
DEV s8 cat(s4 a, s4 b) { return __builtin_shufflevector(a, b, 0, 1, 2, 3, 4, 5, 6, 7); }
DEV s8 as8(v4u a) { return __builtin_bit_cast(s8, a); }
DEV s4 as4(v2u a) { return __builtin_bit_cast(s4, a); }
DEV f4 mfma16(s8 a, s8 b, f4 c) { return __builtin_amdgcn_mfma_f32_16x16x32_bf16(a, b, c, 0, 0, 0); }
DEV f4 mfma16h(s4 a, s4 b, f4 c) { return __builtin_amdgcn_mfma_f32_16x16x16bf16_1k(a, b, c, 0, 0, 0); }
DEV f16v mfma32(s8 a, s8 b, f16v c) { return __builtin_amdgcn_mfma_f32_32x32x16_bf16(a, b, c, 0, 0, 0); }
DEV u32 pk(float a, float b) { return __builtin_bit_cast(u32, __builtin_convertvector(f2{a, b}, bf2)); }
DEV float blo(u32 x) { return __builtin_bit_cast(float, x << 16); }
DEV float bhi(u32 x) { return __builtin_bit_cast(float, x & 0xFFFF0000u); }
DEV float dot2(u32 a, u32 b, float c) { return __builtin_amdgcn_fdot2_f32_bf16(__builtin_bit_cast(bf2, a), __builtin_bit_cast(bf2, b), c, false); }
DEV u32 fu(float x) { return __builtin_bit_cast(u32, x); }
DEV float uf(u32 x) { return __builtin_bit_cast(float, x); }
template <class T> DEV T lld(u32 a) { return *(LAS T*)a; }
template <class T> DEV void lst(u32 a, T v) { *(LAS T*)a = v; }
DEV v4u ld16c(rsrc_t r, u32 vo, u32 so) { return __builtin_amdgcn_raw_buffer_load_b128(r, vo, so, 16); }
DEV v2u ld8c(rsrc_t r, u32 vo, u32 so) { return __builtin_amdgcn_raw_buffer_load_b64(r, vo, so, 16); }
DEV u32 ld4c(rsrc_t r, u32 vo, u32 so) { return __builtin_amdgcn_raw_buffer_load_b32(r, vo, so, 16); }
DEV void st16c(v4u v, rsrc_t r, u32 vo, u32 so) {
  __builtin_amdgcn_raw_buffer_store_b128(v, r, vo, so, 16);
  asm volatile("s_nop 1" ::"v"(v) : "memory");
}
DEV void st16p(v4u v, rsrc_t r, u32 vo, u32 so) {
  __builtin_amdgcn_raw_buffer_store_b128(v, r, vo, so, 0);
  asm volatile("s_nop 1" ::"v"(v) : "memory");
}
DEV void st8c(v2u v, rsrc_t r, u32 vo, u32 so) { __builtin_amdgcn_raw_buffer_store_b64(v, r, vo, so, 16); }
DEV void st4c(u32 v, rsrc_t r, u32 vo, u32 so) { __builtin_amdgcn_raw_buffer_store_b32(v, r, vo, so, 16); }
DEV bool wall(bool c) { return __builtin_amdgcn_ballot_w64(!c) == 0ull; }
DEV u32 san(u32 x) { return x < 0xFFFFFFFEu ? x : 0xFFFFFFFEu; }
DEV u32 swap23(u32 k) { return (k & ~12u) | ((k & 4u) << 1) | ((k & 8u) >> 1); }

template <bool MAXOP, bool RCP> DEV void red4(float& a, float& b, float& c, float& d) {
  auto r1 = __builtin_amdgcn_permlane16_swap(fu(a), fu(b), false, false);
  auto r2 = __builtin_amdgcn_permlane16_swap(fu(c), fu(d), false, false);
  float ab = MAXOP ? __builtin_elementwise_maximum(uf(r1[0]), uf(r1[1])) : uf(r1[0]) + uf(r1[1]);
  float cd = MAXOP ? __builtin_elementwise_maximum(uf(r2[0]), uf(r2[1])) : uf(r2[0]) + uf(r2[1]);
  auto r3 = __builtin_amdgcn_permlane32_swap(fu(ab), fu(cd), false, false);
  float z = MAXOP ? __builtin_elementwise_maximum(uf(r3[0]), uf(r3[1])) : uf(r3[0]) + uf(r3[1]);
  if (RCP) z = __builtin_amdgcn_rcpf(z);
  auto g1 = __builtin_amdgcn_permlane32_swap(fu(z), fu(z), false, false);
  auto g2 = __builtin_amdgcn_permlane16_swap(g1[0], g1[0], false, false);
  auto g3 = __builtin_amdgcn_permlane16_swap(g1[1], g1[1], false, false);
  a = uf(g2[0]); b = uf(g2[1]); c = uf(g3[0]); d = uf(g3[1]);
}
DEV void red2(float& a, float& b) {
  auto r1 = __builtin_amdgcn_permlane16_swap(fu(a), fu(b), false, false);
  float ab = uf(r1[0]) + uf(r1[1]);
  auto r3 = __builtin_amdgcn_permlane32_swap(fu(ab), fu(ab), false, false);
  float z = uf(r3[0]) + uf(r3[1]);
  auto g2 = __builtin_amdgcn_permlane16_swap(fu(z), fu(z), false, false);
  a = uf(g2[0]); b = uf(g2[1]);
}

#define LAM __attribute__((always_inline)) NOPK
template <int V> struct IC { static constexpr int v = V; };
template <int A, int B, class F> DEV void sfor(F&& f) {
  if constexpr (A < B) { f(IC<A>{}); sfor<A + 1, B>(f); }
}
struct Phi { v2u pl[2], mi[2], ex; };
DEV float fmx(float a, float b) { return __builtin_elementwise_maximum(a, b); }
DEV float fmn(float a, float b) { return __builtin_elementwise_minimum(a, b); }
DEV void phi_mm(s8 b0, s8 b1, const s8 (&A)[4], f4 (&p)[3]) {
  const f4 z = {0.f, 0.f, 0.f, 0.f};
  p[2] = mfma16(A[2], b0, z);
  p[0] = mfma16(A[0], b0, z);
  p[1] = mfma16(A[1], b1, z);
  p[2] = mfma16(A[3], b1, p[2]);
}
DEV Phi phi_sm(const f4 (&pp)[3]) {
  const float p[2][5] = {{pp[0][0], pp[0][1], pp[0][2], pp[0][3], pp[2][0]}, {pp[1][0], pp[1][1], pp[1][2], pp[1][3], pp[2][1]}};
  float mx[2], nm[2];
#pragma unroll
  for (int h = 0; h < 2; ++h) {
    mx[h] = fmx(fmx(fmx(fmx(p[h][0], p[h][1]), p[h][2]), p[h][3]), p[h][4]);
    nm[h] = fmx(fmx(fmx(fmx(-p[h][0], -p[h][1]), -p[h][2]), -p[h][3]), -p[h][4]);
  }
  red4<true, false>(mx[0], nm[0], mx[1], nm[1]);
  float ep[2][5], em[2][5], sp[2], sm[2];
#pragma unroll
  for (int h = 0; h < 2; ++h) {
#pragma unroll
    for (int j = 0; j < 5; ++j) {
      ep[h][j] = __builtin_amdgcn_exp2f(p[h][j] - mx[h]);
      em[h][j] = __builtin_amdgcn_exp2f(-p[h][j] - nm[h]);
    }
    sp[h] = ((ep[h][0] + ep[h][1]) + (ep[h][2] + ep[h][3])) + ep[h][4];
    sm[h] = ((em[h][0] + em[h][1]) + (em[h][2] + em[h][3])) + em[h][4];
  }
  red4<false, true>(sp[0], sm[0], sp[1], sm[1]);
  Phi o;
#pragma unroll
  for (int h = 0; h < 2; ++h) {
    o.pl[h] = v2u{pk(ep[h][0] * sp[h], ep[h][1] * sp[h]), pk(ep[h][2] * sp[h], ep[h][3] * sp[h])};
    o.mi[h] = v2u{pk(em[h][0] * sm[h], em[h][1] * sm[h]), pk(em[h][2] * sm[h], em[h][3] * sm[h])};
    o.ex[h] = pk(ep[h][4] * sp[h], em[h][4] * sm[h]);
  }
  return o;
}
DEV v2u lnpk(f4 a, f4 cs, float nmean, float sc) {
  return v2u{pk(__builtin_fmaf(nmean, cs[0], a[0]) * sc, __builtin_fmaf(nmean, cs[1], a[1]) * sc),
             pk(__builtin_fmaf(nmean, cs[2], a[2]) * sc, __builtin_fmaf(nmean, cs[3], a[3]) * sc)};
}
DEV f4 lcs(u32 a) { return lld<f4>(a) + lld<f4>(a + 512); }
DEV u32 bsel(u32 m, u32 a, u32 b) { return (a & m) | (b & ~m); }
DEV void poll_flags(rsrc_t ws, u32 voff, bool active, v2u tag) {
#pragma unroll 1
  for (u32 s = 0; s < SPIN_CAP; ++s) {
    v2u v = tag;
    if (active) v = ld8c(ws, voff, 0);
    if (wall(v.x == tag.x && v.y == tag.y)) break;
    asm volatile("" ::: "memory");
  }
  asm volatile("" ::: "memory");
}
template <int SL = 1> DEV void lds_check(u32 lv, u32 a, u32 n) {
  u32 sv, cnt;
  asm volatile(
      "s_nop 0\n\tv_readfirstlane_b32 %0, %2\n\ts_cmp_ge_u32 %0, %4\n\ts_cbranch_scc1 2f\n\ts_mov_b32 %1, 0x400000\n"
      "1:\n\tv_mov_b32 %2, %3\n\tds_read_b32 %2, %2\n\ts_waitcnt lgkmcnt(0)\n\tv_readfirstlane_b32 %0, %2\n\ts_cmp_ge_u32 %0, %4\n\ts_cbranch_scc1 2f\n\t"
      "s_sleep %5\n\ts_sub_u32 %1, %1, 1\n\ts_cbranch_scc0 1b\n2:"
      : "=&s"(sv), "=&s"(cnt), "+v"(lv) : "s"(a), "s"(n), "n"(SL) : "memory", "scc");
}
template <int SL = 1> DEV void lds_wait(u32 a, u32 n = 4) { lds_check<SL>(0u, a, n); }
DEV void lds_inc(u32 a) {
  u32 t0, t1;
  asm volatile("v_mov_b32 %0, %2\n\tv_mov_b32 %1, 1\n\ts_mov_b64 exec, 1\n\tds_add_u32 %0, %1\n\ts_mov_b64 exec, -1" : "=&v"(t0), "=&v"(t1) : "s"(a) : "memory");
}
DEV void flag_store(v4u srd, v2u tag, u32 so) {
  v2u t;
  asm volatile("v_mov_b64 %0, %2\n\ts_mov_b64 exec, 1\n\tbuffer_store_dwordx2 %0, off, %1, %3 sc1\n\ts_mov_b64 exec, -1"
               : "=&v"(t) : "s"(srd), "s"(tag), "s"(so) : "memory");
}
DEV void poll_mask(rsrc_t ws, u32 fv, u32 base, v2u tag, v2u v) {
#pragma unroll 1
  for (u32 s = 0; s < SPIN_CAP; ++s) {
    fv = (v.x == tag.x && v.y == tag.y) ? OOB : fv;
    if (wall(fv == OOB)) break;
    asm volatile("" ::: "memory");
    v = ld8c(ws, fv, base);
  }
  asm volatile("" ::: "memory");
}
DEV void poll256(rsrc_t ws, u32 base, u32 stride, u32 lane, v2u tag) {
#pragma unroll 1
  for (u32 s = 0; s < SPIN_CAP; ++s) {
    v2u f[4];
    bool ok = true;
#pragma unroll
    for (int n = 0; n < 4; ++n) f[n] = ld8c(ws, lane * stride, base + n * 64 * stride);
#pragma unroll
    for (int n = 0; n < 4; ++n) ok = ok && f[n].x == tag.x && f[n].y == tag.y;
    if (wall(ok)) break;
    asm volatile("" ::: "memory");
  }
  asm volatile("" ::: "memory");
}
DEV u32 lane_id() {
  u32 l;
  asm volatile("v_mbcnt_lo_u32_b32 %0, -1, 0\n\tv_mbcnt_hi_u32_b32 %0, -1, %0" : "=v"(l));
  return l;
}
DEV u32 umax(u32 a, u32 b) { return a > b ? a : b; }
DEV u32 umax4(u32 m, v4u v) { return umax(umax(umax(umax(m, v.x), v.y), v.z), v.w); }

#define STAMP(i) do {} while (0)

DEV void poll_level(rsrc_t ws, u32 voff, v2u tag, u32 level) {
#pragma unroll 1
  for (u32 s = 0; s < SPIN_CAP; ++s) {
    const v2u v = ld8c(ws, voff, level * 128);
    voff = (v.x == tag.x && v.y == tag.y) ? OOB : voff;
    if (wall(voff == OOB)) break;
    __builtin_amdgcn_s_sleep(LVS);
    asm volatile("" ::: "memory");
  }
  asm volatile("" ::: "memory");
}

template <bool TWO> DEV void kv_job(rsrc_t ws, u32 jb, u32 so) {
  const u32 lane = lane_id();
  const u32 q = lane >> 4, tq = (lane >> 2) & 3, tp = lane & 3;
  const u32 a1c = !TWO ? jb * 32 : jb < 6 ? (jb - 4) * 32 : 128;
  const u32 b1c = !TWO ? 160 + (jb >> 1) * 32 : jb == 6 ? 160 : 224;
  const u32 a2c = jb < 6 ? a1c + 64 : a1c;
  const u32 b2c = jb == 6 ? 192 : b1c;
  const u32 rb = L_IMG + (2 * (q >> 1) + 16 * (q & 1) + 4 * tq) * 264 + 8 * tp;
  constexpr int NO = TWO ? 4 : 2;
  u32 ra[4] = {rb + a1c, rb + b1c, rb + a2c, rb + b2c};
  u32 mlo = (jb != 6 && (lane & 1)) ? ~0u : 0u, mhi = (jb == 6 || (lane & 1)) ? ~0u : 0u, vl = lane * 16;
#pragma unroll
  for (int n = 0; n < NO; ++n) OPQ(ra[n]);
  OPQ(vl);
  if (TWO) { OPQ(mlo); OPQ(mhi); }
  bar();
  s8 fr[4][NO];
#pragma unroll
  for (int ks = 0; ks < 4; ++ks)
#pragma unroll
    for (int n = 0; n < NO; ++n) fr[ks][n] = cat(trd(ra[n] + ks * 32 * 264), trd(ra[n] + ks * 32 * 264 + 264));
  SB();
  f4 d1 = {0.f, 0.f, 0.f, 0.f}, d2 = d1;
#pragma unroll
  for (int ks = 0; ks < 4; ++ks) {
    d1 = mfma16(fr[ks][0], fr[ks][1], d1);
    if (TWO) d2 = mfma16(fr[ks][NO - 2], fr[ks][NO - 1], d2);
  }
  if (TWO) d1 = f4{uf(bsel(mlo, fu(d2[0]), fu(d1[0]))), uf(bsel(mlo, fu(d2[1]), fu(d1[1]))), uf(bsel(mhi, fu(d2[2]), fu(d1[2]))), uf(bsel(mhi, fu(d2[3]), fu(d1[3])))};
  st16c(v4u{san(fu(d1[0])), san(fu(d1[1])), san(fu(d1[2])), san(fu(d1[3]))}, ws, vl, so + jb * 1024);
}

typedef u64 v8q __attribute__((ext_vector_type(8)));
__global__ __launch_bounds__(512, 2) NOPK void fused_block(const u16* x_, const u16* wqkv_, const u16* wphi_, const u16* wo_, const u16* wg_, const u16* wu_,
                                                           const u16* wd_, u16* out_, char* wsp_) {
  v8q ka;
  u64 kw;
  asm volatile("s_load_dwordx16 %0, %2, 0x0\n\ts_load_dwordx2 %1, %2, 0x40" : "=&s"(ka), "=&s"(kw) : "s"(__builtin_amdgcn_kernarg_segment_ptr()));
  u64 pc;
  asm volatile("s_getpc_b64 %0" : "=s"(pc));
  SB();
  STAMP(0);
  const u32 wv = __builtin_amdgcn_readfirstlane(threadIdx.x >> 6);
  const u32 b = blockIdx.x, xcd = b & 7, idx = b >> 3;
  const u32 team = (xcd >> 1) * 4 + (idx >> 3), rank = ((idx & 4) << 1) | ((xcd & 1) << 2) | (idx & 3);
  const u32 bid = team * 16 + rank, hq = rank & 7, th = rank >> 3;
  const bool u7 = rank < 12;
  const f4 z4 = {0.f, 0.f, 0.f, 0.f};
#define DUMP()
#define GET_ARGS()                                                                                         \
  SB();                                                                                                    \
  asm volatile("s_waitcnt lgkmcnt(0)" ::: "memory");                                                       \
  SB();                                                                                                    \
  asm volatile("" : "+s"(ka), "+s"(kw));                                                                   \
  char* const wsp = (char*)kw;                                                                             \
  const rsrc_t ws = mk_rsrc(wsp, W_END);                                                                   \
  const v4u sws = mk_srd(wsp, W_END);
#define GET_EPOCH()                                                                                        \
  asm volatile("s_waitcnt lgkmcnt(0)" ::: "memory");                                                       \
  SB();                                                                                                    \
  asm volatile("" : "+s"(ep));                                                                             \
  ep = (ep & ~15ull) + 16ull;                                                                              \
  const v2u tag = {(u32)ep, (u32)(ep >> 32)};

  if (wv >= 4) {
    const u32 w = wv - 4;
    const u32 lane = lane_id();
    const u32 q = lane >> 4, i16 = lane & 15;
    {
      const v4u sc = mk_srd((const void*)(pc & ~255ull), CODE_BYTES);
      if (w == 0) asm volatile("s_mov_b32 m0, %2\n\ts_nop 0\n\tbuffer_load_dword %0, %1, 0 offen lds" ::"v"(lane < 16 ? (lane * 32 + idx) * 128 : OOB), "s"(sc), "s"(L_CS3) : "memory");
    }
    const u32 vx0 = (lane >> 3) * 1280 + (((lane & 7) ^ (lane >> 4)) << 4), vx1 = (vx0 ^ 64) + 8 * 1280;
    u32 xs0 = (team * 128 + w * 32) * 1280, xl0 = L_B + w * 12288;
    OPS(xs0); OPS(xl0);
    GET_ARGS();
    const u16* const x = (const u16*)ka[0];
    u16* const out = (u16*)ka[7];
    const v4u sx = mk_srd(x, SEQ * DM * 2);
    bar_nw();
    u64 ep;
    asm volatile("s_load_dwordx2 %0, %1, 0x0" : "=s"(ep) : "s"(wsp));
    auto stagex = [&](int c) LAM {
      dma_i(vx0, sx, xs0, c * 128, xl0, (c % 3) * 4096);
      dma_i(vx1, sx, xs0, c * 128, xl0, (c % 3) * 4096 + 1024);
      dma_i(vx0, sx, xs0, c * 128 + 16 * 1280, xl0, (c % 3) * 4096 + 2048);
      dma_i(vx1, sx, xs0, c * 128 + 16 * 1280, xl0, (c % 3) * 4096 + 3072);
    };
    touch(lane < 20 ? lane * 128 : OOB, sx, team * (128 * 1280) + (rank * 4 + w) * 2560, L_CS3);
    stagex(0); stagex(1);
    stagex(2);
    u32 tvo[2], ts0[2];
    v4u tsp[2];
#pragma unroll
    for (int e = 0; e < 2; ++e) {
      const u32 piece = w * 256 + b + e * 1024;
      const bool isg = piece < 1080;
      const u32 p2 = isg ? piece : piece - 1080;
      const u32 pk_ = (p2 * (isg ? 1214u : 6554u)) >> 16, pcb = p2 - pk_ * (isg ? 54 : 10), rs = isg ? 3456 : 1280;
      tsp[e] = mk_srd((const void*)(!isg ? ka[6] : pcb >= 27 ? ka[5] : ka[4]), piece < 1620 ? DM * FF * 2 : 0u);
      tsp[e].x = sgpr(tsp[e].x); tsp[e].y = sgpr(tsp[e].y); tsp[e].z = sgpr(tsp[e].z);
      ts0[e] = sgpr(pk_ * 32 * rs + (pcb >= 27 ? pcb - 27 : pcb) * 128);
      tvo[e] = (lane >> 1) * rs + (lane & 1) * 64;
      OPQ(tvo[e]);
    }
    v4u xa[2][20];
    float nm1[2], rs1[2];
    {
      v4u onesv = {0x3F803F80u, 0x3F803F80u, 0x3F803F80u, 0x3F803F80u};
      OPQ(onesv);
      const s8 ones = as8(onesv);
      f4 as[2] = {z4, z4}, ag[2] = {z4, z4};
      u32 ax0 = L_B + w * 12288 + i16 * 128 + ((q ^ ((i16 >> 1) & 7)) << 4), ax1 = ax0 ^ 64;
      OPQ(ax0); OPQ(ax1);
      sfor<0, 11>([&](auto cc_) LAM {
        constexpr int c = decltype(cc_)::v;
        if constexpr (c < 10) waitv<(c < 7 ? 8 : (c == 7 ? 8 : c == 8 ? 4 : 0) + (RT ? 2 : 0))>();
        __builtin_amdgcn_s_waitcnt(0xC07F);
        SB();
        if constexpr (c < 10) {
#pragma unroll
          for (int t = 0; t < 2; ++t) { xa[t][2 * c] = lld<v4u>(ax0 + (c % 3) * 4096 + t * 2048); xa[t][2 * c + 1] = lld<v4u>(ax1 + (c % 3) * 4096 + t * 2048); }
        }
        if constexpr (c < 7) { __builtin_amdgcn_s_waitcnt(0xC07F); SB(); stagex(c + 3); }
        if constexpr (RT && c == 6) {
#pragma unroll
          for (int e = 0; e < 2; ++e) touch(tvo[e], tsp[e], ts0[e], L_CS3);
        }
        if constexpr (c == 10) lds_inc(S_XD);
        if constexpr (c >= 1) {
#pragma unroll
          for (int k = 2 * c - 2; k < 2 * c; ++k)
#pragma unroll
            for (int t = 0; t < 2; ++t) { as[t] = mfma16(ones, as8(xa[t][k]), as[t]); ag[t] = mfma16(as8(xa[t][k]), as8(xa[t][k]), ag[t]); }
        }
        SB();
      });
      STAMP(23);
      float g[2];
#pragma unroll
      for (int t = 0; t < 2; ++t) {
        u32 v = 0;
#pragma unroll
        for (int r = 0; r < 4; ++r) v |= fu(ag[t][r]) & (i16 == 4 * q + r ? ~0u : 0u);
        g[t] = uf(v);
      }
      red2(g[0], g[1]);
#pragma unroll
      for (int t = 0; t < 2; ++t) {
        nm1[t] = -as[t][0] * (1.0f / DM);
        rs1[t] = __builtin_amdgcn_rsqf(__builtin_fmaxf(__builtin_fmaf(-nm1[t], nm1[t], g[t] * (1.0f / DM)), 0.f) + 1e-5f);
        OPQ(nm1[t]); OPQ(rs1[t]);
      }
    }
    GET_EPOCH();
    __builtin_amdgcn_s_setprio(2);
    const float L2E = 1.4426950408889634f;
    {
      f4 acc[2][5] = {{z4, z4, z4, z4, z4}, {z4, z4, z4, z4, z4}};
      u32 bw[2] = {L_A + lane * 16, L_A + 51200 + lane * 16};
      OPQ(bw[0]); OPQ(bw[1]);
      constexpr int KD = KDEP;
      s8 kf[KD + 1];
      u32 lv;
      lds_wait(S_FA);
      { v2u tg = tag; OPQ(tg); st8c(tg, ws, lane_id() << 26, w == 0 ? W_FB + bid * 32 : OOB); }
      lds_wait(S_L1);
      STAMP(25);
#pragma unroll
      for (int f = 0; f < KD; ++f) kf[f] = lld<s8>(bw[0] + f * 1024);
      SB();
      sfor<0, 100>([&](auto gc) LAM {
        constexpr int g = decltype(gc)::v, gn = g + KD;
        if constexpr (gn < 100 && gn % 20 == 0) {
          lds_check(lv, S_L1 + 4 * (gn / 20), 4);

        }
        if constexpr (gn < 100) kf[gn % (KD + 1)] = lld<s8>(bw[gn / 50] + (gn % 50) * 1024);
        if constexpr (gn == 44) lds_inc(S_CS);
        if constexpr (gn % 20 == 14) lv = *(volatile LAS u32*)(gn < 80 ? S_L1 + 4 * (gn / 20 + 1) : S_CS);
        acc[0][g % 5] = mfma16(kf[g % (KD + 1)], as8(xa[0][g / 5]), acc[0][g % 5]);
        acc[1][g % 5] = mfma16(kf[g % (KD + 1)], as8(xa[1][g / 5]), acc[1][g % 5]);
        SB();
      });
      lds_inc(S_KVD);
      lds_check(lv, S_CS, 8);
      STAMP(2);
      const u32 cb = L_CS1 + 160 + q * 16;
      const u32 mq = q < 2 ? ~0u : 0u;
      f4 pp[2][3];
      {
        const f4 c0 = lcs(cb), c1 = lcs(cb + 80), c2 = lcs(L_CS1 + 224 + q * 80);
        const s8 A[4] = {lld<s8>(L_WPHI + lane * 16), lld<s8>(L_WPHI + 1024 + lane * 16), lld<s8>(L_PH2 + lane * 16), lld<s8>(L_PH2 + 1024 + lane * 16)};
#pragma unroll
        for (int t = 0; t < 2; ++t) {
          const float sk = rs1[t] * L2E;
          const v2u k0 = lnpk(acc[t][0], c0, nm1[t], sk), k1 = lnpk(acc[t][1], c1, nm1[t], sk), k2 = lnpk(acc[t][2], c2, nm1[t], uf(bsel(mq, fu(sk), fu(rs1[t]))));
          phi_mm(as8(v4u{k0.x, k0.y, k2.x, k2.y}), as8(v4u{k1.x, k1.y, k2.x, k2.y}), A, pp[t]);
          const u32 e0 = bsel(mq, 0x3F803F80u, k2.x), e1 = bsel(mq, 0x3F803F80u, k2.y), eb = L_IMG + (w * 32 + t * 16 + i16) * 264 + 224 + (q ^ 2) * 2;
          lst<u16>(eb, (u16)e0); lst<u16>(eb + 8, (u16)(e0 >> 16)); lst<u16>(eb + 16, (u16)e1); lst<u16>(eb + 24, (u16)(e1 >> 16));
          SB();
        }
      }
      {
        const f4 c3 = lcs(cb + 160), c4 = lcs(cb + 240);
#pragma unroll
        for (int t = 0; t < 2; ++t) {
          const u32 ib = L_IMG + (w * 32 + t * 16 + i16) * 264;
          lst<v2u>(ib + q * 8 + 160, lnpk(acc[t][3], c3, nm1[t], rs1[t]));
          lst<v2u>(ib + q * 8 + 192, lnpk(acc[t][4], c4, nm1[t], rs1[t]));
        }
      }
      SB();
#pragma unroll
      for (int t = 0; t < 2; ++t) {
        const Phi ph = phi_sm(pp[t]);
        const u32 ib = L_IMG + (w * 32 + t * 16 + i16) * 264 + q * 8;
        lst<v2u>(ib + 0, ph.pl[0]); lst<v2u>(ib + 32, ph.mi[0]); lst<v2u>(ib + 64, ph.pl[1]); lst<v2u>(ib + 96, ph.mi[1]);
        lst<v2u>(ib + 128, ph.ex);
      }
    }
    STAMP(3);
    kv_job<false>(ws, w, W_PART + (rank * 16 + team) * 8192);
    STAMP(4);
    bar_nw();
    {
      const u32 lane = lane_id(), q = lane >> 4, i16 = lane & 15;
      f4 qa[2][3] = {{z4, z4, z4}, {z4, z4, z4}};
      u32 bq = L_B + lane * 16, bq2 = L_B + 4096 + ((lane & 7) + 16 * q) * 16;
      OPQ(bq); OPQ(bq2);
      constexpr int QD = 4;
      s8 qf[QD + 1];
      auto ldq = [&](int f) LAM {
        const int t = f % 3, ks = f / 3;
        qf[f % (QD + 1)] = lld<s8>(t == 2 ? bq2 + (ks >> 1) * 5120 + (ks & 1) * 128 : bq + (ks >> 1) * 5120 + ((ks & 1) * 2 + t) * 1024);
      };
#pragma unroll
      for (int f = 0; f < QD; ++f) ldq(f);
      SB();
#pragma unroll
      for (int f = 0; f < 60; ++f) {
        if (f + QD < 60) ldq(f + QD);
        qa[0][f % 3] = mfma16(qf[f % (QD + 1)], as8(xa[0][f / 3]), qa[0][f % 3]);
        qa[1][f % 3] = mfma16(qf[f % (QD + 1)], as8(xa[1][f / 3]), qa[1][f % 3]);
        SB();
      }
      STAMP(5);
      const f4 c0 = lcs(L_CS1 + q * 16), c1 = lcs(L_CS1 + 80 + q * 16), c2 = lcs(L_CS1 + 64 + (q & 1) * 80);
      const s8 A[4] = {lld<s8>(L_WPHI + lane * 16), lld<s8>(L_WPHI + 1024 + lane * 16), lld<s8>(L_PH2 + lane * 16), lld<s8>(L_PH2 + 1024 + lane * 16)};
      f4 pp[2][3];
      Phi pq[2];
#pragma unroll
      for (int t = 0; t < 2; ++t) {
        const float sk = rs1[t] * L2E;
        const v2u k0 = lnpk(qa[t][0], c0, nm1[t], sk), k1 = lnpk(qa[t][1], c1, nm1[t], sk), k2 = lnpk(qa[t][2], c2, nm1[t], sk);
        phi_mm(as8(v4u{k0.x, k0.y, k2.x, k2.y}), as8(v4u{k1.x, k1.y, k2.x, k2.y}), A, pp[t]);
      }
      v4u b0[2], b1[2], bx[2];
#pragma unroll
      for (int t = 0; t < 2; ++t) {
        pq[t] = phi_sm(pp[t]);
        b0[t] = v4u{pq[t].pl[0].x, pq[t].pl[0].y, pq[t].mi[0].x, pq[t].mi[0].y};
        b1[t] = v4u{pq[t].pl[1].x, pq[t].pl[1].y, pq[t].mi[1].x, pq[t].mi[1].y};
        bx[t] = v4u{pq[t].ex.x, pq[t].ex.y, 0u, 0u};
        OPQ(b0[t]); OPQ(b1[t]); OPQ(bx[t]);
      }
      u32 mo = (lane & 1) ? ~0u : 0u, ve = i16 * 16 + q * 4;
      OPQ(mo); OPQ(ve);
      STAMP(7);
      bar_nw();
      STAMP(8);
      v2u it[8];
#pragma unroll
      for (int n = 0; n < 8; ++n) it[n] = lld<v2u>(L_KV + n * 512 + lane * 8);
      const s8 ae = as8(v4u{it[4].x & ~mo, it[4].y & ~mo, it[5].x & ~mo, it[5].y & ~mo}), ao = as8(v4u{it[4].x & mo, it[4].y & mo, it[5].x & mo, it[5].y & mo});
      const s8 ax = as8(v4u{it[7].x & ~mo, it[7].y & mo, 0u, 0u});
      f4 nx[2], n0[2], n1[2];
#pragma unroll
      for (int t = 0; t < 2; ++t) {
        nx[t] = mfma16(ae, as8(b0[t]), z4);
        nx[t] = mfma16(ao, as8(b1[t]), nx[t]);
        nx[t] = mfma16(ax, as8(bx[t]), nx[t]);
      }
#pragma unroll
      for (int t = 0; t < 2; ++t) {
        n0[t] = mfma16(as8(v4u{it[0].x, it[0].y, it[1].x, it[1].y}), as8(b0[t]), z4);
        n1[t] = mfma16(as8(v4u{it[2].x, it[2].y, it[3].x, it[3].y}), as8(b1[t]), z4);
        n0[t] = mfma16(as8(v4u{it[6].x, 0u, 0u, 0u}), as8(bx[t]), n0[t]);
        n1[t] = mfma16(as8(v4u{0u, it[6].y, 0u, 0u}), as8(bx[t]), n1[t]);
      }
#pragma unroll
      for (int t = 0; t < 2; ++t) {
        const float r0 = __builtin_amdgcn_rcpf(nx[t][2] + 1e-6f), r1 = __builtin_amdgcn_rcpf(nx[t][3] + 1e-6f);
        const u32 ob = W_O + (team * 8 + 2 * w + t) * 20480;
        st4c(pk(nx[t][0] * r0, nx[t][1] * r1), ws, ve, ob + 16384 + rank * 256);
        st16c(v4u{pk(n0[t][0] * r0, n0[t][1] * r0), pk(n0[t][2] * r0, n0[t][3] * r0), pk(n1[t][0] * r1, n1[t][1] * r1), pk(n1[t][2] * r1, n1[t][3] * r1)}, ws,
              lane * 16, ob + rank * 1024);
      }
    }
    {
      const u32 lane = lane_id(), q = lane >> 4, i16 = lane & 15;
      const u32 tk = th * 64 + w * 16 + i16;
      const u32 xo = tk * 1280 + q * 16, xso = team * 128 * 1280 + hq * 160;
      const rsrc_t rx = mk_rsrc(x, SEQ * DM * 2);
      const v4u xr0 = __builtin_amdgcn_raw_buffer_load_b128(rx, xo, xso, 0), xr1 = __builtin_amdgcn_raw_buffer_load_b128(rx, xo + 64, xso, 0);
      const v2u xr2 = __builtin_amdgcn_raw_buffer_load_b64(rx, tk * 1280 + 128 + q * 8, xso, 0);
      constexpr int OD = 6;
      s8 wf[OD][5];
      u32 bo[2] = {L_WO + lane * 16, L_WO + 51200 + lane * 16};
      OPQ(bo[0]); OPQ(bo[1]);
      const u32 oso = W_O + (team * 8 + th * 4 + w) * 20480;
      auto ldo1 = [&](int ks, int t) LAM { wf[ks % OD][t] = lld<s8>(bo[ks / 10] + (ks % 10) * 5120 + t * 1024); };
#pragma unroll
      for (int ks = 0; ks < OD; ++ks)
#pragma unroll
        for (int t = 0; t < 5; ++t) ldo1(ks, t);
      WAITV(3);
      flag_store(sws, tag, W_FS2 + (bid * 4 + w) * 128);
      lds_inc(S_OP);
      STAMP(9);
      f4 ha[5] = {f4{blo(xr0.x), bhi(xr0.x), blo(xr0.y), bhi(xr0.y)}, f4{blo(xr0.z), bhi(xr0.z), blo(xr0.w), bhi(xr0.w)}, f4{blo(xr1.x), bhi(xr1.x), blo(xr1.y), bhi(xr1.y)},
                  f4{blo(xr1.z), bhi(xr1.z), blo(xr1.w), bhi(xr1.w)}, f4{blo(xr2.x), bhi(xr2.x), blo(xr2.y), bhi(xr2.y)}};
#pragma unroll
      for (int t = 0; t < 5; ++t) OPQ(ha[t]);
      __builtin_amdgcn_s_waitcnt(0xC07F);
      __builtin_amdgcn_s_sleep(PD2);
      SB();
      poll_flags(ws, W_FS2 + ((team * 16 + lane) * 4 + th * 2 + (w >> 1)) * 128, lane < 16, tag);
      STAMP(10);
      v4u of[20];
      constexpr int PD = 12;
      auto ldof = [&](int ks) LAM { of[ks] = ld16c(ws, lane * 16 + (ks & 3) * 1024, oso + (ks >> 2) * 4096); };
#pragma unroll
      for (int ks = 0; ks < PD; ++ks) ldof(ks);
      SB();
#pragma unroll
      for (int ks = 0; ks < 20; ++ks) {
#pragma unroll
        for (int t = 0; t < 5; ++t) {
          ha[t] = mfma16(wf[ks % OD][t], as8(of[ks]), ha[t]);
          if (t == 0 && ks + PD < 20) ldof(ks + PD);
          if (t == 1 && ks + PD == 19) lds_inc(S_OL);
          if (ks + OD < 20) ldo1(ks + OD, t);
          SB();
        }
      }
      STAMP(11);
      v4u hc[2];
#pragma unroll
      for (int m = 0; m < 2; ++m)
        hc[m] = v4u{pk(ha[2 * m][0], ha[2 * m][1]), pk(ha[2 * m][2], ha[2 * m][3]), pk(ha[2 * m + 1][0], ha[2 * m + 1][1]), pk(ha[2 * m + 1][2], ha[2 * m + 1][3])};
      const v2u ht = {pk(ha[4][0], ha[4][1]), pk(ha[4][2], ha[4][3])};
      const u32 hb = W_H + (team * 4 + th * 2 + (w >> 1)) * 40960 + hq * 5120;
      const u32 t32 = (w & 1) * 16 + i16;
      st16c(hc[0], ws, (q >> 1) * 1024 + (t32 + 32 * (q & 1)) * 16, hb);
      st16c(hc[1], ws, (q >> 1) * 1024 + (t32 + 32 * (q & 1)) * 16, hb + 2048);
      st8c(ht, ws, (t32 + 32 * (q >> 1)) * 16 + 8 * (q & 1), hb + 4096);
      float a[2] = {0.f, 0.f}, c[2] = {0.f, 0.f};
#pragma unroll
      for (int m = 0; m < 2; ++m) {
#pragma unroll
        for (int j = 0; j < 4; ++j) { a[m] = dot2(hc[m][j], 0x3F803F80u, a[m]); c[m] = dot2(hc[m][j], hc[m][j], c[m]); }
        a[m] = dot2(ht[m], 0x3F803F80u, a[m]); c[m] = dot2(ht[m], ht[m], c[m]);
      }
      float a2 = a[0] + a[1], c2 = c[0] + c[1];
      red2(a2, c2);
      st8c(v2u{fu(a2), fu(c2)}, ws, q == 0 ? tk * 8 : OOB, W_STAT + (team * 8 + hq) * 1024);
      lst<v4u>(L_HST + w * 2560 + lane * 16, hc[0]);
      lst<v4u>(L_HST + w * 2560 + 1024 + lane * 16, hc[1]);
      lst<v2u>(L_HST + w * 2560 + 2048 + lane * 8, ht);
    }
    WAITV(0);
    flag_store(sws, tag, W_FS3 + (bid * 4 + w) * 128);
    lds_inc(S_WOC);
    STAMP(12);
    __builtin_amdgcn_s_sleep(PD2);
    const u32 lane3 = lane_id();
#define lane lane3
    poll_flags(ws, W_FS3 + ((team * 16 + (w >> 1) * 8 + (lane >> 1)) * 4 + 2 * (w & 1) + (lane & 1)) * 128, lane < 16, tag);
    STAMP(13);
    v4u wl[3][5];
    u32 wlv;
    auto ldwl = [&](int k) LAM {
      const int i = 11 + k / 5;
      if (k == 0) wlv = lane_id() * 16;
      wl[k / 5][k % 5] = ld16c(ws, ((i >= 12 && w == 3) ? OOB : wlv) + (k % 5) * 1024, W_WD + (hq * 54 + 2 * w + (i >> 1) * 8 + (i & 1)) * 5120);
    };
    {
      const u32 hh = lane >> 5, t32 = lane & 31;
      v2u sv[4];
#pragma unroll
      for (int n = 0; n < 4; ++n) sv[n] = ld8c(ws, (hh * 4 + n) * 1024 + t32 * 8, W_STAT + team * 8192 + w * 256);
      v4u hf[40];
      constexpr int HD = 12;
      const u32 hso = W_H + (team * 4 + w) * 40960;
      auto ldh = [&](int s) LAM { hf[s] = ld16c(ws, lane * 16 + (s & 3) * 1024, hso + (s >> 2) * 4096); };
#pragma unroll
      for (int s = 0; s < HD; ++s) ldh(s);
      SB();
      lds_wait(L_LAND, 4);
      u32 lb[3] = {L_HS + lane * 16, L_HS + 3 * HSLOT + lane * 16, L_HS + 6 * HSLOT + lane * 16};
      OPQ(lb[0]); OPQ(lb[1]); OPQ(lb[2]);
      auto ldw = [&](int n, int s) LAM -> s8 {
        const int hs = (2 * n + s / 20 + 5) % 7;
        return lld<s8>(lb[hs / 3] + (hs % 3) * HSLOT + (s % 20) * 1024);
      };
      auto ldcs = [&](int n) LAM -> f16v {
        const u32 csb = L_CS3 + (lane_id() & 32);
        const f4 g0 = lld<f4>(csb + n * 128), g1 = lld<f4>(csb + n * 128 + 16), u0 = lld<f4>(csb + n * 128 + 64), u1 = lld<f4>(csb + n * 128 + 80);
        return __builtin_shufflevector(__builtin_shufflevector(g0, g1, 0, 1, 2, 3, 4, 5, 6, 7), __builtin_shufflevector(u0, u1, 0, 1, 2, 3, 4, 5, 6, 7), 0, 1, 2, 3, 4,
                                       5, 6, 7, 8, 9, 10, 11, 12, 13, 14, 15);
      };
      f16v ac[3];
      ac[0] = ldcs(0); ac[1] = ldcs(1); ac[2] = ldcs(2);
      constexpr int RD = RDEP;
      s8 rg[RD];
#pragma unroll
      for (int f = 0; f < RD; ++f) rg[f] = ldw(f & 1, f >> 1);
      SB();
      float nmean, cK1, cK2, cR;
      {
        const float a = (uf(sv[0].x) + uf(sv[1].x)) + (uf(sv[2].x) + uf(sv[3].x)), c = (uf(sv[0].y) + uf(sv[1].y)) + (uf(sv[2].y) + uf(sv[3].y));
        auto r = __builtin_amdgcn_permlane32_swap(fu(a), fu(a), false, false);
        auto r2 = __builtin_amdgcn_permlane32_swap(fu(c), fu(c), false, false);
        nmean = -(uf(r[0]) + uf(r[1])) * (1.0f / DM);
        cR = __builtin_fmaxf(__builtin_fmaf(-nmean, nmean, (uf(r2[0]) + uf(r2[1])) * (1.0f / DM)), 0.f) + 1e-5f;
        const float rstd = __builtin_amdgcn_rsqf(cR), k1 = -2.0f * 0.7978845608028654f * 1.4426950408889634f;
        cK1 = k1 * rstd; cK2 = cK1 * 0.044715f * rstd * rstd;
      }
#pragma unroll
      for (int j = 0; j < 16; ++j) { ac[0][j] *= nmean; ac[1][j] *= nmean; }
      SB();
      u32 lv;
#pragma unroll
      for (int f = 0; f < 80; ++f) {
        ac[f & 1] = mfma32(rg[f % RD], as8(hf[f >> 1]), ac[f & 1]);
        const int fn = f + RD;
        rg[f % RD] = fn < 80 ? ldw(fn & 1, fn >> 1) : ldw(2, fn - 80);
        if (f >= 8 && f < 24) ac[2][f - 8] *= nmean;
        if ((f & 1) == 0 && (f >> 1) + HD < 40) ldh((f >> 1) + HD);
        if (f == 2 * (39 - HD) + 1) lds_inc(S_HL);
        if (f == 80 - RD) lds_inc(L_CONS + 4);
        SB();
      }
      OPQ(cK1); OPQ(cK2); OPQ(cR);
      STAMP(14);
      const u32 gvo = (t32 >> 4) * 54 * 1024 + ((t32 & 15) + 16 * hh) * 16 + (rank & 1) * 512;
      const u32 gso = W_G + (team * 8 + 2 * w) * 54 * 1024 + (rank >> 1) * 1024;
      const u32 fso = W_FS4 + (bid * 4 + w) * 1024;
      float tv, au, pu, pr, ov[2];
      v4u gp;
      auto gstep = [&](const f16v& a, int e) LAM {
        const int j = e / 3, m = e % 3;
        if (m == 0 && j < 8) { tv = a[j] * a[j]; tv = __builtin_fmaf(tv, cK2, cK1); tv = tv * a[j]; au = a[j] * a[8 + j]; }
        if (m == 1) tv = __builtin_amdgcn_exp2f(tv);
        if ((m == 1 && j > 0) || e == 24) { ov[(j - 1) & 1] = pu * pr; if (((j - 1) & 1) == 1) gp[(j - 1) >> 1] = pk(ov[0], ov[1]); }
        if (m == 2) { pr = __builtin_amdgcn_rcpf(__builtin_fmaf(tv, cR, cR)); pu = au; }
      };
      auto pass = [&](auto nc) LAM {
        constexpr int n = decltype(nc)::v;
        sfor<0, 40>([&](auto sc) LAM {
          constexpr int s = decltype(sc)::v, g = (n - 2) * 40 + s, sn = s + RD;
          constexpr int G[6] = {1, 31, 61, 91, 122, 161}, I[4] = {26, 56, 86, 150};
          ac[n % 3] = mfma32(rg[s % RD], as8(hf[s]), ac[n % 3]);
          if constexpr (sn < 40) rg[s % RD] = ldw(n, sn);
          else if constexpr (n < 6) rg[s % RD] = ldw(n + 1, sn - 40);
          sfor<0, 6>([&](auto uc) LAM {
            constexpr int u = decltype(uc)::v;
            if constexpr (g >= G[u] && g <= G[u] + 24 && (!(ABL & 1) || g - G[u] < 6 || g - G[u] == 24)) gstep(ac[u % 3], g - G[u]);
            if constexpr (g == G[u] + 24) {
              if constexpr (u >= 2) { if (!(ABL & 2)) WAITV(0); flag_store(sws, tag, fso + u * 128); }
              st16c(gp, ws, gvo, gso + u * 8192);
            }
          });
          sfor<0, 4>([&](auto kc) LAM {
            constexpr int k = decltype(kc)::v;
            if constexpr (g == I[k] - 3) ac[k % 3] = ldcs(3 + k);
            if constexpr (g > I[k] && g <= I[k] + 4) {
#pragma unroll
              for (int j = 0; j < 4; ++j) ac[k % 3][4 * (g - I[k] - 1) + j] *= nmean;
            }
          });
          if constexpr (WLP && n == 6 && s >= 25) ldwl(s - 25);
          if constexpr (s == 40 - RD) lds_inc(L_CONS + 4 * n);
          if constexpr (s == 8) lv = *(volatile LAS u32*)(L_LAND + 4 * (n - 1));
          if constexpr (s == 14 && !(ABL & 4)) lds_check(lv, L_LAND + 4 * (n - 1), 4);
          SB();
        });
      };
      pass(IC<2>{}); STAMP(26); pass(IC<3>{}); STAMP(27); pass(IC<4>{}); STAMP(28); pass(IC<5>{}); STAMP(29);
      pass(IC<6>{});
      STAMP(15);
#pragma unroll
      for (int e = 0; e < 25; ++e) gstep(ac[0], e);
      st16c(gp, ws, u7 ? gvo : OOB, gso + 6 * 8192);
      if (WLP) WAITV(16); else WAITV(1);
      flag_store(sws, tag, fso + 6 * 128);
    }
#undef lane
    STAMP(30);
    {
      const u32 ln = lane_id(), vl = ln * 16;
      const u32 fv = W_FS4 + ((team * 16 + 4 * w + (ln >> 4)) * 4 + 2 * th + ((ln >> 3) & 1)) * 1024;
      const u32 gso = W_G + (team * 8 + th * 4) * 54 * 1024 + 2 * w * 1024;
      const u32 vz = w < 3 ? vl : OOB;
      u32 gsr[4];
#pragma unroll
      for (int r = 0; r < 4; ++r) gsr[r] = sgpr(gso + ((r + w + 1) & 3) * (54 * 1024));
      v4u wf[6][5], gf[6][4];
      auto ldg = [&](int i) LAM {
#pragma unroll
        for (int r = 0; r < 4; ++r) gf[i - 8][r] = ld16c(ws, i >= 12 ? vz : vl, gsr[r] + ((i >> 1) * 8 + (i & 1)) * 1024);
        SB();
      };
      v2u fl = ld8c(ws, fv, 5 * 128);
      if (!WLP) sfor<0, 5>([&](auto kc) LAM { ldwl(decltype(kc)::v); });
      if (WLP) WAITV(2); else WAITV(7);
      flag_store(sws, tag, W_FS4 + (bid * 4 + w) * 1024 + 7 * 128);
      STAMP(16);
      if (!wall(fl.x == tag.x && fl.y == tag.y)) poll_level(ws, fv, tag, 5);
      asm volatile("" ::: "memory");
      ldg(8); ldg(9);
      fl = ld8c(ws, fv, 6 * 128);
      if (!WLP) sfor<5, 15>([&](auto kc) LAM { ldwl(decltype(kc)::v); });
#pragma unroll
      for (int i = 11; i < 14; ++i)
#pragma unroll
        for (int t = 0; t < 5; ++t) wf[i - 8][t] = wl[i - 11][t];
      u32 sb = L_HS + 2 * HSLOT + w * 5120 + vl;
      OPQ(sb);
      lds_wait(S_SP);
#pragma unroll
      for (int i = 8; i < 10; ++i)
#pragma unroll
        for (int t = 0; t < 5; ++t) wf[i - 8][t] = lld<v4u>(sb + (i - 8) * HSLOT + t * 1024);
      if (!wall(fl.x == tag.x && fl.y == tag.y)) poll_level(ws, fv, tag, 6);
      asm volatile("" ::: "memory");
      STAMP(31);
      ldg(10);
      f4 da[4][5];
      auto step = [&](int i) LAM {
#pragma unroll
        for (int m = 0; m < 20; ++m) da[m / 5][m % 5] = mfma16(as8(wf[i - 8][m % 5]), as8(gf[i - 8][m / 5]), i == 8 ? z4 : da[m / 5][m % 5]);
        SB();
      };
      step(8);
      ldg(11);
      fl = ld8c(ws, fv, 7 * 128);
#pragma unroll
      for (int t = 0; t < 5; ++t) wf[2][t] = lld<v4u>(sb + 2 * HSLOT + t * 1024);
      SB();
      step(9);
      if (w < 3 && !wall(fl.x == tag.x && fl.y == tag.y)) poll_level(ws, fv, tag, 7);
      asm volatile("" ::: "memory");
      STAMP(1);
      ldg(12); ldg(13);
      step(10); step(11);
      STAMP(6);
      step(12);
#pragma unroll
      for (int m = 0; m < 21; ++m) {
        if (m < 20) da[m / 5][m % 5] = mfma16(as8(wf[5][m % 5]), as8(gf[5][m / 5]), da[m / 5][m % 5]);
        if (m >= 6) lst<f4>(sb + ((m - 6) / 5) * HSLOT + ((m - 6) % 5) * 1024, da[(m - 6) / 5][(m - 6) % 5]);
        SB();
      }
      u32 rb[4];
#pragma unroll
      for (int r = 0; r < 3; ++r) { rb[r] = L_HS + (2 + r) * HSLOT + ((w - r - 1) & 3) * 5120 + vl; OPQ(rb[r]); }
      rb[3] = L_HS + ((w + 5) % 7) * HSLOT + vl;
      OPQ(rb[3]);
      const rsrc_t ro = mk_rsrc(out, SEQ * DM * 2);
      const u32 oso = (team * 128 + th * 64 + w * 16) * 1280 + hq * 160;
      u32 ovo = (ln & 15) * 1280 + (ln >> 4) * 16, ov2 = (ln & 15) * 1280 + 128 + (ln >> 4) * 8;
      OPQ(ovo); OPQ(ov2);
      bar();
      STAMP(17);
      f4 r[5][4], rr[5];
#pragma unroll
      for (int n = 0; n < 5; ++n)
#pragma unroll
        for (int j = 0; j < 4; ++j) r[n][j] = lld<f4>(rb[j] + n * 1024);
      SB();
#pragma unroll
      for (int n = 0; n < 5; ++n) rr[n] = ((da[3][n] + r[n][0]) + (r[n][1] + r[n][2])) + r[n][3];
#pragma unroll
      for (int m = 0; m < 2; ++m)
        st16p(v4u{pk(rr[2 * m][0], rr[2 * m][1]), pk(rr[2 * m][2], rr[2 * m][3]), pk(rr[2 * m + 1][0], rr[2 * m + 1][1]), pk(rr[2 * m + 1][2], rr[2 * m + 1][3])}, ro,
              ovo + 64 * m, oso);
      __builtin_amdgcn_raw_buffer_store_b64(v2u{pk(rr[4][0], rr[4][1]), pk(rr[4][2], rr[4][3])}, ro, ov2, oso, 0);
      if (b == 0 && w == 0) flag_store(sws, tag, W_EPOCH);
    }
    STAMP(18);
    DUMP();
  } else {
    const u32 j = wv;
    __builtin_amdgcn_s_setprio(HPRIO);
    const u32 lane = lane_id();
    const u32 q = lane >> 4, i16 = lane & 15, tq = (lane >> 2) & 3, tp = lane & 3, vl = lane * 16;
    const u32 p1 = j * 256 + b;
    const bool isq = p1 < 600;
    const u32 pp = isq ? p1 : p1 - 600, pks = (pp * (isq ? 2185u : 6554u)) >> 16, pcb1 = pp - pks * (isq ? 30 : 10);
    u32 pv0, ps0, psn, pl0 = L_A + j * 4096;
    {
      const u32 jr = lane >> 3;
      const bool ex = pks >= 16;
      const u32 ra = 20 * (jr & 1) + 16 + (jr >> 1), rb = 20 * (jr >> 2) + (jr & 3);
      const u32 m1 = isq ? 3840 : 1280, m2 = isq ? 8 : ex ? 40 : 4, m3 = isq ? 32 : ex ? 160 : 40, m4 = (!isq && ex) ? 2560 : 0;
      pv0 = (isq ? jr : ex ? ra : rb) * m1 + (lane & 7) * 16;
      psn = m2 * m1;
      ps0 = (m3 * pks - m4) * m1 + pcb1 * 128;
    }
    OPS(ps0); OPS(psn); OPS(pl0); OPQ(pv0);
    if (lane < 32) lst<u32>(L_SYNC + lane * 4, 0u);
    GET_ARGS();
    {
      const v4u sp = mk_srd((const void*)(isq ? ka[1] : ka[3]), pks >= 20 ? 0u : isq ? DM * 3 * DM * 2 : DM * DM * 2);
#pragma unroll
      for (int n = 0; n < 4; ++n) { if (NT & 1) dma16n(pv0, sp, ps0 + n * psn, pl0 + n * 1024); else dma16(pv0, sp, ps0 + n * psn, pl0 + n * 1024); }
    }
    bar();
    u64 ep;
    asm volatile("s_load_dwordx2 %0, %1, 0x0" : "=s"(ep) : "s"(wsp));
    if (j == 3) {
      const v4u sp = mk_srd((const void*)ka[2], 32 * 400 * 2);
      dma16(vl, sp, rank * 1600, L_WPHI); dma16(vl, sp, rank * 1600 + 1024, L_WPHI + 1024);
    }
    u32 selv = i16 == q ? 0x3F803F80u : 0u;
    OPQ(selv);
    const s8 sel = as8(v4u{selv, selv, selv, selv});
    const u32 pi4 = ((tp & 1) << 1) | (tp >> 1);
    {
      u32 dv, cv;
      if (isq) {
        const u32 sec = (pcb1 * 26) >> 8, cn = (pcb1 - sec * 10) * 64 + lane, head = (cn * 3277) >> 16, d = cn - head * 20, hh = head & 1;
        const u32 par = pks & 1;
        const u32 tb = sec == 0 ? par * 2048 : sec * 3072 - 3072, xb = sec == 0 ? 4096 - 256 + par * 128 : 2048 - 256 - 128 + sec * 128;
        const u32 ds = W_W1 + (sec == 0 ? 102400 + (pks >> 1) * 5120 : pks * 5120);
        dv = (head >> 1) * 153600 + d * 16 + (d < 16 ? tb + hh * 1024 : xb + hh * 64) + ds;
        cv = i16 < 4 ? W_CS1 + pks * 7680 + pcb1 * 256 + i16 * 64 + q * 16 : OOB;
      } else {
        const u32 sgi = 4 * pcb1 + q, cc = (sgi * 52) >> 8, pos = sgi - cc * 5;
        const u32 o1 = ((pos & 2) + (i16 >> 3)) * 1024 + (8 * (pos & 1) + (i16 & 7)) * 16, o2 = 4096 + swap23(i16) * 16;
        dv = pks >= 20 ? OOB : cc * 102400 + (pos < 4 ? o1 : o2) + W_WO + pks * 5120;
        cv = OOB;
      }
      u32 rb = L_A + j * 4096 + tq * 128 + q * 32 + (isq ? tp : pi4) * 8;
      OPQ(dv); OPQ(rb); OPQ(cv);
      WAITV(0);
      v4u f[4];
#pragma unroll
      for (int n = 0; n < 4; ++n) f[n] = __builtin_bit_cast(v4u, cat(trd(rb + n * 1024), trd(rb + n * 1024 + 512)));
      f4 cs = z4;
#pragma unroll
      for (int n = 0; n < 4; ++n) { st16c(f[n], ws, dv + n * 256, 0); cs = mfma16(as8(f[n]), sel, cs); }
      st16c(__builtin_bit_cast(v4u, cs), ws, cv, 0);
      v4u sent = {SENT, SENT, SENT, SENT};
      OPQ(sent);
      st16c(sent, ws, vl, W_PART + (rank * 16 + team) * 8192 + j * 2048);
      st16c(sent, ws, vl, W_PART + (rank * 16 + team) * 8192 + j * 2048 + 1024);
      st16c(sent, ws, lane < 4 ? vl : OOB, W_RED + rank * 4096 + team * 256 + j * 64);
      SB();
    }
    STAMP(1);
    if (j == 3) {
      v2u pl[2][4];
      const int src = ((lane & ~3u) | tq) * 4;
#pragma unroll
      for (int h = 0; h < 2; ++h) {
        const u32 pb = L_WPHI + h * 800;
        pl[h][0] = __builtin_bit_cast(v2u, trd(pb + (4 * q + tq) * 40 + 8 * tp)); pl[h][1] = __builtin_bit_cast(v2u, trd(pb + (16 + tq) * 40 + 8 * tp));
        pl[h][2] = __builtin_bit_cast(v2u, trd(pb + (4 * q + tq) * 40 + 32)); pl[h][3] = __builtin_bit_cast(v2u, trd(pb + (16 + tq) * 40 + 32));
      }
      asm volatile("" ::: "memory");
#pragma unroll
      for (int h = 0; h < 2; ++h) {
        const u32 qh = q == (u32)h ? ~0u : 0u, rh = tp == (u32)h ? ~0u : 0u;
        lst<v4u>(L_WPHI + h * 1024 + vl, v4u{pl[h][0].x, pl[h][0].y, pl[h][1].x & qh, pl[h][1].y & qh});
        lst<v4u>(L_PH2 + h * 1024 + vl, v4u{(u32)__builtin_amdgcn_ds_bpermute(src, pl[h][2].x) & rh, (u32)__builtin_amdgcn_ds_bpermute(src, pl[h][2].y) & rh,
                                            (u32)__builtin_amdgcn_ds_bpermute(src, pl[h][3].x) & rh & qh, (u32)__builtin_amdgcn_ds_bpermute(src, pl[h][3].y) & rh & qh});
      }
    }
    GET_EPOCH();
    WAITV(0);
    lds_inc(S_PST);
    if (j == 0) { lds_wait<0>(S_PST); flag_store(sws, tag, W_FA + bid * FAS); }
    STAMP(9);
    u32 rdv[2], rdn[2], rcv[2], rvo[2], rs0[2], rsn[2];
    v4u rsp[2];
#pragma unroll
    for (int e = 0; e < 2; ++e) {
      const u32 piece = p1 + e * 1024;
      const bool isg = piece < 1080;
      const u32 p2 = isg ? piece : piece - 1080;
      const u32 pk_ = (p2 * (isg ? 1214u : 6554u)) >> 16, pcb = p2 - pk_ * (isg ? 54 : 10);
      const u32 mat = pcb >= 27 ? 1 : 0, rs = isg ? 3456 : 1280, u0 = 4 * (pcb - mat * 27);
      rsp[e] = mk_srd((const void*)(!isg ? ka[6] : mat ? ka[5] : ka[4]), piece < 1620 ? DM * FF * 2 : 0u);
      rs0[e] = pk_ * 32 * rs + u0 * 32; rsn[e] = 8 * rs; rvo[e] = (lane >> 3) * rs + (lane & 7) * 16;
      const u32 sgi = u0 + q, cc = (sgi * 52) >> 8, pos = sgi - cc * 5;
      const u32 o1 = ((pos & 2) + (i16 >> 3)) * 1024 + (8 * (pos & 1) + (i16 & 7)) * 16, o2 = 4096 + swap23(i16) * 16;
      const u32 dg = W_WGU + sgi * 40960 + i16 * 16 + pk_ * 2048 + mat * 256, dd = W_WD + cc * (54 * 5120) + (pos < 4 ? o1 : o2) + pk_ * 5120;
      rdv[e] = isg ? dg : piece < 1620 ? dd : OOB;
      rdn[e] = isg ? 512 : 256;
      rcv[e] = (isg && i16 < 4) ? W_CSP + (pk_ * 108 + u0 + i16) * 128 + mat * 64 + (((q & 1) << 1) | (q >> 1)) * 16 : OOB;
      OPQ(rdv[e]); OPQ(rcv[e]); OPQ(rvo[e]); OPS(rs0[e]); OPS(rsn[e]); OPS(rdn[e]);
      rsp[e].x = sgpr(rsp[e].x); rsp[e].y = sgpr(rsp[e].y); rsp[e].z = sgpr(rsp[e].z);
    }
    u32 c1v;
    { const u32 t = (j & 1) * 64 + lane, sec = (t * 1639) >> 16; c1v = t < 120 ? (sec * 600 + t) * 4 + (j >> 1) * (10 * 7680) : OOB; OPQ(c1v); }
    v4u rf[10];
    {
      v4u rt[2][4];
#pragma unroll
      for (int e = 0; e < 2; ++e) {
        const rsrc_t sp = mk_rsrc((const void*)(((u64)rsp[e].y << 32) | rsp[e].x), rsp[e].z);
#pragma unroll
        for (int n = 0; n < 4; ++n) rt[e][n] = __builtin_amdgcn_raw_buffer_load_b128(sp, rvo[e], rs0[e] + n * rsn[e], (NT & 2) ? 2 : 0);
      }
      SB();
      const u32 rb = L_KV + j * 1024 + tq * 128 + q * 32 + pi4 * 8;
#pragma unroll
      for (int e = 0; e < 2; ++e) {
        f4 cs = z4;
#pragma unroll
        for (int n = 0; n < 4; ++n) {
          lst<v4u>(L_KV + j * 1024 + vl, rt[e][n]);
          asm volatile("" ::: "memory");
          rf[5 * e + n] = __builtin_bit_cast(v4u, cat(trd(rb), trd(rb + 512)));
          asm volatile("" ::: "memory");
        }
#pragma unroll
        for (int n = 0; n < 4; ++n) cs = mfma16(as8(rf[5 * e + n]), sel, cs);
        rf[5 * e + 4] = __builtin_bit_cast(v4u, cs);
        OPQ(rf[5 * e + 4]);
      }
    }
    auto rst = [&](int k) LAM { if (k % 5 < 4) st16c(rf[k], ws, rdv[k / 5], (k % 5) * rdn[k / 5]); else st16c(rf[k], ws, rcv[k / 5], 0); };
    STAMP(23);
    __builtin_amdgcn_s_sleep(FAD);
    {
      u32 fv = (j * 64 + lane) * FAS;
#pragma unroll 1
      for (u32 s = 0; s < SPIN_CAP; ++s) {
        const v2u v = ld8c(ws, fv, W_FA);
        fv = (v.x == tag.x && v.y == tag.y) ? OOB : fv;
        if (wall(fv == OOB)) break;
        __builtin_amdgcn_s_sleep(FAR);
        asm volatile("" ::: "memory");
      }
    }
    WAITV(0);
    lds_inc(S_FA);
    lds_wait<0>(S_FA);
    STAMP(24);
    {
      u32 w1s = W_W1 + rank * 153600 + j * 5120, w1l = L_A + j * 5120;
      OPS(w1s); OPS(w1l);
      auto dd = [&](int m) LAM { dma5c(vl, sws, w1s + m * 20480, w1l + m * 20480); };
      u32 cv[10];
      dd(0); dd(1); rst(0);
      WAITV(6); lds_inc(S_L1);
      dd(2); rst(1); dd(3); rst(2);
      WAITV(13); lds_inc(S_L1 + 4);
      dd(4); rst(3); rst(4);
      WAITV(14); lds_inc(S_L1 + 8);
#pragma unroll
      for (int k = 0; k < 10; ++k) cv[k] = ld4c(ws, c1v, W_CS1 + k * 7680 + rank * 160);
      rst(5);
      WAITV(19); lds_inc(S_L1 + 12);
      lds_wait(S_XD);
      dd(5); rst(6);
      WAITV(19); lds_inc(S_L1 + 16);
      dd(6); rst(7);
      if (j < 2) dd(7);
      rst(8); rst(9);
      {
        const float c = (((uf(cv[0]) + uf(cv[1])) + (uf(cv[2]) + uf(cv[3]))) + ((uf(cv[4]) + uf(cv[5])) + (uf(cv[6]) + uf(cv[7])))) + (uf(cv[8]) + uf(cv[9]));
        lst<float>(L_CS1 + j * 256 + lane * 4, c);
      }
      lds_inc(S_CS);
      WAITV(0);
      lds_inc(S_RT);
      if (j == 3) { lds_wait<0>(S_RT); flag_store(sws, tag, W_FB + bid * 32); }
    }
    STAMP(5);
    STAMP(25);
    u32 wos = W_WO + hq * 102400;
    OPS(wos);
    v4u wf[11][5], gf[8][4];
    const u32 wso = W_WD + (hq * 54 + 2 * j) * 5120;
    const u32 gso = W_G + (team * 8 + th * 4) * 54 * 1024 + 2 * j * 1024;
    auto ldw1 = [&](int i, int t) LAM { wf[i][t] = ld16c(ws, vl + t * 1024, wso + ((i >> 1) * 8 + (i & 1)) * 5120); };
    auto ldg1 = [&](int i, int r) LAM { gf[i][r] = ld16c(ws, vl, gso + r * 54 * 1024 + ((i >> 1) * 8 + (i & 1)) * 1024); };
    STAMP(2);
    lds_wait<2>(S_KVD);
#pragma unroll
    for (int n = 0; n < 3; ++n) dma5c(vl, sws, wos + (8 + 4 * n + j) * 5120, L_WO + (8 + 4 * n + j) * 5120);
    WAITV(15);
    STAMP(3);
    kv_job<true>(ws, j + 4, W_PART + (rank * 16 + team) * 8192);
    STAMP(4);
    bar_nw();
    dma5c(vl, sws, wos + j * 5120, L_WO + j * 5120);
    dma5c(vl, sws, wos + (4 + j) * 5120, L_WO + (4 + j) * 5120);
    if (j == 3) {
      const u32 pvo = (lane >> 5) * 8 * 8192 + (lane & 31) * 16, pso = W_PART + rank * 16 * 8192 + team * 512;
      v4u pv[8];
      __builtin_amdgcn_s_setprio(3);
      __builtin_amdgcn_s_sleep(HOPD);
#pragma unroll 1
      for (u32 s = 0; s < SPIN_CAP; ++s) {
#pragma unroll
        for (int n = 0; n < 8; ++n) pv[n] = ld16c(ws, pvo, pso + n * 8192);
        SB();
        u32 mx = 0;
#pragma unroll
        for (int n = 0; n < 8; ++n) mx = umax4(mx, pv[n]);
        if (wall(mx != SENT)) break;
        asm volatile("" ::: "memory");
      }
      float sm[4];
#pragma unroll
      for (int k = 0; k < 4; ++k) {
        const float a = ((uf(pv[0][k]) + uf(pv[1][k])) + (uf(pv[2][k]) + uf(pv[3][k]))) + ((uf(pv[4][k]) + uf(pv[5][k])) + (uf(pv[6][k]) + uf(pv[7][k])));
        auto r = __builtin_amdgcn_permlane32_swap(fu(a), fu(a), false, false);
        sm[k] = uf(r[0]) + uf(r[1]);
      }
      st8c(v2u{san(pk(sm[0], sm[1])), san(pk(sm[2], sm[3]))}, ws, lane < 32 ? lane * 8 : OOB, W_RED + rank * 4096 + team * 256);
      STAMP(6);
    } else if (j == 2) { poll256(ws, W_FB, 32, lane, tag); lds_inc(S_FB); }
    lds_wait(S_FB, 1);
    STAMP(22);
    sfor<0, 5 * NW1>([&](auto ic) LAM { ldw1(decltype(ic)::v / 5, decltype(ic)::v % 5); });
    SB();
    if (j == 3) {
      __builtin_amdgcn_s_sleep(HOPE);
      v4u kv[4];
#pragma unroll 1
      for (u32 s = 0; s < SPIN_CAP; ++s) {
#pragma unroll
        for (int n = 0; n < 4; ++n) kv[n] = ld16c(ws, vl, W_RED + rank * 4096 + n * 1024);
        SB();
        u32 mx = 0;
#pragma unroll
        for (int n = 0; n < 4; ++n) mx = umax4(mx, kv[n]);
        if (wall(mx != SENT)) break;
        asm volatile("" ::: "memory");
      }
#pragma unroll
      for (int n = 0; n < 4; ++n) lst<v4u>(L_KV + n * 1024 + vl, kv[n]);
    }
    WAITV(0);
    STAMP(7);
    bar();
    __builtin_amdgcn_s_setprio(HPRIO);
    STAMP(8);
    auto stage_u = [&](u32 hs) LAM {
      const u32 so = W_WGU + (16 * (hs >> 1) + rank) * 40960 + ((hs & 1) * 4 + j) * 5120, dst = L_HS + ((hs + 5) % 7) * HSLOT + j * 5120;
      if (hs >= 7) waitv<PACE>();
      if (!(ABL & 8) || hs < 7) dma5c(vl, sws, so, dst);
    };
    lds_wait(S_OP);
    stage_u(0); stage_u(1);
    {
      const u32 t = j * 64 + lane, u = 16 * (t >> 5) + rank;
      if (u < 108) {
        u32 cv[20];
#pragma unroll
        for (int k = 0; k < 20; ++k) cv[k] = ld4c(ws, u * 128 + (t & 31) * 4, W_CSP + k * 108 * 128);
        SB();
        float c = 0.f;
#pragma unroll
        for (int k = 0; k < 20; ++k) c += uf(cv[k]);
        lst<float>(L_CS3 + t * 4, c);
      }
    }
    u32 sdb[3] = {L_HS + j * 5120 + vl, L_HS + 3 * HSLOT + j * 5120 + vl, L_HS + 6 * HSLOT + j * 5120 + vl};
    OPQ(sdb[0]); OPQ(sdb[1]); OPQ(sdb[2]);
    auto spill = [&](int i) LAM {
#pragma unroll
      for (int t = 0; t < 5; ++t) lst<v4u>(sdb[((i + 1) % 7) / 3] + (((i + 1) % 7) % 3) * HSLOT + t * 1024, wf[i][t]);
      SB();
    };
    auto ldw = [&](auto lo, auto hi) LAM {
      sfor<decltype(lo)::v * 5, decltype(hi)::v * 5>([&](auto ic) LAM { if (decltype(ic)::v % 5 == 0) waitv<PACE>(); ldw1(decltype(ic)::v / 5, decltype(ic)::v % 5); });
      SB();
    };
    auto ldg = [&](int i) LAM {
      waitv<PACE>();
#pragma unroll
      for (int r = 0; r < 4; ++r) ldg1(i, r);
      SB();
    };
    STAMP(10);
    lds_wait(S_OL);
    ldw(IC<NW1>{}, IC<NW2>{});
    lds_wait<2>(S_WOC);
    STAMP(11);
    auto landed = [&](int k) LAM { lds_inc(L_LAND + 4 * k); };
    stage_u(2); stage_u(3); stage_u(4);
    WAITV(0); landed(0);
    STAMP(13);
    stage_u(5); stage_u(6);
    WAITV(0); landed(1);
    lds_wait<2>(S_HL);
    ldw(IC<NW2>{}, IC<NW3>{});
    lds_wait<1>(L_CONS + 4);
    stage_u(7); stage_u(8); stage_u(9);
    WAITV(5); landed(2);
    stage_u(10);
    ldw(IC<NW3>{}, IC<9>{});
    waitv<5 * (9 - NW3)>(); landed(3);
    lds_wait<2>(L_CONS + 8);
    stage_u(11);
    if (u7) stage_u(12);
    ldw(IC<9>{}, IC<11>{});
    WAITV(10); landed(4);
    const u32 fvo = W_FS4 + ((team * 16 + 4 * j + (lane >> 4)) * 4 + 2 * th + ((lane >> 3) & 1)) * 1024;
    lds_wait<2>(L_CONS + 12);
    if (u7) stage_u(13);
    spill(4);
    WAITV(0); landed(5);
    STAMP(26);
    lds_wait<2>(L_CONS + 16);
    STAMP(27);
    v2u fl = ld8c(ws, fvo, 2 * 128);
    spill(5); spill(6);
    if (!wall(fl.x == tag.x && fl.y == tag.y)) poll_level(ws, fvo, tag, 2);
    asm volatile("" ::: "memory");
    ldg(0); ldg(1); ldg(2);
    lds_wait<2>(L_CONS + 20);
    STAMP(28);
    fl = ld8c(ws, fvo, 3 * 128);
    spill(7); spill(8); ldg(3);
    if (!wall(fl.x == tag.x && fl.y == tag.y)) poll_level(ws, fvo, tag, 3);
    asm volatile("" ::: "memory");
    STAMP(29);
    __builtin_amdgcn_s_setprio(P4PRIO);
    lds_wait<1>(L_CONS + 24);
    STAMP(16);
    fl = ld8c(ws, fvo, 4 * 128);
    spill(9); spill(10);
    lds_inc(S_SP);
    f4 da[4][5];
    sfor<0, 8>([&](auto ic) LAM {
      constexpr int i = decltype(ic)::v;
#pragma unroll
      for (int m = 0; m < 20; ++m) {
        const int r = m / 5, t = m % 5;
        da[r][t] = mfma16(as8(wf[i][t]), as8(gf[i][r]), i == 0 ? z4 : da[r][t]);
        if constexpr (i <= 3) { if (t == 4) ldg1(i + 4, r); }
        if constexpr (i >= 3 && i <= 6) { if (m >= 12 && m < 17) wf[i + 1][m - 12] = lld<v4u>(sdb[((i + 2) % 7) / 3] + (((i + 2) % 7) % 3) * HSLOT + (m - 12) * 1024); }
        if constexpr (i == 7) { if (m >= 6) lst<f4>(sdb[(((m - 6) / 5 + 5) % 7) / 3] + ((((m - 6) / 5 + 5) % 7) % 3) * HSLOT + ((m - 6) % 5) * 1024, da[(m - 6) / 5][(m - 6) % 5]); }
        SB();
      }
      if constexpr (i == 1) {
        if (!wall(fl.x == tag.x && fl.y == tag.y)) poll_level(ws, fvo, tag, 4);
        asm volatile("" ::: "memory");
      }
      SB();
    });
    STAMP(18);
#pragma unroll
    for (int m = 14; m < 20; ++m) lst<f4>(sdb[((m / 5 + 5) % 7) / 3] + (((m / 5 + 5) % 7) % 3) * HSLOT + (m % 5) * 1024, da[m / 5][m % 5]);
    lds_inc(S_HP);
    __builtin_amdgcn_s_setprio(0);
    lds_wait(S_HP);
    {
      const u32 pb = L_HS + ((j + 5) % 7) * HSLOT + vl;
      f4 r[5][4];
#pragma unroll
      for (int n = 0; n < 5; ++n)
#pragma unroll
        for (int k = 0; k < 4; ++k) r[n][k] = lld<f4>(pb + k * 5120 + n * 1024);
      const v4u h0 = lld<v4u>(L_HST + j * 2560 + vl), h1 = lld<v4u>(L_HST + j * 2560 + 1024 + vl);
      const v2u h2 = lld<v2u>(L_HST + j * 2560 + 2048 + lane * 8);
      const u32 hw[10] = {h0.x, h0.y, h0.z, h0.w, h1.x, h1.y, h1.z, h1.w, h2.x, h2.y};
#pragma unroll
      for (int n = 0; n < 5; ++n)
        lst<f4>(pb + n * 1024, ((r[n][0] + r[n][1]) + (r[n][2] + r[n][3])) + f4{blo(hw[2 * n]), bhi(hw[2 * n]), blo(hw[2 * n + 1]), bhi(hw[2 * n + 1])});
    }
    bar();
    DUMP();
  }
}

extern "C" void kernel_launch(void* const* d_in, const int* in_sizes, int n_in, void* d_out, int out_size, void* d_ws, size_t ws_size,
                              hipStream_t stream) {
  (void)hipFuncSetAttribute((const void*)fused_block, hipFuncAttributeMaxDynamicSharedMemorySize, L_BYTES);
  fused_block<<<256, 512, L_BYTES, stream>>>((const u16*)d_in[0], (const u16*)d_in[1], (const u16*)d_in[2], (const u16*)d_in[3],
                                             (const u16*)d_in[5], (const u16*)d_in[6], (const u16*)d_in[7], (u16*)d_out, (char*)d_ws);
}
```

```cpp
#include <hip/hip_runtime.h>
#include <hip/hip_bf16.h>

typedef unsigned short u16;
typedef unsigned int u32;
typedef unsigned long long u64;
typedef u32 v4u __attribute__((ext_vector_type(4)));
typedef u32 v2u __attribute__((ext_vector_type(2)));
typedef float f4 __attribute__((ext_vector_type(4)));
typedef float f2 __attribute__((ext_vector_type(2)));
typedef float f16v __attribute__((ext_vector_type(16)));
typedef short s4 __attribute__((ext_vector_type(4)));
typedef short s8 __attribute__((ext_vector_type(8)));
typedef __bf16 bf2 __attribute__((ext_vector_type(2)));
#define LAS __attribute__((address_space(3)))
#define CAS __attribute__((address_space(4)))
#if defined(__HIP_DEVICE_COMPILE__)
#define NOPK __attribute__((target("no-packed-fp32-ops")))
#else
#define NOPK
#endif
#define DEV __device__ __forceinline__ NOPK
#define SB() __builtin_amdgcn_sched_barrier(0)
#define OPQ(x) asm volatile("" : "+v"(x))
typedef __amdgpu_buffer_rsrc_t rsrc_t;

#define STMASK (1u << 0)
#define DBG 3
#define ROT ((rank + 3 * (team & 1)) % 10)
#define P1ST 0
#define HOPA0 28
#define HOPA1 56
#define HOPB 1
#define FAS 32
#define NRM1 3
#define NRM2 8
#define HOPA 2
#define NWPRE 2
#define HOPD 12
#define HOPE 4
#define FAD 0
#define FAR 4
#define PD2 14
#define KDEP 6
#define FBD 0
#define LVS 8
#define WD6 8
#define NW2 6
#define NW0 4
#define NW1 4
#define XD3 1
#define HPRIO 0
#define RDEP 4
#define CODEPF 1
#define CODE_BYTES 45056
#define HROT 0
#define OROT 0
#define P4PRIO 3
#define ABL 0
#define NW3 7
#define WLP 0
#define PACE 63
#define XT 1
#define RT 0
#define RLATE 0
#define RSP 2
#define NT 0
#define XMAP 4

constexpr int SEQ = 2048, DM = 640, FF = 1728;
constexpr u32 W_EPOCH = 0;
constexpr u32 W_FA = 4096;
constexpr u32 W_FB = W_FA + 256 * 128;
constexpr u32 W_FS2 = W_FB + 256 * 32;
constexpr u32 W_FS3 = W_FS2 + 256 * 4 * 128;
constexpr u32 W_FS4 = W_FS3 + 256 * 4 * 128;
constexpr u32 W_RED = W_FS4 + 256 * 4 * 8 * 128;
constexpr u32 W_STAT = W_RED + 65536;
constexpr u32 W_CSP = W_STAT + 131072;
constexpr u32 W_CS1 = W_CSP + 20 * 108 * 128;
constexpr u32 W_PART = W_CS1 + 20 * 7680;
constexpr u32 W_O = W_PART + 16 * 16 * 8192;
constexpr u32 W_H = W_O + 16 * 8 * 20 * 1024;
constexpr u32 W_G = W_H + 16 * 4 * 40 * 1024;
constexpr u32 W_WGU = W_G + 16 * 8 * 54 * 1024;
constexpr u32 W_WD = W_WGU + 108 * 40 * 1024;
constexpr u32 W_WO = W_WD + 8 * 54 * 5 * 1024;
constexpr u32 W_W1 = W_WO + 8 * 20 * 5 * 1024;
constexpr u32 W_DBG = W_W1 + 16 * 153600;
constexpr u32 W_END = W_DBG + 256 * 8 * 32 * 4;
constexpr u32 L_WPHI = 0;
constexpr u32 L_CS3 = 3200;
constexpr u32 L_SYNC = 3072;
constexpr u32 L_CS1 = 2048;
constexpr u32 S_PST = L_SYNC, S_FA = L_SYNC + 4, S_XD = L_SYNC + 8, S_CS = L_SYNC + 12, S_KVD = L_SYNC + 16, S_WOC = L_SYNC + 20, S_RT = L_SYNC + 24, S_FB = L_SYNC + 28, S_OP = L_SYNC + 52, S_OL = L_SYNC + 56, S_HL = L_SYNC + 60;
constexpr u32 S_L1 = L_SYNC + 32, S_SP = L_SYNC + 64, S_HP = L_SYNC + 68, L_LAND = L_SYNC + 80, L_CONS = L_SYNC + 100;
constexpr u32 L_KV = 4096;
constexpr u32 L_A = 8192;
constexpr u32 L_B = L_A + 102400;
constexpr u32 L_PH2 = L_B + 51200;
constexpr u32 L_IMG = L_A, L_WO = L_A;
constexpr u32 L_HS = L_A, HSLOT = 20480;
constexpr u32 L_HST = L_HS + 7 * HSLOT;
constexpr u32 L_BYTES = 163840;
constexpr u32 OOB = 0x80000000u;
constexpr u32 SENT = 0xFFFFFFFFu;
constexpr u32 SPIN_CAP = 1u << 18;

DEV v4u mk_srd(const void* p, u32 bytes) { return v4u{(u32)(size_t)p, (u32)((size_t)p >> 32), bytes, 0x00020000u}; }
DEV rsrc_t mk_rsrc(const void* p, u32 bytes) { return __builtin_amdgcn_make_buffer_rsrc((void*)p, 0, bytes, 0x00020000); }
DEV void dma16(u32 voff, v4u srd, u32 soff, u32 lds) {
  asm volatile("s_mov_b32 m0, %2\n\ts_nop 0\n\tbuffer_load_dwordx4 %0, %1, %3 offen lds" ::"v"(voff), "s"(srd), "s"(lds), "s"(soff) : "memory");
}
DEV void dma16c(u32 voff, v4u srd, u32 soff, u32 lds) {
  asm volatile("s_mov_b32 m0, %2\n\ts_nop 0\n\tbuffer_load_dwordx4 %0, %1, %3 offen sc1 lds" ::"v"(voff), "s"(srd), "s"(lds), "s"(soff) : "memory");
}
DEV void dma16n(u32 voff, v4u srd, u32 soff, u32 lds) {
  asm volatile("s_mov_b32 m0, %2\n\ts_nop 0\n\tbuffer_load_dwordx4 %0, %1, %3 offen nt lds" ::"v"(voff), "s"(srd), "s"(lds), "s"(soff) : "memory");
}
DEV void dma_a(u32 voff, v4u srd, u32 sb, u32 sa, u32 lb, u32 la) {
  u32 t;
  asm volatile("s_add_u32 m0, %5, %6\n\ts_add_u32 %0, %3, %4\n\tbuffer_load_dwordx4 %1, %2, %0 offen lds"
               : "=&s"(t) : "v"(voff), "s"(srd), "s"(sb), "s"(sa), "s"(lb), "s"(la) : "memory", "scc");
}
DEV void dma_ac(u32 voff, v4u srd, u32 sb, u32 sa, u32 lb, u32 la) {
  u32 t;
  asm volatile("s_add_u32 m0, %5, %6\n\ts_add_u32 %0, %3, %4\n\tbuffer_load_dwordx4 %1, %2, %0 offen sc1 lds"
               : "=&s"(t) : "v"(voff), "s"(srd), "s"(sb), "s"(sa), "s"(lb), "s"(la) : "memory", "scc");
}
DEV void dma_i(u32 voff, v4u srd, u32 sb, u32 sa, u32 lb, u32 la) {
  u32 t;
  asm volatile("s_add_u32 m0, %5, %6\n\ts_add_u32 %0, %3, %4\n\tbuffer_load_dwordx4 %1, %2, %0 offen lds"
               : "=&s"(t) : "v"(voff), "s"(srd), "s"(sb), "i"(sa), "s"(lb), "i"(la) : "memory", "scc");
}
DEV void touch(u32 voff, v4u srd, u32 soff, u32 lds) {
  asm volatile("s_mov_b32 m0, %2\n\ts_nop 0\n\tbuffer_load_dword %0, %1, %3 offen lds" ::"v"(voff), "s"(srd), "s"(lds), "s"(soff) : "memory");
}
DEV void dma5c(u32 voff, v4u srd, u32 so, u32 lds) {
  u32 t;
  asm volatile("s_mov_b32 m0, %4\n\ts_add_u32 %0, %3, 0x1000\n\t"
               "buffer_load_dwordx4 %1, %2, %3 offen sc1 lds\n\t"
               "buffer_load_dwordx4 %1, %2, %3 offen offset:1024 sc1 lds\n\t"
               "buffer_load_dwordx4 %1, %2, %3 offen offset:2048 sc1 lds\n\t"
               "buffer_load_dwordx4 %1, %2, %3 offen offset:3072 sc1 lds\n\t"
               "s_add_u32 m0, %4, 0x1000\n\ts_nop 0\n\t"
               "buffer_load_dwordx4 %1, %2, %0 offen sc1 lds"
               : "=&s"(t) : "v"(voff), "s"(srd), "s"(so), "s"(lds) : "memory", "scc");
}
DEV u32 sgpr(u32 x) {
  u32 o;
  asm volatile("s_mov_b32 %0, %1" : "=s"(o) : "s"(__builtin_amdgcn_readfirstlane(x)));
  return o;
}
#define OPS(x) x = sgpr(x)
template <int N> DEV void waitv() { asm volatile("s_waitcnt vmcnt(%0)" ::"n"(N) : "memory"); }
#define WAITV(n) asm volatile("s_waitcnt vmcnt(" #n ")" ::: "memory")
DEV void bar() {
  asm volatile("" ::: "memory");
  __builtin_amdgcn_s_waitcnt(0xC07F);
  __builtin_amdgcn_s_barrier();
  asm volatile("" ::: "memory");
  SB();
}
DEV void bar_nw() {
  asm volatile("" ::: "memory");
  __builtin_amdgcn_s_barrier();
  asm volatile("" ::: "memory");
  SB();
}
DEV s4 trd(u32 a) { return __builtin_amdgcn_ds_read_tr16_b64_v4i16((LAS s4*)a); }
DEV s8 cat(s4 a, s4 b) { return __builtin_shufflevector(a, b, 0, 1, 2, 3, 4, 5, 6, 7); }
DEV s8 as8(v4u a) { return __builtin_bit_cast(s8, a); }
DEV s4 as4(v2u a) { return __builtin_bit_cast(s4, a); }
DEV f4 mfma16(s8 a, s8 b, f4 c) { return __builtin_amdgcn_mfma_f32_16x16x32_bf16(a, b, c, 0, 0, 0); }
DEV f4 mfma16h(s4 a, s4 b, f4 c) { return __builtin_amdgcn_mfma_f32_16x16x16bf16_1k(a, b, c, 0, 0, 0); }
DEV f16v mfma32(s8 a, s8 b, f16v c) { return __builtin_amdgcn_mfma_f32_32x32x16_bf16(a, b, c, 0, 0, 0); }
DEV u32 pk(float a, float b) { return __builtin_bit_cast(u32, __builtin_convertvector(f2{a, b}, bf2)); }
DEV float blo(u32 x) { return __builtin_bit_cast(float, x << 16); }
DEV float bhi(u32 x) { return __builtin_bit_cast(float, x & 0xFFFF0000u); }
DEV float dot2(u32 a, u32 b, float c) { return __builtin_amdgcn_fdot2_f32_bf16(__builtin_bit_cast(bf2, a), __builtin_bit_cast(bf2, b), c, false); }
DEV u32 fu(float x) { return __builtin_bit_cast(u32, x); }
DEV float uf(u32 x) { return __builtin_bit_cast(float, x); }
template <class T> DEV T lld(u32 a) { return *(LAS T*)a; }
template <class T> DEV void lst(u32 a, T v) { *(LAS T*)a = v; }
DEV v4u ld16c(rsrc_t r, u32 vo, u32 so) { return __builtin_amdgcn_raw_buffer_load_b128(r, vo, so, 16); }
DEV v2u ld8c(rsrc_t r, u32 vo, u32 so) { return __builtin_amdgcn_raw_buffer_load_b64(r, vo, so, 16); }
DEV u32 ld4c(rsrc_t r, u32 vo, u32 so) { return __builtin_amdgcn_raw_buffer_load_b32(r, vo, so, 16); }
DEV void st16c(v4u v, rsrc_t r, u32 vo, u32 so) {
  __builtin_amdgcn_raw_buffer_store_b128(v, r, vo, so, 16);
  asm volatile("s_nop 1" ::"v"(v) : "memory");
}
DEV void st16p(v4u v, rsrc_t r, u32 vo, u32 so) {
  __builtin_amdgcn_raw_buffer_store_b128(v, r, vo, so, 0);
  asm volatile("s_nop 1" ::"v"(v) : "memory");
}
DEV void st8c(v2u v, rsrc_t r, u32 vo, u32 so) { __builtin_amdgcn_raw_buffer_store_b64(v, r, vo, so, 16); }
DEV void st4c(u32 v, rsrc_t r, u32 vo, u32 so) { __builtin_amdgcn_raw_buffer_store_b32(v, r, vo, so, 16); }
DEV bool wall(bool c) { return __builtin_amdgcn_ballot_w64(!c) == 0ull; }
DEV u32 san(u32 x) { return x < 0xFFFFFFFEu ? x : 0xFFFFFFFEu; }
DEV u32 swap23(u32 k) { return (k & ~12u) | ((k & 4u) << 1) | ((k & 8u) >> 1); }

template <bool MAXOP, bool RCP> DEV void red4(float& a, float& b, float& c, float& d) {
  auto r1 = __builtin_amdgcn_permlane16_swap(fu(a), fu(b), false, false);
  auto r2 = __builtin_amdgcn_permlane16_swap(fu(c), fu(d), false, false);
  float ab = MAXOP ? __builtin_elementwise_maximum(uf(r1[0]), uf(r1[1])) : uf(r1[0]) + uf(r1[1]);
  float cd = MAXOP ? __builtin_elementwise_maximum(uf(r2[0]), uf(r2[1])) : uf(r2[0]) + uf(r2[1]);
  auto r3 = __builtin_amdgcn_permlane32_swap(fu(ab), fu(cd), false, false);
  float z = MAXOP ? __builtin_elementwise_maximum(uf(r3[0]), uf(r3[1])) : uf(r3[0]) + uf(r3[1]);
  if (RCP) z = __builtin_amdgcn_rcpf(z);
  auto g1 = __builtin_amdgcn_permlane32_swap(fu(z), fu(z), false, false);
  auto g2 = __builtin_amdgcn_permlane16_swap(g1[0], g1[0], false, false);
  auto g3 = __builtin_amdgcn_permlane16_swap(g1[1], g1[1], false, false);
  a = uf(g2[0]); b = uf(g2[1]); c = uf(g3[0]); d = uf(g3[1]);
}
DEV void red2(float& a, float& b) {
  auto r1 = __builtin_amdgcn_permlane16_swap(fu(a), fu(b), false, false);
  float ab = uf(r1[0]) + uf(r1[1]);
  auto r3 = __builtin_amdgcn_permlane32_swap(fu(ab), fu(ab), false, false);
  float z = uf(r3[0]) + uf(r3[1]);
  auto g2 = __builtin_amdgcn_permlane16_swap(fu(z), fu(z), false, false);
  a = uf(g2[0]); b = uf(g2[1]);
}

#define LAM __attribute__((always_inline)) NOPK
template <int V> struct IC { static constexpr int v = V; };
template <int A, int B, class F> DEV void sfor(F&& f) {
  if constexpr (A < B) { f(IC<A>{}); sfor<A + 1, B>(f); }
}
struct Phi { v2u pl[2], mi[2], ex; };
DEV float fmx(float a, float b) { return __builtin_elementwise_maximum(a, b); }
DEV float fmn(float a, float b) { return __builtin_elementwise_minimum(a, b); }
DEV void phi_mm(s8 b0, s8 b1, const s8 (&A)[4], f4 (&p)[3]) {
  const f4 z = {0.f, 0.f, 0.f, 0.f};
  p[2] = mfma16(A[2], b0, z);
  p[0] = mfma16(A[0], b0, z);
  p[1] = mfma16(A[1], b1, z);
  p[2] = mfma16(A[3], b1, p[2]);
}
DEV Phi phi_sm(const f4 (&pp)[3]) {
  const float p[2][5] = {{pp[0][0], pp[0][1], pp[0][2], pp[0][3], pp[2][0]}, {pp[1][0], pp[1][1], pp[1][2], pp[1][3], pp[2][1]}};
  float mx[2], nm[2];
#pragma unroll
  for (int h = 0; h < 2; ++h) {
    mx[h] = fmx(fmx(fmx(fmx(p[h][0], p[h][1]), p[h][2]), p[h][3]), p[h][4]);
    nm[h] = fmx(fmx(fmx(fmx(-p[h][0], -p[h][1]), -p[h][2]), -p[h][3]), -p[h][4]);
  }
  red4<true, false>(mx[0], nm[0], mx[1], nm[1]);
  float ep[2][5], em[2][5], sp[2], sm[2];
#pragma unroll
  for (int h = 0; h < 2; ++h) {
#pragma unroll
    for (int j = 0; j < 5; ++j) {
      ep[h][j] = __builtin_amdgcn_exp2f(p[h][j] - mx[h]);
      em[h][j] = __builtin_amdgcn_exp2f(-p[h][j] - nm[h]);
    }
    sp[h] = ((ep[h][0] + ep[h][1]) + (ep[h][2] + ep[h][3])) + ep[h][4];
    sm[h] = ((em[h][0] + em[h][1]) + (em[h][2] + em[h][3])) + em[h][4];
  }
  red4<false, true>(sp[0], sm[0], sp[1], sm[1]);
  Phi o;
#pragma unroll
  for (int h = 0; h < 2; ++h) {
    o.pl[h] = v2u{pk(ep[h][0] * sp[h], ep[h][1] * sp[h]), pk(ep[h][2] * sp[h], ep[h][3] * sp[h])};
    o.mi[h] = v2u{pk(em[h][0] * sm[h], em[h][1] * sm[h]), pk(em[h][2] * sm[h], em[h][3] * sm[h])};
    o.ex[h] = pk(ep[h][4] * sp[h], em[h][4] * sm[h]);
  }
  return o;
}
DEV v2u lnpk(f4 a, f4 cs, float nmean, float sc) {
  return v2u{pk(__builtin_fmaf(nmean, cs[0], a[0]) * sc, __builtin_fmaf(nmean, cs[1], a[1]) * sc),
             pk(__builtin_fmaf(nmean, cs[2], a[2]) * sc, __builtin_fmaf(nmean, cs[3], a[3]) * sc)};
}
DEV f4 lcs(u32 a) { return lld<f4>(a) + lld<f4>(a + 512); }
DEV u32 bsel(u32 m, u32 a, u32 b) { return (a & m) | (b & ~m); }
DEV void poll_flags(rsrc_t ws, u32 voff, bool active, v2u tag) {
#pragma unroll 1
  for (u32 s = 0; s < SPIN_CAP; ++s) {
    v2u v = tag;
    if (active) v = ld8c(ws, voff, 0);
    if (wall(v.x == tag.x && v.y == tag.y)) break;
    asm volatile("" ::: "memory");
  }
  asm volatile("" ::: "memory");
}
template <int SL = 1> DEV void lds_check(u32 lv, u32 a, u32 n) {
  u32 sv, cnt;
  asm volatile(
      "s_nop 0\n\tv_readfirstlane_b32 %0, %2\n\ts_cmp_ge_u32 %0, %4\n\ts_cbranch_scc1 2f\n\ts_mov_b32 %1, 0x400000\n"
      "1:\n\tv_mov_b32 %2, %3\n\tds_read_b32 %2, %2\n\ts_waitcnt lgkmcnt(0)\n\tv_readfirstlane_b32 %0, %2\n\ts_cmp_ge_u32 %0, %4\n\ts_cbranch_scc1 2f\n\t"
      "s_sleep %5\n\ts_sub_u32 %1, %1, 1\n\ts_cbranch_scc0 1b\n2:"
      : "=&s"(sv), "=&s"(cnt), "+v"(lv) : "s"(a), "s"(n), "n"(SL) : "memory", "scc");
}
template <int SL = 1> DEV void lds_wait(u32 a, u32 n = 4) { lds_check<SL>(0u, a, n); }
DEV void lds_inc(u32 a) {
  u32 t0, t1;
  asm volatile("v_mov_b32 %0, %2\n\tv_mov_b32 %1, 1\n\ts_mov_b64 exec, 1\n\tds_add_u32 %0, %1\n\ts_mov_b64 exec, -1" : "=&v"(t0), "=&v"(t1) : "s"(a) : "memory");
}
DEV void flag_store(v4u srd, v2u tag, u32 so) {
  v2u t;
  asm volatile("v_mov_b64 %0, %2\n\ts_mov_b64 exec, 1\n\tbuffer_store_dwordx2 %0, off, %1, %3 sc1\n\ts_mov_b64 exec, -1"
               : "=&v"(t) : "s"(srd), "s"(tag), "s"(so) : "memory");
}
DEV void poll_mask(rsrc_t ws, u32 fv, u32 base, v2u tag, v2u v) {
#pragma unroll 1
  for (u32 s = 0; s < SPIN_CAP; ++s) {
    fv = (v.x == tag.x && v.y == tag.y) ? OOB : fv;
    if (wall(fv == OOB)) break;
    asm volatile("" ::: "memory");
    v = ld8c(ws, fv, base);
  }
  asm volatile("" ::: "memory");
}
DEV void poll256(rsrc_t ws, u32 base, u32 stride, u32 lane, v2u tag) {
#pragma unroll 1
  for (u32 s = 0; s < SPIN_CAP; ++s) {
    v2u f[4];
    bool ok = true;
#pragma unroll
    for (int n = 0; n < 4; ++n) f[n] = ld8c(ws, lane * stride, base + n * 64 * stride);
#pragma unroll
    for (int n = 0; n < 4; ++n) ok = ok && f[n].x == tag.x && f[n].y == tag.y;
    if (wall(ok)) break;
    asm volatile("" ::: "memory");
  }
  asm volatile("" ::: "memory");
}
DEV u32 lane_id() {
  u32 l;
  asm volatile("v_mbcnt_lo_u32_b32 %0, -1, 0\n\tv_mbcnt_hi_u32_b32 %0, -1, %0" : "=v"(l));
  return l;
}
DEV u32 umax(u32 a, u32 b) { return a > b ? a : b; }
DEV u32 umax4(u32 m, v4u v) { return umax(umax(umax(umax(m, v.x), v.y), v.z), v.w); }

#define STAMP(i) do { if ((i) == 0 || (i) == 19 || ((STMASK >> (i)) & 1u)) stamp[i] = (u32)__builtin_amdgcn_s_memrealtime(); } while (0)

DEV void poll_level(rsrc_t ws, u32 voff, v2u tag, u32 level) {
#pragma unroll 1
  for (u32 s = 0; s < SPIN_CAP; ++s) {
    const v2u v = ld8c(ws, voff, level * 128);
    voff = (v.x == tag.x && v.y == tag.y) ? OOB : voff;
    if (wall(voff == OOB)) break;
    __builtin_amdgcn_s_sleep(LVS);
    asm volatile("" ::: "memory");
  }
  asm volatile("" ::: "memory");
}

template <bool TWO> DEV void kv_job(rsrc_t ws, u32 jb, u32 so) {
  const u32 lane = lane_id();
  const u32 q = lane >> 4, tq = (lane >> 2) & 3, tp = lane & 3;
  const u32 a1c = !TWO ? jb * 32 : jb < 6 ? (jb - 4) * 32 : 128;
  const u32 b1c = !TWO ? 160 + (jb >> 1) * 32 : jb == 6 ? 160 : 224;
  const u32 a2c = jb < 6 ? a1c + 64 : a1c;
  const u32 b2c = jb == 6 ? 192 : b1c;
  const u32 rb = L_IMG + (2 * (q >> 1) + 16 * (q & 1) + 4 * tq) * 264 + 8 * tp;
  constexpr int NO = TWO ? 4 : 2;
  u32 ra[4] = {rb + a1c, rb + b1c, rb + a2c, rb + b2c};
  u32 mlo = (jb != 6 && (lane & 1)) ? ~0u : 0u, mhi = (jb == 6 || (lane & 1)) ? ~0u : 0u, vl = lane * 16;
#pragma unroll
  for (int n = 0; n < NO; ++n) OPQ(ra[n]);
  OPQ(vl);
  if (TWO) { OPQ(mlo); OPQ(mhi); }
  bar();
  s8 fr[4][NO];
#pragma unroll
  for (int ks = 0; ks < 4; ++ks)
#pragma unroll
    for (int n = 0; n < NO; ++n) fr[ks][n] = cat(trd(ra[n] + ks * 32 * 264), trd(ra[n] + ks * 32 * 264 + 264));
  SB();
  f4 d1 = {0.f, 0.f, 0.f, 0.f}, d2 = d1;
#pragma unroll
  for (int ks = 0; ks < 4; ++ks) {
    d1 = mfma16(fr[ks][0], fr[ks][1], d1);
    if (TWO) d2 = mfma16(fr[ks][NO - 2], fr[ks][NO - 1], d2);
  }
  if (TWO) d1 = f4{uf(bsel(mlo, fu(d2[0]), fu(d1[0]))), uf(bsel(mlo, fu(d2[1]), fu(d1[1]))), uf(bsel(mhi, fu(d2[2]), fu(d1[2]))), uf(bsel(mhi, fu(d2[3]), fu(d1[3])))};
  st16c(v4u{san(fu(d1[0])), san(fu(d1[1])), san(fu(d1[2])), san(fu(d1[3]))}, ws, vl, so + jb * 1024);
}

typedef u64 v8q __attribute__((ext_vector_type(8)));
__global__ __launch_bounds__(512, 2) NOPK void fused_block(const u16* x_, const u16* wqkv_, const u16* wphi_, const u16* wo_, const u16* wg_, const u16* wu_,
                                                           const u16* wd_, u16* out_, char* wsp_) {
  v8q ka;
  u64 kw;
  asm volatile("s_load_dwordx16 %0, %2, 0x0\n\ts_load_dwordx2 %1, %2, 0x40" : "=&s"(ka), "=&s"(kw) : "s"(__builtin_amdgcn_kernarg_segment_ptr()));
  u64 pc;
  asm volatile("s_getpc_b64 %0" : "=s"(pc));
  SB();
  u32 stamp[32];
#pragma unroll
  for (int i = 0; i < 32; ++i) stamp[i] = 0;
  STAMP(0);
  const u32 wv = __builtin_amdgcn_readfirstlane(threadIdx.x >> 6);
  const u32 b = blockIdx.x, xcd = b & 7, idx = b >> 3;
  const u32 team = (xcd >> 1) * 4 + (idx >> 3), rank = ((idx & 4) << 1) | ((xcd & 1) << 2) | (idx & 3);
  const u32 bid = team * 16 + rank, hq = rank & 7, th = rank >> 3;
  const bool u7 = rank < 12;
  const f4 z4 = {0.f, 0.f, 0.f, 0.f};
#define DUMP() STAMP(19); if (wv == 0) { const u32 d_ = stamp[0] - stamp[0]; while ((u32)__builtin_amdgcn_s_memrealtime() - stamp[19] < d_) __builtin_amdgcn_s_sleep(1); }
#define GET_ARGS()                                                                                         \
  SB();                                                                                                    \
  asm volatile("s_waitcnt lgkmcnt(0)" ::: "memory");                                                       \
  SB();                                                                                                    \
  asm volatile("" : "+s"(ka), "+s"(kw));                                                                   \
  char* const wsp = (char*)kw;                                                                             \
  const rsrc_t ws = mk_rsrc(wsp, W_END);                                                                   \
  const v4u sws = mk_srd(wsp, W_END);
#define GET_EPOCH()                                                                                        \
  asm volatile("s_waitcnt lgkmcnt(0)" ::: "memory");                                                       \
  SB();                                                                                                    \
  asm volatile("" : "+s"(ep));                                                                             \
  ep = (ep & ~15ull) + 16ull;                                                                              \
  const v2u tag = {(u32)ep, (u32)(ep >> 32)};

  if (wv >= 4) {
    const u32 w = wv - 4;
    const u32 lane = lane_id();
    const u32 q = lane >> 4, i16 = lane & 15;
    {
      const v4u sc = mk_srd((const void*)(pc & ~255ull), CODE_BYTES);
      if (w == 0) asm volatile("s_mov_b32 m0, %2\n\ts_nop 0\n\tbuffer_load_dword %0, %1, 0 offen lds" ::"v"(lane < 16 ? (lane * 32 + idx) * 128 : OOB), "s"(sc), "s"(L_CS3) : "memory");
    }
    const u32 vx0 = (lane >> 3) * 1280 + (((lane & 7) ^ (lane >> 4)) << 4), vx1 = (vx0 ^ 64) + 8 * 1280;
    u32 xs0 = (team * 128 + w * 32) * 1280, xl0 = L_B + w * 12288;
    OPS(xs0); OPS(xl0);
    GET_ARGS();
    const u16* const x = (const u16*)ka[0];
    u16* const out = (u16*)ka[7];
    const v4u sx = mk_srd(x, SEQ * DM * 2);
    bar_nw();
    u64 ep;
    asm volatile("s_load_dwordx2 %0, %1, 0x0" : "=s"(ep) : "s"(wsp));
    auto stagex = [&](int c) LAM {
      dma_i(vx0, sx, xs0, c * 128, xl0, (c % 3) * 4096);
      dma_i(vx1, sx, xs0, c * 128, xl0, (c % 3) * 4096 + 1024);
      dma_i(vx0, sx, xs0, c * 128 + 16 * 1280, xl0, (c % 3) * 4096 + 2048);
      dma_i(vx1, sx, xs0, c * 128 + 16 * 1280, xl0, (c % 3) * 4096 + 3072);
    };
    touch(lane < 20 ? lane * 128 : OOB, sx, team * (128 * 1280) + (rank * 4 + w) * 2560, L_CS3);
    stagex(0); stagex(1);
    stagex(2);
    u32 tvo[2], ts0[2];
    v4u tsp[2];
#pragma unroll
    for (int e = 0; e < 2; ++e) {
      const u32 piece = w * 256 + b + e * 1024;
      const bool isg = piece < 1080;
      const u32 p2 = isg ? piece : piece - 1080;
      const u32 pk_ = (p2 * (isg ? 1214u : 6554u)) >> 16, pcb = p2 - pk_ * (isg ? 54 : 10), rs = isg ? 3456 : 1280;
      tsp[e] = mk_srd((const void*)(!isg ? ka[6] : pcb >= 27 ? ka[5] : ka[4]), piece < 1620 ? DM * FF * 2 : 0u);
      tsp[e].x = sgpr(tsp[e].x); tsp[e].y = sgpr(tsp[e].y); tsp[e].z = sgpr(tsp[e].z);
      ts0[e] = sgpr(pk_ * 32 * rs + (pcb >= 27 ? pcb - 27 : pcb) * 128);
      tvo[e] = (lane >> 1) * rs + (lane & 1) * 64;
      OPQ(tvo[e]);
    }
    v4u xa[2][20];
    float nm1[2], rs1[2];
    {
      v4u onesv = {0x3F803F80u, 0x3F803F80u, 0x3F803F80u, 0x3F803F80u};
      OPQ(onesv);
      const s8 ones = as8(onesv);
      f4 as[2] = {z4, z4}, ag[2] = {z4, z4};
      u32 ax0 = L_B + w * 12288 + i16 * 128 + ((q ^ ((i16 >> 1) & 7)) << 4), ax1 = ax0 ^ 64;
      OPQ(ax0); OPQ(ax1);
      sfor<0, 11>([&](auto cc_) LAM {
        constexpr int c = decltype(cc_)::v;
        if constexpr (c < 10) waitv<(c < 7 ? 8 : (c == 7 ? 8 : c == 8 ? 4 : 0) + (RT ? 2 : 0))>();
        __builtin_amdgcn_s_waitcnt(0xC07F);
        SB();
        if constexpr (c < 10) {
#pragma unroll
          for (int t = 0; t < 2; ++t) { xa[t][2 * c] = lld<v4u>(ax0 + (c % 3) * 4096 + t * 2048); xa[t][2 * c + 1] = lld<v4u>(ax1 + (c % 3) * 4096 + t * 2048); }
        }
        if constexpr (c < 7) { __builtin_amdgcn_s_waitcnt(0xC07F); SB(); stagex(c + 3); }
        if constexpr (RT && c == 6) {
#pragma unroll
          for (int e = 0; e < 2; ++e) touch(tvo[e], tsp[e], ts0[e], L_CS3);
        }
        if constexpr (c == 10) lds_inc(S_XD);
        if constexpr (c >= 1) {
#pragma unroll
          for (int k = 2 * c - 2; k < 2 * c; ++k)
#pragma unroll
            for (int t = 0; t < 2; ++t) { as[t] = mfma16(ones, as8(xa[t][k]), as[t]); ag[t] = mfma16(as8(xa[t][k]), as8(xa[t][k]), ag[t]); }
        }
        SB();
      });
      STAMP(23);
      float g[2];
#pragma unroll
      for (int t = 0; t < 2; ++t) {
        u32 v = 0;
#pragma unroll
        for (int r = 0; r < 4; ++r) v |= fu(ag[t][r]) & (i16 == 4 * q + r ? ~0u : 0u);
        g[t] = uf(v);
      }
      red2(g[0], g[1]);
#pragma unroll
      for (int t = 0; t < 2; ++t) {
        nm1[t] = -as[t][0] * (1.0f / DM);
        rs1[t] = __builtin_amdgcn_rsqf(__builtin_fmaxf(__builtin_fmaf(-nm1[t], nm1[t], g[t] * (1.0f / DM)), 0.f) + 1e-5f);
        OPQ(nm1[t]); OPQ(rs1[t]);
      }
    }
    GET_EPOCH();
    __builtin_amdgcn_s_setprio(2);
    const float L2E = 1.4426950408889634f;
    {
      f4 acc[2][5] = {{z4, z4, z4, z4, z4}, {z4, z4, z4, z4, z4}};
      u32 bw[2] = {L_A + lane * 16, L_A + 51200 + lane * 16};
      OPQ(bw[0]); OPQ(bw[1]);
      constexpr int KD = KDEP;
      s8 kf[KD + 1];
      u32 lv;
      lds_wait(S_FA);
      { v2u tg = tag; OPQ(tg); st8c(tg, ws, lane_id() << 26, w == 0 ? W_FB + bid * 32 : OOB); }
      lds_wait(S_L1);
      STAMP(25);
#pragma unroll
      for (int f = 0; f < KD; ++f) kf[f] = lld<s8>(bw[0] + f * 1024);
      SB();
      sfor<0, 100>([&](auto gc) LAM {
        constexpr int g = decltype(gc)::v, gn = g + KD;
        if constexpr (gn < 100 && gn % 20 == 0) {
          lds_check(lv, S_L1 + 4 * (gn / 20), 4);

        }
        if constexpr (gn < 100) kf[gn % (KD + 1)] = lld<s8>(bw[gn / 50] + (gn % 50) * 1024);
        if constexpr (gn == 44) lds_inc(S_CS);
        if constexpr (gn % 20 == 14) lv = *(volatile LAS u32*)(gn < 80 ? S_L1 + 4 * (gn / 20 + 1) : S_CS);
        acc[0][g % 5] = mfma16(kf[g % (KD + 1)], as8(xa[0][g / 5]), acc[0][g % 5]);
        acc[1][g % 5] = mfma16(kf[g % (KD + 1)], as8(xa[1][g / 5]), acc[1][g % 5]);
        SB();
      });
      lds_inc(S_KVD);
      lds_check(lv, S_CS, 8);
      STAMP(2);
      const u32 cb = L_CS1 + 160 + q * 16;
      const u32 mq = q < 2 ? ~0u : 0u;
      f4 pp[2][3];
      {
        const f4 c0 = lcs(cb), c1 = lcs(cb + 80), c2 = lcs(L_CS1 + 224 + q * 80);
        const s8 A[4] = {lld<s8>(L_WPHI + lane * 16), lld<s8>(L_WPHI + 1024 + lane * 16), lld<s8>(L_PH2 + lane * 16), lld<s8>(L_PH2 + 1024 + lane * 16)};
#pragma unroll
        for (int t = 0; t < 2; ++t) {
          const float sk = rs1[t] * L2E;
          const v2u k0 = lnpk(acc[t][0], c0, nm1[t], sk), k1 = lnpk(acc[t][1], c1, nm1[t], sk), k2 = lnpk(acc[t][2], c2, nm1[t], uf(bsel(mq, fu(sk), fu(rs1[t]))));
          phi_mm(as8(v4u{k0.x, k0.y, k2.x, k2.y}), as8(v4u{k1.x, k1.y, k2.x, k2.y}), A, pp[t]);
          const u32 e0 = bsel(mq, 0x3F803F80u, k2.x), e1 = bsel(mq, 0x3F803F80u, k2.y), eb = L_IMG + (w * 32 + t * 16 + i16) * 264 + 224 + (q ^ 2) * 2;
          lst<u16>(eb, (u16)e0); lst<u16>(eb + 8, (u16)(e0 >> 16)); lst<u16>(eb + 16, (u16)e1); lst<u16>(eb + 24, (u16)(e1 >> 16));
          SB();
        }
      }
      {
        const f4 c3 = lcs(cb + 160), c4 = lcs(cb + 240);
#pragma unroll
        for (int t = 0; t < 2; ++t) {
          const u32 ib = L_IMG + (w * 32 + t * 16 + i16) * 264;
          lst<v2u>(ib + q * 8 + 160, lnpk(acc[t][3], c3, nm1[t], rs1[t]));
          lst<v2u>(ib + q * 8 + 192, lnpk(acc[t][4], c4, nm1[t], rs1[t]));
        }
      }
      SB();
#pragma unroll
      for (int t = 0; t < 2; ++t) {
        const Phi ph = phi_sm(pp[t]);
        const u32 ib = L_IMG + (w * 32 + t * 16 + i16) * 264 + q * 8;
        lst<v2u>(ib + 0, ph.pl[0]); lst<v2u>(ib + 32, ph.mi[0]); lst<v2u>(ib + 64, ph.pl[1]); lst<v2u>(ib + 96, ph.mi[1]);
        lst<v2u>(ib + 128, ph.ex);
      }
    }
    STAMP(3);
    kv_job<false>(ws, w, W_PART + (rank * 16 + team) * 8192);
    STAMP(4);
    bar_nw();
    {
      const u32 lane = lane_id(), q = lane >> 4, i16 = lane & 15;
      f4 qa[2][3] = {{z4, z4, z4}, {z4, z4, z4}};
      u32 bq = L_B + lane * 16, bq2 = L_B + 4096 + ((lane & 7) + 16 * q) * 16;
      OPQ(bq); OPQ(bq2);
      constexpr int QD = 4;
      s8 qf[QD + 1];
      auto ldq = [&](int f) LAM {
        const int t = f % 3, ks = f / 3;
        qf[f % (QD + 1)] = lld<s8>(t == 2 ? bq2 + (ks >> 1) * 5120 + (ks & 1) * 128 : bq + (ks >> 1) * 5120 + ((ks & 1) * 2 + t) * 1024);
      };
#pragma unroll
      for (int f = 0; f < QD; ++f) ldq(f);
      SB();
#pragma unroll
      for (int f = 0; f < 60; ++f) {
        if (f + QD < 60) ldq(f + QD);
        qa[0][f % 3] = mfma16(qf[f % (QD + 1)], as8(xa[0][f / 3]), qa[0][f % 3]);
        qa[1][f % 3] = mfma16(qf[f % (QD + 1)], as8(xa[1][f / 3]), qa[1][f % 3]);
        SB();
      }
      STAMP(5);
      const f4 c0 = lcs(L_CS1 + q * 16), c1 = lcs(L_CS1 + 80 + q * 16), c2 = lcs(L_CS1 + 64 + (q & 1) * 80);
      const s8 A[4] = {lld<s8>(L_WPHI + lane * 16), lld<s8>(L_WPHI + 1024 + lane * 16), lld<s8>(L_PH2 + lane * 16), lld<s8>(L_PH2 + 1024 + lane * 16)};
      f4 pp[2][3];
      Phi pq[2];
#pragma unroll
      for (int t = 0; t < 2; ++t) {
        const float sk = rs1[t] * L2E;
        const v2u k0 = lnpk(qa[t][0], c0, nm1[t], sk), k1 = lnpk(qa[t][1], c1, nm1[t], sk), k2 = lnpk(qa[t][2], c2, nm1[t], sk);
        phi_mm(as8(v4u{k0.x, k0.y, k2.x, k2.y}), as8(v4u{k1.x, k1.y, k2.x, k2.y}), A, pp[t]);
      }
      v4u b0[2], b1[2], bx[2];
#pragma unroll
      for (int t = 0; t < 2; ++t) {
        pq[t] = phi_sm(pp[t]);
        b0[t] = v4u{pq[t].pl[0].x, pq[t].pl[0].y, pq[t].mi[0].x, pq[t].mi[0].y};
        b1[t] = v4u{pq[t].pl[1].x, pq[t].pl[1].y, pq[t].mi[1].x, pq[t].mi[1].y};
        bx[t] = v4u{pq[t].ex.x, pq[t].ex.y, 0u, 0u};
        OPQ(b0[t]); OPQ(b1[t]); OPQ(bx[t]);
      }
      u32 mo = (lane & 1) ? ~0u : 0u, ve = i16 * 16 + q * 4;
      OPQ(mo); OPQ(ve);
      STAMP(7);
      bar_nw();
      STAMP(8);
      v2u it[8];
#pragma unroll
      for (int n = 0; n < 8; ++n) it[n] = lld<v2u>(L_KV + n * 512 + lane * 8);
      const s8 ae = as8(v4u{it[4].x & ~mo, it[4].y & ~mo, it[5].x & ~mo, it[5].y & ~mo}), ao = as8(v4u{it[4].x & mo, it[4].y & mo, it[5].x & mo, it[5].y & mo});
      const s8 ax = as8(v4u{it[7].x & ~mo, it[7].y & mo, 0u, 0u});
      f4 nx[2], n0[2], n1[2];
#pragma unroll
      for (int t = 0; t < 2; ++t) {
        nx[t] = mfma16(ae, as8(b0[t]), z4);
        nx[t] = mfma16(ao, as8(b1[t]), nx[t]);
        nx[t] = mfma16(ax, as8(bx[t]), nx[t]);
      }
#pragma unroll
      for (int t = 0; t < 2; ++t) {
        n0[t] = mfma16(as8(v4u{it[0].x, it[0].y, it[1].x, it[1].y}), as8(b0[t]), z4);
        n1[t] = mfma16(as8(v4u{it[2].x, it[2].y, it[3].x, it[3].y}), as8(b1[t]), z4);
        n0[t] = mfma16(as8(v4u{it[6].x, 0u, 0u, 0u}), as8(bx[t]), n0[t]);
        n1[t] = mfma16(as8(v4u{0u, it[6].y, 0u, 0u}), as8(bx[t]), n1[t]);
      }
#pragma unroll
      for (int t = 0; t < 2; ++t) {
        const float r0 = __builtin_amdgcn_rcpf(nx[t][2] + 1e-6f), r1 = __builtin_amdgcn_rcpf(nx[t][3] + 1e-6f);
        const u32 ob = W_O + (team * 8 + 2 * w + t) * 20480;
        st4c(pk(nx[t][0] * r0, nx[t][1] * r1), ws, ve, ob + 16384 + rank * 256);
        st16c(v4u{pk(n0[t][0] * r0, n0[t][1] * r0), pk(n0[t][2] * r0, n0[t][3] * r0), pk(n1[t][0] * r1, n1[t][1] * r1), pk(n1[t][2] * r1, n1[t][3] * r1)}, ws,
              lane * 16, ob + rank * 1024);
      }
    }
    {
      const u32 lane = lane_id(), q = lane >> 4, i16 = lane & 15;
      const u32 tk = th * 64 + w * 16 + i16;
      const u32 xo = tk * 1280 + q * 16, xso = team * 128 * 1280 + hq * 160;
      const rsrc_t rx = mk_rsrc(x, SEQ * DM * 2);
      const v4u xr0 = __builtin_amdgcn_raw_buffer_load_b128(rx, xo, xso, 0), xr1 = __builtin_amdgcn_raw_buffer_load_b128(rx, xo + 64, xso, 0);
      const v2u xr2 = __builtin_amdgcn_raw_buffer_load_b64(rx, tk * 1280 + 128 + q * 8, xso, 0);
      constexpr int OD = 6;
      s8 wf[OD][5];
      u32 bo[2] = {L_WO + lane * 16, L_WO + 51200 + lane * 16};
      OPQ(bo[0]); OPQ(bo[1]);
      const u32 oso = W_O + (team * 8 + th * 4 + w) * 20480;
      auto ldo1 = [&](int ks, int t) LAM { wf[ks % OD][t] = lld<s8>(bo[ks / 10] + (ks % 10) * 5120 + t * 1024); };
#pragma unroll
      for (int ks = 0; ks < OD; ++ks)
#pragma unroll
        for (int t = 0; t < 5; ++t) ldo1(ks, t);
      WAITV(3);
      flag_store(sws, tag, W_FS2 + (bid * 4 + w) * 128);
      lds_inc(S_OP);
      STAMP(9);
      f4 ha[5] = {f4{blo(xr0.x), bhi(xr0.x), blo(xr0.y), bhi(xr0.y)}, f4{blo(xr0.z), bhi(xr0.z), blo(xr0.w), bhi(xr0.w)}, f4{blo(xr1.x), bhi(xr1.x), blo(xr1.y), bhi(xr1.y)},
                  f4{blo(xr1.z), bhi(xr1.z), blo(xr1.w), bhi(xr1.w)}, f4{blo(xr2.x), bhi(xr2.x), blo(xr2.y), bhi(xr2.y)}};
#pragma unroll
      for (int t = 0; t < 5; ++t) OPQ(ha[t]);
      __builtin_amdgcn_s_waitcnt(0xC07F);
      __builtin_amdgcn_s_sleep(PD2);
      SB();
      poll_flags(ws, W_FS2 + ((team * 16 + lane) * 4 + th * 2 + (w >> 1)) * 128, lane < 16, tag);
      STAMP(10);
      v4u of[20];
      constexpr int PD = 12;
      auto ldof = [&](int ks) LAM { of[ks] = ld16c(ws, lane * 16 + (ks & 3) * 1024, oso + (ks >> 2) * 4096); };
#pragma unroll
      for (int ks = 0; ks < PD; ++ks) ldof(ks);
      SB();
#pragma unroll
      for (int ks = 0; ks < 20; ++ks) {
#pragma unroll
        for (int t = 0; t < 5; ++t) {
          ha[t] = mfma16(wf[ks % OD][t], as8(of[ks]), ha[t]);
          if (t == 0 && ks + PD < 20) ldof(ks + PD);
          if (t == 1 && ks + PD == 19) lds_inc(S_OL);
          if (ks + OD < 20) ldo1(ks + OD, t);
          SB();
        }
      }
      STAMP(11);
      v4u hc[2];
#pragma unroll
      for (int m = 0; m < 2; ++m)
        hc[m] = v4u{pk(ha[2 * m][0], ha[2 * m][1]), pk(ha[2 * m][2], ha[2 * m][3]), pk(ha[2 * m + 1][0], ha[2 * m + 1][1]), pk(ha[2 * m + 1][2], ha[2 * m + 1][3])};
      const v2u ht = {pk(ha[4][0], ha[4][1]), pk(ha[4][2], ha[4][3])};
      const u32 hb = W_H + (team * 4 + th * 2 + (w >> 1)) * 40960 + hq * 5120;
      const u32 t32 = (w & 1) * 16 + i16;
      st16c(hc[0], ws, (q >> 1) * 1024 + (t32 + 32 * (q & 1)) * 16, hb);
      st16c(hc[1], ws, (q >> 1) * 1024 + (t32 + 32 * (q & 1)) * 16, hb + 2048);
      st8c(ht, ws, (t32 + 32 * (q >> 1)) * 16 + 8 * (q & 1), hb + 4096);
      float a[2] = {0.f, 0.f}, c[2] = {0.f, 0.f};
#pragma unroll
      for (int m = 0; m < 2; ++m) {
#pragma unroll
        for (int j = 0; j < 4; ++j) { a[m] = dot2(hc[m][j], 0x3F803F80u, a[m]); c[m] = dot2(hc[m][j], hc[m][j], c[m]); }
        a[m] = dot2(ht[m], 0x3F803F80u, a[m]); c[m] = dot2(ht[m], ht[m], c[m]);
      }
      float a2 = a[0] + a[1], c2 = c[0] + c[1];
      red2(a2, c2);
      st8c(v2u{fu(a2), fu(c2)}, ws, q == 0 ? tk * 8 : OOB, W_STAT + (team * 8 + hq) * 1024);
      lst<v4u>(L_HST + w * 2560 + lane * 16, hc[0]);
      lst<v4u>(L_HST + w * 2560 + 1024 + lane * 16, hc[1]);
      lst<v2u>(L_HST + w * 2560 + 2048 + lane * 8, ht);
    }
    WAITV(0);
    flag_store(sws, tag, W_FS3 + (bid * 4 + w) * 128);
    lds_inc(S_WOC);
    STAMP(12);
    __builtin_amdgcn_s_sleep(PD2);
    const u32 lane3 = lane_id();
#define lane lane3
    poll_flags(ws, W_FS3 + ((team * 16 + (w >> 1) * 8 + (lane >> 1)) * 4 + 2 * (w & 1) + (lane & 1)) * 128, lane < 16, tag);
    STAMP(13);
    v4u wl[3][5];
    u32 wlv;
    auto ldwl = [&](int k) LAM {
      const int i = 11 + k / 5;
      if (k == 0) wlv = lane_id() * 16;
      wl[k / 5][k % 5] = ld16c(ws, ((i >= 12 && w == 3) ? OOB : wlv) + (k % 5) * 1024, W_WD + (hq * 54 + 2 * w + (i >> 1) * 8 + (i & 1)) * 5120);
    };
    {
      const u32 hh = lane >> 5, t32 = lane & 31;
      v2u sv[4];
#pragma unroll
      for (int n = 0; n < 4; ++n) sv[n] = ld8c(ws, (hh * 4 + n) * 1024 + t32 * 8, W_STAT + team * 8192 + w * 256);
      v4u hf[40];
      constexpr int HD = 12;
      const u32 hso = W_H + (team * 4 + w) * 40960;
      auto ldh = [&](int s) LAM { hf[s] = ld16c(ws, lane * 16 + (s & 3) * 1024, hso + (s >> 2) * 4096); };
#pragma unroll
      for (int s = 0; s < HD; ++s) ldh(s);
      SB();
      lds_wait(L_LAND, 4);
      u32 lb[3] = {L_HS + lane * 16, L_HS + 3 * HSLOT + lane * 16, L_HS + 6 * HSLOT + lane * 16};
      OPQ(lb[0]); OPQ(lb[1]); OPQ(lb[2]);
      auto ldw = [&](int n, int s) LAM -> s8 {
        const int hs = (2 * n + s / 20 + 5) % 7;
        return lld<s8>(lb[hs / 3] + (hs % 3) * HSLOT + (s % 20) * 1024);
      };
      auto ldcs = [&](int n) LAM -> f16v {
        const u32 csb = L_CS3 + (lane_id() & 32);
        const f4 g0 = lld<f4>(csb + n * 128), g1 = lld<f4>(csb + n * 128 + 16), u0 = lld<f4>(csb + n * 128 + 64), u1 = lld<f4>(csb + n * 128 + 80);
        return __builtin_shufflevector(__builtin_shufflevector(g0, g1, 0, 1, 2, 3, 4, 5, 6, 7), __builtin_shufflevector(u0, u1, 0, 1, 2, 3, 4, 5, 6, 7), 0, 1, 2, 3, 4,
                                       5, 6, 7, 8, 9, 10, 11, 12, 13, 14, 15);
      };
      f16v ac[3];
      ac[0] = ldcs(0); ac[1] = ldcs(1); ac[2] = ldcs(2);
      constexpr int RD = RDEP;
      s8 rg[RD];
#pragma unroll
      for (int f = 0; f < RD; ++f) rg[f] = ldw(f & 1, f >> 1);
      SB();
      float nmean, cK1, cK2, cR;
      {
        const float a = (uf(sv[0].x) + uf(sv[1].x)) + (uf(sv[2].x) + uf(sv[3].x)), c = (uf(sv[0].y) + uf(sv[1].y)) + (uf(sv[2].y) + uf(sv[3].y));
        auto r = __builtin_amdgcn_permlane32_swap(fu(a), fu(a), false, false);
        auto r2 = __builtin_amdgcn_permlane32_swap(fu(c), fu(c), false, false);
        nmean = -(uf(r[0]) + uf(r[1])) * (1.0f / DM);
        cR = __builtin_fmaxf(__builtin_fmaf(-nmean, nmean, (uf(r2[0]) + uf(r2[1])) * (1.0f / DM)), 0.f) + 1e-5f;
        const float rstd = __builtin_amdgcn_rsqf(cR), k1 = -2.0f * 0.7978845608028654f * 1.4426950408889634f;
        cK1 = k1 * rstd; cK2 = cK1 * 0.044715f * rstd * rstd;
      }
#pragma unroll
      for (int j = 0; j < 16; ++j) { ac[0][j] *= nmean; ac[1][j] *= nmean; }
      SB();
      u32 lv;
#pragma unroll
      for (int f = 0; f < 80; ++f) {
        ac[f & 1] = mfma32(rg[f % RD], as8(hf[f >> 1]), ac[f & 1]);
        const int fn = f + RD;
        rg[f % RD] = fn < 80 ? ldw(fn & 1, fn >> 1) : ldw(2, fn - 80);
        if (f >= 8 && f < 24) ac[2][f - 8] *= nmean;
        if ((f & 1) == 0 && (f >> 1) + HD < 40) ldh((f >> 1) + HD);
        if (f == 2 * (39 - HD) + 1) lds_inc(S_HL);
        if (f == 80 - RD) lds_inc(L_CONS + 4);
        SB();
      }
      OPQ(cK1); OPQ(cK2); OPQ(cR);
      STAMP(14);
      const u32 gvo = (t32 >> 4) * 54 * 1024 + ((t32 & 15) + 16 * hh) * 16 + (rank & 1) * 512;
      const u32 gso = W_G + (team * 8 + 2 * w) * 54 * 1024 + (rank >> 1) * 1024;
      const u32 fso = W_FS4 + (bid * 4 + w) * 1024;
      float tv, au, pu, pr, ov[2];
      v4u gp;
      auto gstep = [&](const f16v& a, int e) LAM {
        const int j = e / 3, m = e % 3;
        if (m == 0 && j < 8) { tv = a[j] * a[j]; tv = __builtin_fmaf(tv, cK2, cK1); tv = tv * a[j]; au = a[j] * a[8 + j]; }
        if (m == 1) tv = __builtin_amdgcn_exp2f(tv);
        if ((m == 1 && j > 0) || e == 24) { ov[(j - 1) & 1] = pu * pr; if (((j - 1) & 1) == 1) gp[(j - 1) >> 1] = pk(ov[0], ov[1]); }
        if (m == 2) { pr = __builtin_amdgcn_rcpf(__builtin_fmaf(tv, cR, cR)); pu = au; }
      };
      auto pass = [&](auto nc) LAM {
        constexpr int n = decltype(nc)::v;
        sfor<0, 40>([&](auto sc) LAM {
          constexpr int s = decltype(sc)::v, g = (n - 2) * 40 + s, sn = s + RD;
          constexpr int G[6] = {1, 31, 61, 91, 122, 161}, I[4] = {26, 56, 86, 150};
          ac[n % 3] = mfma32(rg[s % RD], as8(hf[s]), ac[n % 3]);
          if constexpr (sn < 40) rg[s % RD] = ldw(n, sn);
          else if constexpr (n < 6) rg[s % RD] = ldw(n + 1, sn - 40);
          sfor<0, 6>([&](auto uc) LAM {
            constexpr int u = decltype(uc)::v;
            if constexpr (g >= G[u] && g <= G[u] + 24 && (!(ABL & 1) || g - G[u] < 6 || g - G[u] == 24)) gstep(ac[u % 3], g - G[u]);
            if constexpr (g == G[u] + 24) {
              if constexpr (u >= 2) { if (!(ABL & 2)) WAITV(0); flag_store(sws, tag, fso + u * 128); }
              st16c(gp, ws, gvo, gso + u * 8192);
            }
          });
          sfor<0, 4>([&](auto kc) LAM {
            constexpr int k = decltype(kc)::v;
            if constexpr (g == I[k] - 3) ac[k % 3] = ldcs(3 + k);
            if constexpr (g > I[k] && g <= I[k] + 4) {
#pragma unroll
              for (int j = 0; j < 4; ++j) ac[k % 3][4 * (g - I[k] - 1) + j] *= nmean;
            }
          });
          if constexpr (WLP && n == 6 && s >= 25) ldwl(s - 25);
          if constexpr (s == 40 - RD) lds_inc(L_CONS + 4 * n);
          if constexpr (s == 8) lv = *(volatile LAS u32*)(L_LAND + 4 * (n - 1));
          if constexpr (s == 14 && !(ABL & 4)) lds_check(lv, L_LAND + 4 * (n - 1), 4);
          SB();
        });
      };
      pass(IC<2>{}); STAMP(26); pass(IC<3>{}); STAMP(27); pass(IC<4>{}); STAMP(28); pass(IC<5>{}); STAMP(29);
      pass(IC<6>{});
      STAMP(15);
#pragma unroll
      for (int e = 0; e < 25; ++e) gstep(ac[0], e);
      st16c(gp, ws, u7 ? gvo : OOB, gso + 6 * 8192);
      if (WLP) WAITV(16); else WAITV(1);
      flag_store(sws, tag, fso + 6 * 128);
    }
#undef lane
    STAMP(30);
    {
      const u32 ln = lane_id(), vl = ln * 16;
      const u32 fv = W_FS4 + ((team * 16 + 4 * w + (ln >> 4)) * 4 + 2 * th + ((ln >> 3) & 1)) * 1024;
      const u32 gso = W_G + (team * 8 + th * 4) * 54 * 1024 + 2 * w * 1024;
      const u32 vz = w < 3 ? vl : OOB;
      u32 gsr[4];
#pragma unroll
      for (int r = 0; r < 4; ++r) gsr[r] = sgpr(gso + ((r + w + 1) & 3) * (54 * 1024));
      v4u wf[6][5], gf[6][4];
      auto ldg = [&](int i) LAM {
#pragma unroll
        for (int r = 0; r < 4; ++r) gf[i - 8][r] = ld16c(ws, i >= 12 ? vz : vl, gsr[r] + ((i >> 1) * 8 + (i & 1)) * 1024);
        SB();
      };
      v2u fl = ld8c(ws, fv, 5 * 128);
      if (!WLP) sfor<0, 5>([&](auto kc) LAM { ldwl(decltype(kc)::v); });
      if (WLP) WAITV(2); else WAITV(7);
      flag_store(sws, tag, W_FS4 + (bid * 4 + w) * 1024 + 7 * 128);
      STAMP(16);
      if (!wall(fl.x == tag.x && fl.y == tag.y)) poll_level(ws, fv, tag, 5);
      asm volatile("" ::: "memory");
      ldg(8); ldg(9);
      fl = ld8c(ws, fv, 6 * 128);
      if (!WLP) sfor<5, 15>([&](auto kc) LAM { ldwl(decltype(kc)::v); });
#pragma unroll
      for (int i = 11; i < 14; ++i)
#pragma unroll
        for (int t = 0; t < 5; ++t) wf[i - 8][t] = wl[i - 11][t];
      u32 sb = L_HS + 2 * HSLOT + w * 5120 + vl;
      OPQ(sb);
      lds_wait(S_SP);
#pragma unroll
      for (int i = 8; i < 10; ++i)
#pragma unroll
        for (int t = 0; t < 5; ++t) wf[i - 8][t] = lld<v4u>(sb + (i - 8) * HSLOT + t * 1024);
      if (!wall(fl.x == tag.x && fl.y == tag.y)) poll_level(ws, fv, tag, 6);
      asm volatile("" ::: "memory");
      STAMP(31);
      ldg(10);
      f4 da[4][5];
      auto step = [&](int i) LAM {
#pragma unroll
        for (int m = 0; m < 20; ++m) da[m / 5][m % 5] = mfma16(as8(wf[i - 8][m % 5]), as8(gf[i - 8][m / 5]), i == 8 ? z4 : da[m / 5][m % 5]);
        SB();
      };
      step(8);
      ldg(11);
      fl = ld8c(ws, fv, 7 * 128);
#pragma unroll
      for (int t = 0; t < 5; ++t) wf[2][t] = lld<v4u>(sb + 2 * HSLOT + t * 1024);
      SB();
      step(9);
      if (w < 3 && !wall(fl.x == tag.x && fl.y == tag.y)) poll_level(ws, fv, tag, 7);
      asm volatile("" ::: "memory");
      STAMP(1);
      ldg(12); ldg(13);
      step(10); step(11);
      STAMP(6);
      step(12);
#pragma unroll
      for (int m = 0; m < 21; ++m) {
        if (m < 20) da[m / 5][m % 5] = mfma16(as8(wf[5][m % 5]), as8(gf[5][m / 5]), da[m / 5][m % 5]);
        if (m >= 6) lst<f4>(sb + ((m - 6) / 5) * HSLOT + ((m - 6) % 5) * 1024, da[(m - 6) / 5][(m - 6) % 5]);
        SB();
      }
      u32 rb[4];
#pragma unroll
      for (int r = 0; r < 3; ++r) { rb[r] = L_HS + (2 + r) * HSLOT + ((w - r - 1) & 3) * 5120 + vl; OPQ(rb[r]); }
      rb[3] = L_HS + ((w + 5) % 7) * HSLOT + vl;
      OPQ(rb[3]);
      const rsrc_t ro = mk_rsrc(out, SEQ * DM * 2);
      const u32 oso = (team * 128 + th * 64 + w * 16) * 1280 + hq * 160;
      u32 ovo = (ln & 15) * 1280 + (ln >> 4) * 16, ov2 = (ln & 15) * 1280 + 128 + (ln >> 4) * 8;
      OPQ(ovo); OPQ(ov2);
      bar();
      STAMP(17);
      f4 r[5][4], rr[5];
#pragma unroll
      for (int n = 0; n < 5; ++n)
#pragma unroll
        for (int j = 0; j < 4; ++j) r[n][j] = lld<f4>(rb[j] + n * 1024);
      SB();
#pragma unroll
      for (int n = 0; n < 5; ++n) rr[n] = ((da[3][n] + r[n][0]) + (r[n][1] + r[n][2])) + r[n][3];
#pragma unroll
      for (int m = 0; m < 2; ++m)
        st16p(v4u{pk(rr[2 * m][0], rr[2 * m][1]), pk(rr[2 * m][2], rr[2 * m][3]), pk(rr[2 * m + 1][0], rr[2 * m + 1][1]), pk(rr[2 * m + 1][2], rr[2 * m + 1][3])}, ro,
              ovo + 64 * m, oso);
      __builtin_amdgcn_raw_buffer_store_b64(v2u{pk(rr[4][0], rr[4][1]), pk(rr[4][2], rr[4][3])}, ro, ov2, oso, 0);
      if (b == 0 && w == 0) flag_store(sws, tag, W_EPOCH);
    }
    STAMP(18);
    DUMP();
  } else {
    const u32 j = wv;
    __builtin_amdgcn_s_setprio(HPRIO);
    const u32 lane = lane_id();
    const u32 q = lane >> 4, i16 = lane & 15, tq = (lane >> 2) & 3, tp = lane & 3, vl = lane * 16;
    const u32 p1 = j * 256 + b;
    const bool isq = p1 < 600;
    const u32 pp = isq ? p1 : p1 - 600, pks = (pp * (isq ? 2185u : 6554u)) >> 16, pcb1 = pp - pks * (isq ? 30 : 10);
    u32 pv0, ps0, psn, pl0 = L_A + j * 4096;
    {
      const u32 jr = lane >> 3;
      const bool ex = pks >= 16;
      const u32 ra = 20 * (jr & 1) + 16 + (jr >> 1), rb = 20 * (jr >> 2) + (jr & 3);
      const u32 m1 = isq ? 3840 : 1280, m2 = isq ? 8 : ex ? 40 : 4, m3 = isq ? 32 : ex ? 160 : 40, m4 = (!isq && ex) ? 2560 : 0;
      pv0 = (isq ? jr : ex ? ra : rb) * m1 + (lane & 7) * 16;
      psn = m2 * m1;
      ps0 = (m3 * pks - m4) * m1 + pcb1 * 128;
    }
    OPS(ps0); OPS(psn); OPS(pl0); OPQ(pv0);
    if (lane < 32) lst<u32>(L_SYNC + lane * 4, 0u);
    GET_ARGS();
    {
      const v4u sp = mk_srd((const void*)(isq ? ka[1] : ka[3]), pks >= 20 ? 0u : isq ? DM * 3 * DM * 2 : DM * DM * 2);
#pragma unroll
      for (int n = 0; n < 4; ++n) { if (NT & 1) dma16n(pv0, sp, ps0 + n * psn, pl0 + n * 1024); else dma16(pv0, sp, ps0 + n * psn, pl0 + n * 1024); }
    }
    bar();
    u64 ep;
    asm volatile("s_load_dwordx2 %0, %1, 0x0" : "=s"(ep) : "s"(wsp));
    if (j == 3) {
      const v4u sp = mk_srd((const void*)ka[2], 32 * 400 * 2);
      dma16(vl, sp, rank * 1600, L_WPHI); dma16(vl, sp, rank * 1600 + 1024, L_WPHI + 1024);
    }
    u32 selv = i16 == q ? 0x3F803F80u : 0u;
    OPQ(selv);
    const s8 sel = as8(v4u{selv, selv, selv, selv});
    const u32 pi4 = ((tp & 1) << 1) | (tp >> 1);
    {
      u32 dv, cv;
      if (isq) {
        const u32 sec = (pcb1 * 26) >> 8, cn = (pcb1 - sec * 10) * 64 + lane, head = (cn * 3277) >> 16, d = cn - head * 20, hh = head & 1;
        const u32 par = pks & 1;
        const u32 tb = sec == 0 ? par * 2048 : sec * 3072 - 3072, xb = sec == 0 ? 4096 - 256 + par * 128 : 2048 - 256 - 128 + sec * 128;
        const u32 ds = W_W1 + (sec == 0 ? 102400 + (pks >> 1) * 5120 : pks * 5120);
        dv = (head >> 1) * 153600 + d * 16 + (d < 16 ? tb + hh * 1024 : xb + hh * 64) + ds;
        cv = i16 < 4 ? W_CS1 + pks * 7680 + pcb1 * 256 + i16 * 64 + q * 16 : OOB;
      } else {
        const u32 sgi = 4 * pcb1 + q, cc = (sgi * 52) >> 8, pos = sgi - cc * 5;
        const u32 o1 = ((pos & 2) + (i16 >> 3)) * 1024 + (8 * (pos & 1) + (i16 & 7)) * 16, o2 = 4096 + swap23(i16) * 16;
        dv = pks >= 20 ? OOB : cc * 102400 + (pos < 4 ? o1 : o2) + W_WO + pks * 5120;
        cv = OOB;
      }
      u32 rb = L_A + j * 4096 + tq * 128 + q * 32 + (isq ? tp : pi4) * 8;
      OPQ(dv); OPQ(rb); OPQ(cv);
      WAITV(0);
      v4u f[4];
#pragma unroll
      for (int n = 0; n < 4; ++n) f[n] = __builtin_bit_cast(v4u, cat(trd(rb + n * 1024), trd(rb + n * 1024 + 512)));
      f4 cs = z4;
#pragma unroll
      for (int n = 0; n < 4; ++n) { st16c(f[n], ws, dv + n * 256, 0); cs = mfma16(as8(f[n]), sel, cs); }
      st16c(__builtin_bit_cast(v4u, cs), ws, cv, 0);
      v4u sent = {SENT, SENT, SENT, SENT};
      OPQ(sent);
      st16c(sent, ws, vl, W_PART + (rank * 16 + team) * 8192 + j * 2048);
      st16c(sent, ws, vl, W_PART + (rank * 16 + team) * 8192 + j * 2048 + 1024);
      st16c(sent, ws, lane < 4 ? vl : OOB, W_RED + rank * 4096 + team * 256 + j * 64);
      SB();
    }
    STAMP(1);
    if (j == 3) {
      v2u pl[2][4];
      const int src = ((lane & ~3u) | tq) * 4;
#pragma unroll
      for (int h = 0; h < 2; ++h) {
        const u32 pb = L_WPHI + h * 800;
        pl[h][0] = __builtin_bit_cast(v2u, trd(pb + (4 * q + tq) * 40 + 8 * tp)); pl[h][1] = __builtin_bit_cast(v2u, trd(pb + (16 + tq) * 40 + 8 * tp));
        pl[h][2] = __builtin_bit_cast(v2u, trd(pb + (4 * q + tq) * 40 + 32)); pl[h][3] = __builtin_bit_cast(v2u, trd(pb + (16 + tq) * 40 + 32));
      }
      asm volatile("" ::: "memory");
#pragma unroll
      for (int h = 0; h < 2; ++h) {
        const u32 qh = q == (u32)h ? ~0u : 0u, rh = tp == (u32)h ? ~0u : 0u;
        lst<v4u>(L_WPHI + h * 1024 + vl, v4u{pl[h][0].x, pl[h][0].y, pl[h][1].x & qh, pl[h][1].y & qh});
        lst<v4u>(L_PH2 + h * 1024 + vl, v4u{(u32)__builtin_amdgcn_ds_bpermute(src, pl[h][2].x) & rh, (u32)__builtin_amdgcn_ds_bpermute(src, pl[h][2].y) & rh,
                                            (u32)__builtin_amdgcn_ds_bpermute(src, pl[h][3].x) & rh & qh, (u32)__builtin_amdgcn_ds_bpermute(src, pl[h][3].y) & rh & qh});
      }
    }
    GET_EPOCH();
    STAMP(30);
    WAITV(0);
    STAMP(31);
    lds_inc(S_PST);
    if (j == 0) { lds_wait<0>(S_PST); flag_store(sws, tag, W_FA + bid * FAS); }
    STAMP(9);
    u32 rdv[2], rdn[2], rcv[2], rvo[2], rs0[2], rsn[2];
    v4u rsp[2];
#pragma unroll
    for (int e = 0; e < 2; ++e) {
      const u32 piece = p1 + e * 1024;
      const bool isg = piece < 1080;
      const u32 p2 = isg ? piece : piece - 1080;
      const u32 pk_ = (p2 * (isg ? 1214u : 6554u)) >> 16, pcb = p2 - pk_ * (isg ? 54 : 10);
      const u32 mat = pcb >= 27 ? 1 : 0, rs = isg ? 3456 : 1280, u0 = 4 * (pcb - mat * 27);
      rsp[e] = mk_srd((const void*)(!isg ? ka[6] : mat ? ka[5] : ka[4]), piece < 1620 ? DM * FF * 2 : 0u);
      rs0[e] = pk_ * 32 * rs + u0 * 32; rsn[e] = 8 * rs; rvo[e] = (lane >> 3) * rs + (lane & 7) * 16;
      const u32 sgi = u0 + q, cc = (sgi * 52) >> 8, pos = sgi - cc * 5;
      const u32 o1 = ((pos & 2) + (i16 >> 3)) * 1024 + (8 * (pos & 1) + (i16 & 7)) * 16, o2 = 4096 + swap23(i16) * 16;
      const u32 dg = W_WGU + sgi * 40960 + i16 * 16 + pk_ * 2048 + mat * 256, dd = W_WD + cc * (54 * 5120) + (pos < 4 ? o1 : o2) + pk_ * 5120;
      rdv[e] = isg ? dg : piece < 1620 ? dd : OOB;
      rdn[e] = isg ? 512 : 256;
      rcv[e] = (isg && i16 < 4) ? W_CSP + (pk_ * 108 + u0 + i16) * 128 + mat * 64 + (((q & 1) << 1) | (q >> 1)) * 16 : OOB;
      OPQ(rdv[e]); OPQ(rcv[e]); OPQ(rvo[e]); OPS(rs0[e]); OPS(rsn[e]); OPS(rdn[e]);
      rsp[e].x = sgpr(rsp[e].x); rsp[e].y = sgpr(rsp[e].y); rsp[e].z = sgpr(rsp[e].z);
    }
    u32 c1v;
    { const u32 t = (j & 1) * 64 + lane, sec = (t * 1639) >> 16; c1v = t < 120 ? (sec * 600 + t) * 4 + (j >> 1) * (10 * 7680) : OOB; OPQ(c1v); }
    v4u rf[10];
    {
      v4u rt[2][4];
#pragma unroll
      for (int e = 0; e < 2; ++e) {
        const rsrc_t sp = mk_rsrc((const void*)(((u64)rsp[e].y << 32) | rsp[e].x), rsp[e].z);
#pragma unroll
        for (int n = 0; n < 4; ++n) rt[e][n] = __builtin_amdgcn_raw_buffer_load_b128(sp, rvo[e], rs0[e] + n * rsn[e], (NT & 2) ? 2 : 0);
      }
      SB();
      const u32 rb = L_KV + j * 1024 + tq * 128 + q * 32 + pi4 * 8;
#pragma unroll
      for (int e = 0; e < 2; ++e) {
        f4 cs = z4;
#pragma unroll
        for (int n = 0; n < 4; ++n) {
          lst<v4u>(L_KV + j * 1024 + vl, rt[e][n]);
          asm volatile("" ::: "memory");
          rf[5 * e + n] = __builtin_bit_cast(v4u, cat(trd(rb), trd(rb + 512)));
          asm volatile("" ::: "memory");
        }
#pragma unroll
        for (int n = 0; n < 4; ++n) cs = mfma16(as8(rf[5 * e + n]), sel, cs);
        rf[5 * e + 4] = __builtin_bit_cast(v4u, cs);
        OPQ(rf[5 * e + 4]);
      }
    }
    auto rst = [&](int k) LAM { if (k % 5 < 4) st16c(rf[k], ws, rdv[k / 5], (k % 5) * rdn[k / 5]); else st16c(rf[k], ws, rcv[k / 5], 0); };
    STAMP(23);
    __builtin_amdgcn_s_sleep(FAD);
    {
      u32 fv = (j * 64 + lane) * FAS;
#pragma unroll 1
      for (u32 s = 0; s < SPIN_CAP; ++s) {
        const v2u v = ld8c(ws, fv, W_FA);
        fv = (v.x == tag.x && v.y == tag.y) ? OOB : fv;
        if (wall(fv == OOB)) break;
        __builtin_amdgcn_s_sleep(FAR);
        asm volatile("" ::: "memory");
      }
    }
    WAITV(0);
    lds_inc(S_FA);
    lds_wait<0>(S_FA);
    STAMP(24);
    {
      u32 w1s = W_W1 + rank * 153600 + j * 5120, w1l = L_A + j * 5120;
      OPS(w1s); OPS(w1l);
      auto dd = [&](int m) LAM { dma5c(vl, sws, w1s + m * 20480, w1l + m * 20480); };
      u32 cv[10];
      dd(0); dd(1); rst(0);
      WAITV(6); lds_inc(S_L1);
      dd(2); rst(1); dd(3); rst(2);
      WAITV(13); lds_inc(S_L1 + 4);
      dd(4); rst(3); rst(4);
      WAITV(14); lds_inc(S_L1 + 8);
#pragma unroll
      for (int k = 0; k < 10; ++k) cv[k] = ld4c(ws, c1v, W_CS1 + k * 7680 + rank * 160);
      rst(5);
      WAITV(19); lds_inc(S_L1 + 12);
      lds_wait(S_XD);
      dd(5); rst(6);
      WAITV(19); lds_inc(S_L1 + 16);
      dd(6); rst(7);
      if (j < 2) dd(7);
      rst(8); rst(9);
      {
        const float c = (((uf(cv[0]) + uf(cv[1])) + (uf(cv[2]) + uf(cv[3]))) + ((uf(cv[4]) + uf(cv[5])) + (uf(cv[6]) + uf(cv[7])))) + (uf(cv[8]) + uf(cv[9]));
        lst<float>(L_CS1 + j * 256 + lane * 4, c);
      }
      lds_inc(S_CS);
      WAITV(0);
      lds_inc(S_RT);
      if (j == 3) { lds_wait<0>(S_RT); flag_store(sws, tag, W_FB + bid * 32); }
    }
    STAMP(5);
    STAMP(25);
    u32 wos = W_WO + hq * 102400;
    OPS(wos);
    v4u wf[11][5], gf[8][4];
    const u32 wso = W_WD + (hq * 54 + 2 * j) * 5120;
    const u32 gso = W_G + (team * 8 + th * 4) * 54 * 1024 + 2 * j * 1024;
    auto ldw1 = [&](int i, int t) LAM { wf[i][t] = ld16c(ws, vl + t * 1024, wso + ((i >> 1) * 8 + (i & 1)) * 5120); };
    auto ldg1 = [&](int i, int r) LAM { gf[i][r] = ld16c(ws, vl, gso + r * 54 * 1024 + ((i >> 1) * 8 + (i & 1)) * 1024); };
    STAMP(2);
    lds_wait<2>(S_KVD);
#pragma unroll
    for (int n = 0; n < 3; ++n) dma5c(vl, sws, wos + (8 + 4 * n + j) * 5120, L_WO + (8 + 4 * n + j) * 5120);
    WAITV(15);
    STAMP(3);
    kv_job<true>(ws, j + 4, W_PART + (rank * 16 + team) * 8192);
    STAMP(4);
    bar_nw();
    dma5c(vl, sws, wos + j * 5120, L_WO + j * 5120);
    dma5c(vl, sws, wos + (4 + j) * 5120, L_WO + (4 + j) * 5120);
    if (j == 3) {
      const u32 pvo = (lane >> 5) * 8 * 8192 + (lane & 31) * 16, pso = W_PART + rank * 16 * 8192 + team * 512;
      v4u pv[8];
      __builtin_amdgcn_s_setprio(3);
      __builtin_amdgcn_s_sleep(HOPD);
#pragma unroll 1
      for (u32 s = 0; s < SPIN_CAP; ++s) {
#pragma unroll
        for (int n = 0; n < 8; ++n) pv[n] = ld16c(ws, pvo, pso + n * 8192);
        SB();
        u32 mx = 0;
#pragma unroll
        for (int n = 0; n < 8; ++n) mx = umax4(mx, pv[n]);
        if (wall(mx != SENT)) break;
        asm volatile("" ::: "memory");
      }
      float sm[4];
#pragma unroll
      for (int k = 0; k < 4; ++k) {
        const float a = ((uf(pv[0][k]) + uf(pv[1][k])) + (uf(pv[2][k]) + uf(pv[3][k]))) + ((uf(pv[4][k]) + uf(pv[5][k])) + (uf(pv[6][k]) + uf(pv[7][k])));
        auto r = __builtin_amdgcn_permlane32_swap(fu(a), fu(a), false, false);
        sm[k] = uf(r[0]) + uf(r[1]);
      }
      st8c(v2u{san(pk(sm[0], sm[1])), san(pk(sm[2], sm[3]))}, ws, lane < 32 ? lane * 8 : OOB, W_RED + rank * 4096 + team * 256);
      STAMP(6);
    } else if (j == 2) { poll256(ws, W_FB, 32, lane, tag); lds_inc(S_FB); }
    lds_wait(S_FB, 1);
    STAMP(22);
    sfor<0, 5 * NW1>([&](auto ic) LAM { ldw1(decltype(ic)::v / 5, decltype(ic)::v % 5); });
    SB();
    if (j == 3) {
      __builtin_amdgcn_s_sleep(HOPE);
      v4u kv[4];
#pragma unroll 1
      for (u32 s = 0; s < SPIN_CAP; ++s) {
#pragma unroll
        for (int n = 0; n < 4; ++n) kv[n] = ld16c(ws, vl, W_RED + rank * 4096 + n * 1024);
        SB();
        u32 mx = 0;
#pragma unroll
        for (int n = 0; n < 4; ++n) mx = umax4(mx, kv[n]);
        if (wall(mx != SENT)) break;
        asm volatile("" ::: "memory");
      }
#pragma unroll
      for (int n = 0; n < 4; ++n) lst<v4u>(L_KV + n * 1024 + vl, kv[n]);
    }
    WAITV(0);
    STAMP(7);
    bar();
    __builtin_amdgcn_s_setprio(HPRIO);
    STAMP(8);
    auto stage_u = [&](u32 hs) LAM {
      const u32 so = W_WGU + (16 * (hs >> 1) + rank) * 40960 + ((hs & 1) * 4 + j) * 5120, dst = L_HS + ((hs + 5) % 7) * HSLOT + j * 5120;
      if (hs >= 7) waitv<PACE>();
      if (!(ABL & 8) || hs < 7) dma5c(vl, sws, so, dst);
    };
    lds_wait(S_OP);
    stage_u(0); stage_u(1);
    {
      const u32 t = j * 64 + lane, u = 16 * (t >> 5) + rank;
      if (u < 108) {
        u32 cv[20];
#pragma unroll
        for (int k = 0; k < 20; ++k) cv[k] = ld4c(ws, u * 128 + (t & 31) * 4, W_CSP + k * 108 * 128);
        SB();
        float c = 0.f;
#pragma unroll
        for (int k = 0; k < 20; ++k) c += uf(cv[k]);
        lst<float>(L_CS3 + t * 4, c);
      }
    }
    u32 sdb[3] = {L_HS + j * 5120 + vl, L_HS + 3 * HSLOT + j * 5120 + vl, L_HS + 6 * HSLOT + j * 5120 + vl};
    OPQ(sdb[0]); OPQ(sdb[1]); OPQ(sdb[2]);
    auto spill = [&](int i) LAM {
#pragma unroll
      for (int t = 0; t < 5; ++t) lst<v4u>(sdb[((i + 1) % 7) / 3] + (((i + 1) % 7) % 3) * HSLOT + t * 1024, wf[i][t]);
      SB();
    };
    auto ldw = [&](auto lo, auto hi) LAM {
      sfor<decltype(lo)::v * 5, decltype(hi)::v * 5>([&](auto ic) LAM { if (decltype(ic)::v % 5 == 0) waitv<PACE>(); ldw1(decltype(ic)::v / 5, decltype(ic)::v % 5); });
      SB();
    };
    auto ldg = [&](int i) LAM {
      waitv<PACE>();
#pragma unroll
      for (int r = 0; r < 4; ++r) ldg1(i, r);
      SB();
    };
    STAMP(10);
    lds_wait(S_OL);
    ldw(IC<NW1>{}, IC<NW2>{});
    lds_wait<2>(S_WOC);
    STAMP(11);
    auto landed = [&](int k) LAM { lds_inc(L_LAND + 4 * k); };
    stage_u(2); stage_u(3); stage_u(4);
    WAITV(0); landed(0);
    STAMP(13);
    stage_u(5); stage_u(6);
    WAITV(0); landed(1);
    lds_wait<2>(S_HL);
    ldw(IC<NW2>{}, IC<NW3>{});
    lds_wait<1>(L_CONS + 4);
    stage_u(7); stage_u(8); stage_u(9);
    WAITV(5); landed(2);
    stage_u(10);
    ldw(IC<NW3>{}, IC<9>{});
    waitv<5 * (9 - NW3)>(); landed(3);
    lds_wait<2>(L_CONS + 8);
    stage_u(11);
    if (u7) stage_u(12);
    ldw(IC<9>{}, IC<11>{});
    WAITV(10); landed(4);
    const u32 fvo = W_FS4 + ((team * 16 + 4 * j + (lane >> 4)) * 4 + 2 * th + ((lane >> 3) & 1)) * 1024;
    lds_wait<2>(L_CONS + 12);
    if (u7) stage_u(13);
    spill(4);
    WAITV(0); landed(5);
    STAMP(26);
    lds_wait<2>(L_CONS + 16);
    STAMP(27);
    v2u fl = ld8c(ws, fvo, 2 * 128);
    spill(5); spill(6);
    if (!wall(fl.x == tag.x && fl.y == tag.y)) poll_level(ws, fvo, tag, 2);
    asm volatile("" ::: "memory");
    ldg(0); ldg(1); ldg(2);
    lds_wait<2>(L_CONS + 20);
    STAMP(28);
    fl = ld8c(ws, fvo, 3 * 128);
    spill(7); spill(8); ldg(3);
    if (!wall(fl.x == tag.x && fl.y == tag.y)) poll_level(ws, fvo, tag, 3);
    asm volatile("" ::: "memory");
    STAMP(29);
    __builtin_amdgcn_s_setprio(P4PRIO);
    lds_wait<1>(L_CONS + 24);
    STAMP(16);
    fl = ld8c(ws, fvo, 4 * 128);
    spill(9); spill(10);
    lds_inc(S_SP);
    f4 da[4][5];
    sfor<0, 8>([&](auto ic) LAM {
      constexpr int i = decltype(ic)::v;
#pragma unroll
      for (int m = 0; m < 20; ++m) {
        const int r = m / 5, t = m % 5;
        da[r][t] = mfma16(as8(wf[i][t]), as8(gf[i][r]), i == 0 ? z4 : da[r][t]);
        if constexpr (i <= 3) { if (t == 4) ldg1(i + 4, r); }
        if constexpr (i >= 3 && i <= 6) { if (m >= 12 && m < 17) wf[i + 1][m - 12] = lld<v4u>(sdb[((i + 2) % 7) / 3] + (((i + 2) % 7) % 3) * HSLOT + (m - 12) * 1024); }
        if constexpr (i == 7) { if (m >= 6) lst<f4>(sdb[(((m - 6) / 5 + 5) % 7) / 3] + ((((m - 6) / 5 + 5) % 7) % 3) * HSLOT + ((m - 6) % 5) * 1024, da[(m - 6) / 5][(m - 6) % 5]); }
        SB();
      }
      if constexpr (i == 1) {
        if (!wall(fl.x == tag.x && fl.y == tag.y)) poll_level(ws, fvo, tag, 4);
        asm volatile("" ::: "memory");
      }
      SB();
    });
    STAMP(18);
#pragma unroll
    for (int m = 14; m < 20; ++m) lst<f4>(sdb[((m / 5 + 5) % 7) / 3] + (((m / 5 + 5) % 7) % 3) * HSLOT + (m % 5) * 1024, da[m / 5][m % 5]);
    lds_inc(S_HP);
    __builtin_amdgcn_s_setprio(0);
    lds_wait(S_HP);
    {
      const u32 pb = L_HS + ((j + 5) % 7) * HSLOT + vl;
      f4 r[5][4];
#pragma unroll
      for (int n = 0; n < 5; ++n)
#pragma unroll
        for (int k = 0; k < 4; ++k) r[n][k] = lld<f4>(pb + k * 5120 + n * 1024);
      const v4u h0 = lld<v4u>(L_HST + j * 2560 + vl), h1 = lld<v4u>(L_HST + j * 2560 + 1024 + vl);
      const v2u h2 = lld<v2u>(L_HST + j * 2560 + 2048 + lane * 8);
      const u32 hw[10] = {h0.x, h0.y, h0.z, h0.w, h1.x, h1.y, h1.z, h1.w, h2.x, h2.y};
#pragma unroll
      for (int n = 0; n < 5; ++n)
        lst<f4>(pb + n * 1024, ((r[n][0] + r[n][1]) + (r[n][2] + r[n][3])) + f4{blo(hw[2 * n]), bhi(hw[2 * n]), blo(hw[2 * n + 1]), bhi(hw[2 * n + 1])});
    }
    bar();
    DUMP();
  }
}

extern "C" void kernel_launch(void* const* d_in, const int* in_sizes, int n_in, void* d_out, int out_size, void* d_ws, size_t ws_size,
                              hipStream_t stream) {
  (void)hipFuncSetAttribute((const void*)fused_block, hipFuncAttributeMaxDynamicSharedMemorySize, L_BYTES);
  fused_block<<<256, 512, L_BYTES, stream>>>((const u16*)d_in[0], (const u16*)d_in[1], (const u16*)d_in[2], (const u16*)d_in[3],
                                             (const u16*)d_in[5], (const u16*)d_in[6], (const u16*)d_in[7], (u16*)d_out, (char*)d_ws);
}
```

```cpp
#include <hip/hip_runtime.h>
#include <hip/hip_bf16.h>

typedef unsigned short u16;
typedef unsigned int u32;
typedef unsigned long long u64;
typedef u32 v4u __attribute__((ext_vector_type(4)));
typedef u32 v2u __attribute__((ext_vector_type(2)));
typedef float f4 __attribute__((ext_vector_type(4)));
typedef float f2 __attribute__((ext_vector_type(2)));
typedef float f16v __attribute__((ext_vector_type(16)));
typedef short s4 __attribute__((ext_vector_type(4)));
typedef short s8 __attribute__((ext_vector_type(8)));
typedef __bf16 bf2 __attribute__((ext_vector_type(2)));
#define LAS __attribute__((address_space(3)))
#define CAS __attribute__((address_space(4)))
#if defined(__HIP_DEVICE_COMPILE__)
#define NOPK __attribute__((target("no-packed-fp32-ops")))
#else
#define NOPK
#endif
#define DEV __device__ __forceinline__ NOPK
#define SB() __builtin_amdgcn_sched_barrier(0)
#define OPQ(x) asm volatile("" : "+v"(x))
typedef __amdgpu_buffer_rsrc_t rsrc_t;

#define DBG 0
#define ROT ((rank + 3 * (team & 1)) % 10)
#define P1ST 0
#define HOPA0 28
#define HOPA1 56
#define HOPB 1
#define FAS 128
#define NRM1 3
#define NRM2 8
#define HOPA 2
#define NWPRE 2
#define HOPD 12
#define HOPE 10
#define FAD 10
#define FAR 4
#define PD2 14
#define KDEP 6
#define FBD 28
#define LVS 8
#define WD6 8
#define NW2 6
#define NW0 4
#define NW1 4
#define XD3 1
#define HPRIO 0
#define RDEP 4
#define CODEPF 1
#define CODE_BYTES 45056
#define HROT 0
#define OROT 0
#define P4PRIO 3
#define ABL 0
#define NW3 7
#define WLP 0
#define PACE 63
#define XT 1
#define RT 0
#define RLATE 0
#define RSP 2
#define NT 0
#define XPS 1
#define XMAP 4

constexpr int SEQ = 2048, DM = 640, FF = 1728;
constexpr u32 W_EPOCH = 0;
constexpr u32 W_FA = 4096;
constexpr u32 W_FB = W_FA + 256 * 128;
constexpr u32 W_FC = W_FB + 256 * 32;
constexpr u32 W_FS2 = W_FC + 256 * 128;
constexpr u32 W_FS3 = W_FS2 + 256 * 4 * 128;
constexpr u32 W_FS4 = W_FS3 + 256 * 4 * 128;
constexpr u32 W_RED = W_FS4 + 256 * 4 * 8 * 128;
constexpr u32 W_STAT = W_RED + 65536;
constexpr u32 W_CSP = W_STAT + 131072;
constexpr u32 W_CS1 = W_CSP + 20 * 108 * 128;
constexpr u32 W_PART = W_CS1 + 20 * 7680;
constexpr u32 W_O = W_PART + 16 * 16 * 8192;
constexpr u32 W_H = W_O + 16 * 8 * 20 * 1024;
constexpr u32 W_G = W_H + 16 * 4 * 40 * 1024;
constexpr u32 W_WGU = W_G + 16 * 8 * 54 * 1024;
constexpr u32 W_WD = W_WGU + 108 * 40 * 1024;
constexpr u32 W_WO = W_WD + 8 * 54 * 5 * 1024;
constexpr u32 W_W1 = W_WO + 8 * 20 * 5 * 1024;
constexpr u32 W_DBG = W_W1 + 16 * 153600;
constexpr u32 W_END = W_DBG + 256 * 8 * 32 * 4;
constexpr u32 L_WPHI = 0;
constexpr u32 L_CS3 = 3200;
constexpr u32 L_SYNC = 3072;
constexpr u32 L_CS1 = 2048;
constexpr u32 S_PST = L_SYNC, S_FA = L_SYNC + 4, S_XD = L_SYNC + 8, S_CS = L_SYNC + 12, S_KVD = L_SYNC + 16, S_WOC = L_SYNC + 20, S_ST = L_SYNC + 24, S_FB = L_SYNC + 28, S_OP = L_SYNC + 52, S_OL = L_SYNC + 56, S_HL = L_SYNC + 60;
constexpr u32 S_L1 = L_SYNC + 32, S_SP = L_SYNC + 64, S_HP = L_SYNC + 68, S_KV = L_SYNC + 72, L_LAND = L_SYNC + 80, L_CONS = L_SYNC + 100;
constexpr u32 L_KV = 4096;
constexpr u32 L_A = 8192;
constexpr u32 L_B = L_A + 102400;
constexpr u32 L_PH2 = L_B + 51200;
constexpr u32 L_IMG = L_A, L_WO = L_A;
constexpr u32 L_HS = L_A, HSLOT = 20480;
constexpr u32 L_HST = L_HS + 7 * HSLOT;
constexpr u32 L_BYTES = 163840;
constexpr u32 OOB = 0x80000000u;
constexpr u32 SENT = 0xFFFFFFFFu;
constexpr u32 SPIN_CAP = 1u << 18;

DEV v4u mk_srd(const void* p, u32 bytes) { return v4u{(u32)(size_t)p, (u32)((size_t)p >> 32), bytes, 0x00020000u}; }
DEV rsrc_t mk_rsrc(const void* p, u32 bytes) { return __builtin_amdgcn_make_buffer_rsrc((void*)p, 0, bytes, 0x00020000); }
DEV void dma16(u32 voff, v4u srd, u32 soff, u32 lds) {
  asm volatile("s_mov_b32 m0, %2\n\ts_nop 0\n\tbuffer_load_dwordx4 %0, %1, %3 offen lds" ::"v"(voff), "s"(srd), "s"(lds), "s"(soff) : "memory");
}
DEV void dma16c(u32 voff, v4u srd, u32 soff, u32 lds) {
  asm volatile("s_mov_b32 m0, %2\n\ts_nop 0\n\tbuffer_load_dwordx4 %0, %1, %3 offen sc1 lds" ::"v"(voff), "s"(srd), "s"(lds), "s"(soff) : "memory");
}
DEV void dma16n(u32 voff, v4u srd, u32 soff, u32 lds) {
  asm volatile("s_mov_b32 m0, %2\n\ts_nop 0\n\tbuffer_load_dwordx4 %0, %1, %3 offen nt lds" ::"v"(voff), "s"(srd), "s"(lds), "s"(soff) : "memory");
}
DEV void dma_a(u32 voff, v4u srd, u32 sb, u32 sa, u32 lb, u32 la) {
  u32 t;
  asm volatile("s_add_u32 m0, %5, %6\n\ts_add_u32 %0, %3, %4\n\tbuffer_load_dwordx4 %1, %2, %0 offen lds"
               : "=&s"(t) : "v"(voff), "s"(srd), "s"(sb), "s"(sa), "s"(lb), "s"(la) : "memory", "scc");
}
DEV void dma_ac(u32 voff, v4u srd, u32 sb, u32 sa, u32 lb, u32 la) {
  u32 t;
  asm volatile("s_add_u32 m0, %5, %6\n\ts_add_u32 %0, %3, %4\n\tbuffer_load_dwordx4 %1, %2, %0 offen sc1 lds"
               : "=&s"(t) : "v"(voff), "s"(srd), "s"(sb), "s"(sa), "s"(lb), "s"(la) : "memory", "scc");
}
DEV void dma_i(u32 voff, v4u srd, u32 sb, u32 sa, u32 lb, u32 la) {
  u32 t;
  asm volatile("s_add_u32 m0, %5, %6\n\ts_add_u32 %0, %3, %4\n\tbuffer_load_dwordx4 %1, %2, %0 offen lds"
               : "=&s"(t) : "v"(voff), "s"(srd), "s"(sb), "i"(sa), "s"(lb), "i"(la) : "memory", "scc");
}
DEV void touch(u32 voff, v4u srd, u32 soff, u32 lds) {
  asm volatile("s_mov_b32 m0, %2\n\ts_nop 0\n\tbuffer_load_dword %0, %1, %3 offen lds" ::"v"(voff), "s"(srd), "s"(lds), "s"(soff) : "memory");
}
DEV void dma5c(u32 voff, v4u srd, u32 so, u32 lds) {
  u32 t;
  asm volatile("s_mov_b32 m0, %4\n\ts_add_u32 %0, %3, 0x1000\n\t"
               "buffer_load_dwordx4 %1, %2, %3 offen sc1 lds\n\t"
               "buffer_load_dwordx4 %1, %2, %3 offen offset:1024 sc1 lds\n\t"
               "buffer_load_dwordx4 %1, %2, %3 offen offset:2048 sc1 lds\n\t"
               "buffer_load_dwordx4 %1, %2, %3 offen offset:3072 sc1 lds\n\t"
               "s_add_u32 m0, %4, 0x1000\n\ts_nop 0\n\t"
               "buffer_load_dwordx4 %1, %2, %0 offen sc1 lds"
               : "=&s"(t) : "v"(voff), "s"(srd), "s"(so), "s"(lds) : "memory", "scc");
}
DEV u32 sgpr(u32 x) {
  u32 o;
  asm volatile("s_mov_b32 %0, %1" : "=s"(o) : "s"(__builtin_amdgcn_readfirstlane(x)));
  return o;
}
#define OPS(x) x = sgpr(x)
template <int N> DEV void waitv() { asm volatile("s_waitcnt vmcnt(%0)" ::"n"(N) : "memory"); }
#define WAITV(n) asm volatile("s_waitcnt vmcnt(" #n ")" ::: "memory")
DEV void bar() {
  asm volatile("" ::: "memory");
  __builtin_amdgcn_s_waitcnt(0xC07F);
  __builtin_amdgcn_s_barrier();
  asm volatile("" ::: "memory");
  SB();
}
DEV void bar_nw() {
  asm volatile("" ::: "memory");
  __builtin_amdgcn_s_barrier();
  asm volatile("" ::: "memory");
  SB();
}
DEV s4 trd(u32 a) { return __builtin_amdgcn_ds_read_tr16_b64_v4i16((LAS s4*)a); }
DEV s8 cat(s4 a, s4 b) { return __builtin_shufflevector(a, b, 0, 1, 2, 3, 4, 5, 6, 7); }
DEV s8 as8(v4u a) { return __builtin_bit_cast(s8, a); }
DEV s4 as4(v2u a) { return __builtin_bit_cast(s4, a); }
DEV f4 mfma16(s8 a, s8 b, f4 c) { return __builtin_amdgcn_mfma_f32_16x16x32_bf16(a, b, c, 0, 0, 0); }
DEV f4 mfma16h(s4 a, s4 b, f4 c) { return __builtin_amdgcn_mfma_f32_16x16x16bf16_1k(a, b, c, 0, 0, 0); }
DEV f16v mfma32(s8 a, s8 b, f16v c) { return __builtin_amdgcn_mfma_f32_32x32x16_bf16(a, b, c, 0, 0, 0); }
DEV u32 pk(float a, float b) { return __builtin_bit_cast(u32, __builtin_convertvector(f2{a, b}, bf2)); }
DEV float blo(u32 x) { return __builtin_bit_cast(float, x << 16); }
DEV float bhi(u32 x) { return __builtin_bit_cast(float, x & 0xFFFF0000u); }
DEV float dot2(u32 a, u32 b, float c) { return __builtin_amdgcn_fdot2_f32_bf16(__builtin_bit_cast(bf2, a), __builtin_bit_cast(bf2, b), c, false); }
DEV u32 fu(float x) { return __builtin_bit_cast(u32, x); }
DEV float uf(u32 x) { return __builtin_bit_cast(float, x); }
template <class T> DEV T lld(u32 a) { return *(LAS T*)a; }
template <class T> DEV void lst(u32 a, T v) { *(LAS T*)a = v; }
DEV v4u ld16c(rsrc_t r, u32 vo, u32 so) { return __builtin_amdgcn_raw_buffer_load_b128(r, vo, so, 16); }
DEV v2u ld8c(rsrc_t r, u32 vo, u32 so) { return __builtin_amdgcn_raw_buffer_load_b64(r, vo, so, 16); }
DEV u32 ld4c(rsrc_t r, u32 vo, u32 so) { return __builtin_amdgcn_raw_buffer_load_b32(r, vo, so, 16); }
DEV void st16c(v4u v, rsrc_t r, u32 vo, u32 so) {
  __builtin_amdgcn_raw_buffer_store_b128(v, r, vo, so, 16);
  asm volatile("s_nop 1" ::"v"(v) : "memory");
}
DEV void st16p(v4u v, rsrc_t r, u32 vo, u32 so) {
  __builtin_amdgcn_raw_buffer_store_b128(v, r, vo, so, 0);
  asm volatile("s_nop 1" ::"v"(v) : "memory");
}
DEV void st8c(v2u v, rsrc_t r, u32 vo, u32 so) { __builtin_amdgcn_raw_buffer_store_b64(v, r, vo, so, 16); }
DEV void st4c(u32 v, rsrc_t r, u32 vo, u32 so) { __builtin_amdgcn_raw_buffer_store_b32(v, r, vo, so, 16); }
DEV bool wall(bool c) { return __builtin_amdgcn_ballot_w64(!c) == 0ull; }
DEV u32 san(u32 x) { return x < 0xFFFFFFFEu ? x : 0xFFFFFFFEu; }
DEV u32 swap23(u32 k) { return (k & ~12u) | ((k & 4u) << 1) | ((k & 8u) >> 1); }

template <bool MAXOP, bool RCP> DEV void red4(float& a, float& b, float& c, float& d) {
  auto r1 = __builtin_amdgcn_permlane16_swap(fu(a), fu(b), false, false);
  auto r2 = __builtin_amdgcn_permlane16_swap(fu(c), fu(d), false, false);
  float ab = MAXOP ? __builtin_elementwise_maximum(uf(r1[0]), uf(r1[1])) : uf(r1[0]) + uf(r1[1]);
  float cd = MAXOP ? __builtin_elementwise_maximum(uf(r2[0]), uf(r2[1])) : uf(r2[0]) + uf(r2[1]);
  auto r3 = __builtin_amdgcn_permlane32_swap(fu(ab), fu(cd), false, false);
  float z = MAXOP ? __builtin_elementwise_maximum(uf(r3[0]), uf(r3[1])) : uf(r3[0]) + uf(r3[1]);
  if (RCP) z = __builtin_amdgcn_rcpf(z);
  auto g1 = __builtin_amdgcn_permlane32_swap(fu(z), fu(z), false, false);
  auto g2 = __builtin_amdgcn_permlane16_swap(g1[0], g1[0], false, false);
  auto g3 = __builtin_amdgcn_permlane16_swap(g1[1], g1[1], false, false);
  a = uf(g2[0]); b = uf(g2[1]); c = uf(g3[0]); d = uf(g3[1]);
}
DEV void red2(float& a, float& b) {
  auto r1 = __builtin_amdgcn_permlane16_swap(fu(a), fu(b), false, false);
  float ab = uf(r1[0]) + uf(r1[1]);
  auto r3 = __builtin_amdgcn_permlane32_swap(fu(ab), fu(ab), false, false);
  float z = uf(r3[0]) + uf(r3[1]);
  auto g2 = __builtin_amdgcn_permlane16_swap(fu(z), fu(z), false, false);
  a = uf(g2[0]); b = uf(g2[1]);
}

#define LAM __attribute__((always_inline)) NOPK
template <int V> struct IC { static constexpr int v = V; };
template <int A, int B, class F> DEV void sfor(F&& f) {
  if constexpr (A < B) { f(IC<A>{}); sfor<A + 1, B>(f); }
}
struct Phi { v2u pl[2], mi[2], ex; };
DEV float fmx(float a, float b) { return __builtin_elementwise_maximum(a, b); }
DEV float fmn(float a, float b) { return __builtin_elementwise_minimum(a, b); }
DEV void phi_mm(s8 b0, s8 b1, const s8 (&A)[4], f4 (&p)[3]) {
  const f4 z = {0.f, 0.f, 0.f, 0.f};
  p[2] = mfma16(A[2], b0, z);
  p[0] = mfma16(A[0], b0, z);
  p[1] = mfma16(A[1], b1, z);
  p[2] = mfma16(A[3], b1, p[2]);
}
DEV Phi phi_sm(const f4 (&pp)[3]) {
  const float p[2][5] = {{pp[0][0], pp[0][1], pp[0][2], pp[0][3], pp[2][0]}, {pp[1][0], pp[1][1], pp[1][2], pp[1][3], pp[2][1]}};
  float mx[2], nm[2];
#pragma unroll
  for (int h = 0; h < 2; ++h) {
    mx[h] = fmx(fmx(fmx(fmx(p[h][0], p[h][1]), p[h][2]), p[h][3]), p[h][4]);
    nm[h] = fmx(fmx(fmx(fmx(-p[h][0], -p[h][1]), -p[h][2]), -p[h][3]), -p[h][4]);
  }
  red4<true, false>(mx[0], nm[0], mx[1], nm[1]);
  float ep[2][5], em[2][5], sp[2], sm[2];
#pragma unroll
  for (int h = 0; h < 2; ++h) {
#pragma unroll
    for (int j = 0; j < 5; ++j) {
      ep[h][j] = __builtin_amdgcn_exp2f(p[h][j] - mx[h]);
      em[h][j] = __builtin_amdgcn_exp2f(-p[h][j] - nm[h]);
    }
    sp[h] = ((ep[h][0] + ep[h][1]) + (ep[h][2] + ep[h][3])) + ep[h][4];
    sm[h] = ((em[h][0] + em[h][1]) + (em[h][2] + em[h][3])) + em[h][4];
  }
  red4<false, true>(sp[0], sm[0], sp[1], sm[1]);
  Phi o;
#pragma unroll
  for (int h = 0; h < 2; ++h) {
    o.pl[h] = v2u{pk(ep[h][0] * sp[h], ep[h][1] * sp[h]), pk(ep[h][2] * sp[h], ep[h][3] * sp[h])};
    o.mi[h] = v2u{pk(em[h][0] * sm[h], em[h][1] * sm[h]), pk(em[h][2] * sm[h], em[h][3] * sm[h])};
    o.ex[h] = pk(ep[h][4] * sp[h], em[h][4] * sm[h]);
  }
  return o;
}
DEV v2u lnpk(f4 a, f4 cs, float nmean, float sc) {
  return v2u{pk(__builtin_fmaf(nmean, cs[0], a[0]) * sc, __builtin_fmaf(nmean, cs[1], a[1]) * sc),
             pk(__builtin_fmaf(nmean, cs[2], a[2]) * sc, __builtin_fmaf(nmean, cs[3], a[3]) * sc)};
}
DEV f4 lcs(u32 a) { return lld<f4>(a) + lld<f4>(a + 512); }
DEV u32 bsel(u32 m, u32 a, u32 b) { return (a & m) | (b & ~m); }
DEV void poll_flags(rsrc_t ws, u32 voff, bool active, v2u tag) {
#pragma unroll 1
  for (u32 s = 0; s < SPIN_CAP; ++s) {
    v2u v = tag;
    if (active) v = ld8c(ws, voff, 0);
    if (wall(v.x == tag.x && v.y == tag.y)) break;
    asm volatile("" ::: "memory");
  }
  asm volatile("" ::: "memory");
}
template <int SL = 1> DEV void lds_check(u32 lv, u32 a, u32 n) {
  u32 sv, cnt;
  asm volatile(
      "s_nop 0\n\tv_readfirstlane_b32 %0, %2\n\ts_cmp_ge_u32 %0, %4\n\ts_cbranch_scc1 2f\n\ts_mov_b32 %1, 0x400000\n"
      "1:\n\tv_mov_b32 %2, %3\n\tds_read_b32 %2, %2\n\ts_waitcnt lgkmcnt(0)\n\tv_readfirstlane_b32 %0, %2\n\ts_cmp_ge_u32 %0, %4\n\ts_cbranch_scc1 2f\n\t"
      "s_sleep %5\n\ts_sub_u32 %1, %1, 1\n\ts_cbranch_scc0 1b\n2:"
      : "=&s"(sv), "=&s"(cnt), "+v"(lv) : "s"(a), "s"(n), "n"(SL) : "memory", "scc");
}
template <int SL = 1> DEV void lds_wait(u32 a, u32 n = 4) { lds_check<SL>(0u, a, n); }
DEV void lds_inc(u32 a) {
  u32 t0, t1;
  asm volatile("v_mov_b32 %0, %2\n\tv_mov_b32 %1, 1\n\ts_mov_b64 exec, 1\n\tds_add_u32 %0, %1\n\ts_mov_b64 exec, -1" : "=&v"(t0), "=&v"(t1) : "s"(a) : "memory");
}
DEV void flag_store(v4u srd, v2u tag, u32 so) {
  v2u t;
  asm volatile("v_mov_b64 %0, %2\n\ts_mov_b64 exec, 1\n\tbuffer_store_dwordx2 %0, off, %1, %3 sc1\n\ts_mov_b64 exec, -1"
               : "=&v"(t) : "s"(srd), "s"(tag), "s"(so) : "memory");
}
template <int SL = 0> DEV void poll_mask(rsrc_t ws, u32 fv, u32 base, v2u tag, v2u v) {
#pragma unroll 1
  for (u32 s = 0; s < SPIN_CAP; ++s) {
    fv = (v.x == tag.x && v.y == tag.y) ? OOB : fv;
    if (wall(fv == OOB)) break;
    __builtin_amdgcn_s_sleep(SL);
    asm volatile("" ::: "memory");
    v = ld8c(ws, fv, base);
  }
  asm volatile("" ::: "memory");
}
DEV void poll256(rsrc_t ws, u32 base, u32 stride, u32 lane, v2u tag) {
#pragma unroll 1
  for (u32 s = 0; s < SPIN_CAP; ++s) {
    v2u f[4];
    bool ok = true;
#pragma unroll
    for (int n = 0; n < 4; ++n) f[n] = ld8c(ws, lane * stride, base + n * 64 * stride);
#pragma unroll
    for (int n = 0; n < 4; ++n) ok = ok && f[n].x == tag.x && f[n].y == tag.y;
    if (wall(ok)) break;
    __builtin_amdgcn_s_sleep(8);
    asm volatile("" ::: "memory");
  }
  asm volatile("" ::: "memory");
}
DEV u32 lane_id() {
  u32 l;
  asm volatile("v_mbcnt_lo_u32_b32 %0, -1, 0\n\tv_mbcnt_hi_u32_b32 %0, -1, %0" : "=v"(l));
  return l;
}
DEV u32 umax(u32 a, u32 b) { return a > b ? a : b; }
DEV u32 umax4(u32 m, v4u v) { return umax(umax(umax(umax(m, v.x), v.y), v.z), v.w); }

#define STAMP(i) do {} while (0)

DEV void poll_level(rsrc_t ws, u32 voff, v2u tag, u32 level) {
#pragma unroll 1
  for (u32 s = 0; s < SPIN_CAP; ++s) {
    const v2u v = ld8c(ws, voff, level * 128);
    voff = (v.x == tag.x && v.y == tag.y) ? OOB : voff;
    if (wall(voff == OOB)) break;
    __builtin_amdgcn_s_sleep(LVS);
    asm volatile("" ::: "memory");
  }
  asm volatile("" ::: "memory");
}

template <bool TWO> DEV void kv_job(rsrc_t ws, u32 jb, u32 so) {
  const u32 lane = lane_id();
  const u32 q = lane >> 4, tq = (lane >> 2) & 3, tp = lane & 3;
  const u32 a1c = !TWO ? jb * 32 : jb < 6 ? (jb - 4) * 32 : 128;
  const u32 b1c = !TWO ? 160 + (jb >> 1) * 32 : jb == 6 ? 160 : 224;
  const u32 a2c = jb < 6 ? a1c + 64 : a1c;
  const u32 b2c = jb == 6 ? 192 : b1c;
  const u32 rb = L_IMG + (2 * (q >> 1) + 16 * (q & 1) + 4 * tq) * 264 + 8 * tp;
  constexpr int NO = TWO ? 4 : 2;
  u32 ra[4] = {rb + a1c, rb + b1c, rb + a2c, rb + b2c};
  u32 mlo = (jb != 6 && (lane & 1)) ? ~0u : 0u, mhi = (jb == 6 || (lane & 1)) ? ~0u : 0u, vl = lane * 16;
#pragma unroll
  for (int n = 0; n < NO; ++n) OPQ(ra[n]);
  OPQ(vl);
  if (TWO) { OPQ(mlo); OPQ(mhi); }
  bar();
  s8 fr[4][NO];
#pragma unroll
  for (int ks = 0; ks < 4; ++ks)
#pragma unroll
    for (int n = 0; n < NO; ++n) fr[ks][n] = cat(trd(ra[n] + ks * 32 * 264), trd(ra[n] + ks * 32 * 264 + 264));
  SB();
  f4 d1 = {0.f, 0.f, 0.f, 0.f}, d2 = d1;
#pragma unroll
  for (int ks = 0; ks < 4; ++ks) {
    d1 = mfma16(fr[ks][0], fr[ks][1], d1);
    if (TWO) d2 = mfma16(fr[ks][NO - 2], fr[ks][NO - 1], d2);
  }
  if (TWO) d1 = f4{uf(bsel(mlo, fu(d2[0]), fu(d1[0]))), uf(bsel(mlo, fu(d2[1]), fu(d1[1]))), uf(bsel(mhi, fu(d2[2]), fu(d1[2]))), uf(bsel(mhi, fu(d2[3]), fu(d1[3])))};
  st16c(v4u{san(fu(d1[0])), san(fu(d1[1])), san(fu(d1[2])), san(fu(d1[3]))}, ws, vl, so + jb * 1024);
}

typedef u64 v8q __attribute__((ext_vector_type(8)));
__global__ __launch_bounds__(512, 2) NOPK void fused_block(const u16* x_, const u16* wqkv_, const u16* wphi_, const u16* wo_, const u16* wg_, const u16* wu_,
                                                           const u16* wd_, u16* out_, char* wsp_) {
  v8q ka;
  u64 kw;
  asm volatile("s_load_dwordx16 %0, %2, 0x0\n\ts_load_dwordx2 %1, %2, 0x40" : "=&s"(ka), "=&s"(kw) : "s"(__builtin_amdgcn_kernarg_segment_ptr()));
  u64 pc;
  asm volatile("s_getpc_b64 %0" : "=s"(pc));
  SB();
  STAMP(0);
  const u32 wv = __builtin_amdgcn_readfirstlane(threadIdx.x >> 6);
  const u32 b = blockIdx.x, xcd = b & 7, idx = b >> 3;
  const u32 team = (xcd >> 1) * 4 + (idx >> 3), rank = ((idx & 4) << 1) | ((xcd & 1) << 2) | (idx & 3);
  const u32 bid = team * 16 + rank, hq = rank & 7, th = rank >> 3;
  const bool u7 = rank < 12;
  const f4 z4 = {0.f, 0.f, 0.f, 0.f};
#define DUMP()
#define GET_ARGS()                                                                                         \
  SB();                                                                                                    \
  asm volatile("s_waitcnt lgkmcnt(0)" ::: "memory");                                                       \
  SB();                                                                                                    \
  asm volatile("" : "+s"(ka), "+s"(kw));                                                                   \
  char* const wsp = (char*)kw;                                                                             \
  const rsrc_t ws = mk_rsrc(wsp, W_END);                                                                   \
  const v4u sws = mk_srd(wsp, W_END);
#define GET_EPOCH()                                                                                        \
  asm volatile("s_waitcnt lgkmcnt(0)" ::: "memory");                                                       \
  SB();                                                                                                    \
  asm volatile("" : "+s"(ep));                                                                             \
  ep = (ep & ~15ull) + 16ull;                                                                              \
  const v2u tag = {(u32)ep, (u32)(ep >> 32)};

  if (wv >= 4) {
    const u32 w = wv - 4;
    const u32 lane = lane_id();
    const u32 q = lane >> 4, i16 = lane & 15;
    {
      const v4u sc = mk_srd((const void*)(pc & ~255ull), CODE_BYTES);
      if (w == 0) asm volatile("s_mov_b32 m0, %2\n\ts_nop 0\n\tbuffer_load_dword %0, %1, 0 offen lds" ::"v"(lane < 16 ? (lane * 32 + idx) * 128 : OOB), "s"(sc), "s"(L_CS3) : "memory");
    }
    const u32 vx0 = (lane >> 3) * 1280 + (((lane & 7) ^ (lane >> 4)) << 4), vx1 = (vx0 ^ 64) + 8 * 1280;
    u32 xs0 = (team * 128 + w * 32) * 1280, xl0 = L_B + w * 12288;
    OPS(xs0); OPS(xl0);
    GET_ARGS();
    const u16* const x = (const u16*)ka[0];
    u16* const out = (u16*)ka[7];
    const v4u sx = mk_srd(x, SEQ * DM * 2);
    bar_nw();
    u64 ep;
    asm volatile("s_load_dwordx2 %0, %1, 0x0" : "=s"(ep) : "s"(wsp));
    auto stagex = [&](int c) LAM {
      dma_i(vx0, sx, xs0, c * 128, xl0, (c % 3) * 4096);
      dma_i(vx1, sx, xs0, c * 128, xl0, (c % 3) * 4096 + 1024);
      dma_i(vx0, sx, xs0, c * 128 + 16 * 1280, xl0, (c % 3) * 4096 + 2048);
      dma_i(vx1, sx, xs0, c * 128 + 16 * 1280, xl0, (c % 3) * 4096 + 3072);
    };
    touch(lane < 20 ? lane * 128 : OOB, sx, team * (128 * 1280) + (rank * 4 + w) * 2560, L_CS3);
    stagex(0); stagex(1);
    stagex(2);
    v4u xa[2][20];
    float nm1[2], rs1[2];
    {
      v4u onesv = {0x3F803F80u, 0x3F803F80u, 0x3F803F80u, 0x3F803F80u};
      OPQ(onesv);
      const s8 ones = as8(onesv);
      f4 as[2] = {z4, z4}, ag[2] = {z4, z4};
      u32 ax0 = L_B + w * 12288 + i16 * 128 + ((q ^ ((i16 >> 1) & 7)) << 4), ax1 = ax0 ^ 64;
      OPQ(ax0); OPQ(ax1);
      sfor<0, 11>([&](auto cc_) LAM {
        constexpr int c = decltype(cc_)::v;
        if constexpr (c < 10) waitv<(c < 8 ? 8 : c == 8 ? 4 : 0)>();
        __builtin_amdgcn_s_waitcnt(0xC07F);
        SB();
        if constexpr (c < 10) {
#pragma unroll
          for (int t = 0; t < 2; ++t) { xa[t][2 * c] = lld<v4u>(ax0 + (c % 3) * 4096 + t * 2048); xa[t][2 * c + 1] = lld<v4u>(ax1 + (c % 3) * 4096 + t * 2048); }
        }
        if constexpr (c == 0 && XPS) lds_wait(XPS == 1 ? S_ST : S_PST);
        if constexpr (c < 7) { __builtin_amdgcn_s_waitcnt(0xC07F); SB(); stagex(c + 3); }
        if constexpr (c == 10) lds_inc(S_XD);
        if constexpr (c >= 1) {
#pragma unroll
          for (int k = 2 * c - 2; k < 2 * c; ++k)
#pragma unroll
            for (int t = 0; t < 2; ++t) { as[t] = mfma16(ones, as8(xa[t][k]), as[t]); ag[t] = mfma16(as8(xa[t][k]), as8(xa[t][k]), ag[t]); }
        }
        SB();
      });
      STAMP(23);
      float g[2];
#pragma unroll
      for (int t = 0; t < 2; ++t) {
        u32 v = 0;
#pragma unroll
        for (int r = 0; r < 4; ++r) v |= fu(ag[t][r]) & (i16 == 4 * q + r ? ~0u : 0u);
        g[t] = uf(v);
      }
      red2(g[0], g[1]);
#pragma unroll
      for (int t = 0; t < 2; ++t) {
        nm1[t] = -as[t][0] * (1.0f / DM);
        rs1[t] = __builtin_amdgcn_rsqf(__builtin_fmaxf(__builtin_fmaf(-nm1[t], nm1[t], g[t] * (1.0f / DM)), 0.f) + 1e-5f);
        OPQ(nm1[t]); OPQ(rs1[t]);
      }
    }
    GET_EPOCH();
    __builtin_amdgcn_s_setprio(2);
    const float L2E = 1.4426950408889634f;
    {
      f4 acc[2][5] = {{z4, z4, z4, z4, z4}, {z4, z4, z4, z4, z4}};
      u32 bw[2] = {L_A + lane * 16, L_A + 51200 + lane * 16};
      OPQ(bw[0]); OPQ(bw[1]);
      constexpr int KD = KDEP;
      s8 kf[KD + 1];
      u32 lv;
      lds_wait(S_FA);
      { v2u tg = tag; OPQ(tg); st8c(tg, ws, lane_id() << 26, w == 0 ? W_FB + bid * 32 : OOB); }
      lds_wait(S_FA);
      if (w == 0) flag_store(sws, tag, W_FC + bid * 128);
      lds_wait(S_L1);
      STAMP(25);
#pragma unroll
      for (int f = 0; f < KD; ++f) kf[f] = lld<s8>(bw[0] + f * 1024);
      SB();
      sfor<0, 100>([&](auto gc) LAM {
        constexpr int g = decltype(gc)::v, gn = g + KD;
        if constexpr (gn < 100 && gn % 20 == 0) {
          lds_check(lv, S_L1 + 4 * (gn / 20), 4);

        }
        if constexpr (gn < 100) kf[gn % (KD + 1)] = lld<s8>(bw[gn / 50] + (gn % 50) * 1024);
        if constexpr (gn == 44) lds_inc(S_CS);
        if constexpr (gn % 20 == 14) lv = *(volatile LAS u32*)(gn < 80 ? S_L1 + 4 * (gn / 20 + 1) : S_CS);
        acc[0][g % 5] = mfma16(kf[g % (KD + 1)], as8(xa[0][g / 5]), acc[0][g % 5]);
        acc[1][g % 5] = mfma16(kf[g % (KD + 1)], as8(xa[1][g / 5]), acc[1][g % 5]);
        SB();
      });
      lds_inc(S_KVD);
      lds_check(lv, S_CS, 8);
      STAMP(2);
      const u32 cb = L_CS1 + 160 + q * 16;
      const u32 mq = q < 2 ? ~0u : 0u;
      f4 pp[2][3];
      {
        const f4 c0 = lcs(cb), c1 = lcs(cb + 80), c2 = lcs(L_CS1 + 224 + q * 80);
        const s8 A[4] = {lld<s8>(L_WPHI + lane * 16), lld<s8>(L_WPHI + 1024 + lane * 16), lld<s8>(L_PH2 + lane * 16), lld<s8>(L_PH2 + 1024 + lane * 16)};
#pragma unroll
        for (int t = 0; t < 2; ++t) {
          const float sk = rs1[t] * L2E;
          const v2u k0 = lnpk(acc[t][0], c0, nm1[t], sk), k1 = lnpk(acc[t][1], c1, nm1[t], sk), k2 = lnpk(acc[t][2], c2, nm1[t], uf(bsel(mq, fu(sk), fu(rs1[t]))));
          phi_mm(as8(v4u{k0.x, k0.y, k2.x, k2.y}), as8(v4u{k1.x, k1.y, k2.x, k2.y}), A, pp[t]);
          const u32 e0 = bsel(mq, 0x3F803F80u, k2.x), e1 = bsel(mq, 0x3F803F80u, k2.y), eb = L_IMG + (w * 32 + t * 16 + i16) * 264 + 224 + (q ^ 2) * 2;
          lst<u16>(eb, (u16)e0); lst<u16>(eb + 8, (u16)(e0 >> 16)); lst<u16>(eb + 16, (u16)e1); lst<u16>(eb + 24, (u16)(e1 >> 16));
          SB();
        }
      }
      {
        const f4 c3 = lcs(cb + 160), c4 = lcs(cb + 240);
#pragma unroll
        for (int t = 0; t < 2; ++t) {
          const u32 ib = L_IMG + (w * 32 + t * 16 + i16) * 264;
          lst<v2u>(ib + q * 8 + 160, lnpk(acc[t][3], c3, nm1[t], rs1[t]));
          lst<v2u>(ib + q * 8 + 192, lnpk(acc[t][4], c4, nm1[t], rs1[t]));
        }
      }
      SB();
#pragma unroll
      for (int t = 0; t < 2; ++t) {
        const Phi ph = phi_sm(pp[t]);
        const u32 ib = L_IMG + (w * 32 + t * 16 + i16) * 264 + q * 8;
        lst<v2u>(ib + 0, ph.pl[0]); lst<v2u>(ib + 32, ph.mi[0]); lst<v2u>(ib + 64, ph.pl[1]); lst<v2u>(ib + 96, ph.mi[1]);
        lst<v2u>(ib + 128, ph.ex);
      }
    }
    STAMP(3);
    kv_job<false>(ws, w, W_PART + (rank * 16 + team) * 8192);
    STAMP(4);
    bar_nw();
    {
      const u32 lane = lane_id(), q = lane >> 4, i16 = lane & 15;
      f4 qa[2][3] = {{z4, z4, z4}, {z4, z4, z4}};
      u32 bq = L_B + lane * 16, bq2 = L_B + 4096 + ((lane & 7) + 16 * q) * 16;
      OPQ(bq); OPQ(bq2);
      constexpr int QD = 4;
      s8 qf[QD + 1];
      auto ldq = [&](int f) LAM {
        const int t = f % 3, ks = f / 3;
        qf[f % (QD + 1)] = lld<s8>(t == 2 ? bq2 + (ks >> 1) * 5120 + (ks & 1) * 128 : bq + (ks >> 1) * 5120 + ((ks & 1) * 2 + t) * 1024);
      };
#pragma unroll
      for (int f = 0; f < QD; ++f) ldq(f);
      SB();
#pragma unroll
      for (int f = 0; f < 60; ++f) {
        if (f + QD < 60) ldq(f + QD);
        qa[0][f % 3] = mfma16(qf[f % (QD + 1)], as8(xa[0][f / 3]), qa[0][f % 3]);
        qa[1][f % 3] = mfma16(qf[f % (QD + 1)], as8(xa[1][f / 3]), qa[1][f % 3]);
        SB();
      }
      STAMP(5);
      const f4 c0 = lcs(L_CS1 + q * 16), c1 = lcs(L_CS1 + 80 + q * 16), c2 = lcs(L_CS1 + 64 + (q & 1) * 80);
      const s8 A[4] = {lld<s8>(L_WPHI + lane * 16), lld<s8>(L_WPHI + 1024 + lane * 16), lld<s8>(L_PH2 + lane * 16), lld<s8>(L_PH2 + 1024 + lane * 16)};
      f4 pp[2][3];
      Phi pq[2];
#pragma unroll
      for (int t = 0; t < 2; ++t) {
        const float sk = rs1[t] * L2E;
        const v2u k0 = lnpk(qa[t][0], c0, nm1[t], sk), k1 = lnpk(qa[t][1], c1, nm1[t], sk), k2 = lnpk(qa[t][2], c2, nm1[t], sk);
        phi_mm(as8(v4u{k0.x, k0.y, k2.x, k2.y}), as8(v4u{k1.x, k1.y, k2.x, k2.y}), A, pp[t]);
      }
      v4u b0[2], b1[2], bx[2];
#pragma unroll
      for (int t = 0; t < 2; ++t) {
        pq[t] = phi_sm(pp[t]);
        b0[t] = v4u{pq[t].pl[0].x, pq[t].pl[0].y, pq[t].mi[0].x, pq[t].mi[0].y};
        b1[t] = v4u{pq[t].pl[1].x, pq[t].pl[1].y, pq[t].mi[1].x, pq[t].mi[1].y};
        bx[t] = v4u{pq[t].ex.x, pq[t].ex.y, 0u, 0u};
        OPQ(b0[t]); OPQ(b1[t]); OPQ(bx[t]);
      }
      u32 mo = (lane & 1) ? ~0u : 0u, ve = i16 * 16 + q * 4;
      OPQ(mo); OPQ(ve);
      STAMP(7);
      lds_wait<0>(S_KV);
      STAMP(8);
      v2u it[8];
#pragma unroll
      for (int n = 0; n < 8; ++n) it[n] = lld<v2u>(L_KV + n * 512 + lane * 8);
      const s8 ae = as8(v4u{it[4].x & ~mo, it[4].y & ~mo, it[5].x & ~mo, it[5].y & ~mo}), ao = as8(v4u{it[4].x & mo, it[4].y & mo, it[5].x & mo, it[5].y & mo});
      const s8 ax = as8(v4u{it[7].x & ~mo, it[7].y & mo, 0u, 0u});
      f4 nx[2], n0[2], n1[2];
#pragma unroll
      for (int t = 0; t < 2; ++t) {
        nx[t] = mfma16(ae, as8(b0[t]), z4);
        nx[t] = mfma16(ao, as8(b1[t]), nx[t]);
        nx[t] = mfma16(ax, as8(bx[t]), nx[t]);
      }
#pragma unroll
      for (int t = 0; t < 2; ++t) {
        n0[t] = mfma16(as8(v4u{it[0].x, it[0].y, it[1].x, it[1].y}), as8(b0[t]), z4);
        n1[t] = mfma16(as8(v4u{it[2].x, it[2].y, it[3].x, it[3].y}), as8(b1[t]), z4);
        n0[t] = mfma16(as8(v4u{it[6].x, 0u, 0u, 0u}), as8(bx[t]), n0[t]);
        n1[t] = mfma16(as8(v4u{0u, it[6].y, 0u, 0u}), as8(bx[t]), n1[t]);
      }
#pragma unroll
      for (int t = 0; t < 2; ++t) {
        const float r0 = __builtin_amdgcn_rcpf(nx[t][2] + 1e-6f), r1 = __builtin_amdgcn_rcpf(nx[t][3] + 1e-6f);
        const u32 ob = W_O + (team * 8 + 2 * w + t) * 20480;
        st4c(pk(nx[t][0] * r0, nx[t][1] * r1), ws, ve, ob + 16384 + rank * 256);
        st16c(v4u{pk(n0[t][0] * r0, n0[t][1] * r0), pk(n0[t][2] * r0, n0[t][3] * r0), pk(n1[t][0] * r1, n1[t][1] * r1), pk(n1[t][2] * r1, n1[t][3] * r1)}, ws,
              lane * 16, ob + rank * 1024);
      }
    }
    {
      const u32 lane = lane_id(), q = lane >> 4, i16 = lane & 15;
      const u32 tk = th * 64 + w * 16 + i16;
      const u32 xo = tk * 1280 + q * 16, xso = team * 128 * 1280 + hq * 160;
      const rsrc_t rx = mk_rsrc(x, SEQ * DM * 2);
      const v4u xr0 = __builtin_amdgcn_raw_buffer_load_b128(rx, xo, xso, 0), xr1 = __builtin_amdgcn_raw_buffer_load_b128(rx, xo + 64, xso, 0);
      const v2u xr2 = __builtin_amdgcn_raw_buffer_load_b64(rx, tk * 1280 + 128 + q * 8, xso, 0);
      constexpr int OD = 6;
      s8 wf[OD][5];
      u32 bo[2] = {L_WO + lane * 16, L_WO + 51200 + lane * 16};
      OPQ(bo[0]); OPQ(bo[1]);
      const u32 oso = W_O + (team * 8 + th * 4 + w) * 20480;
      auto ldo1 = [&](int ks, int t) LAM { wf[ks % OD][t] = lld<s8>(bo[ks / 10] + (ks % 10) * 5120 + t * 1024); };
#pragma unroll
      for (int ks = 0; ks < OD; ++ks)
#pragma unroll
        for (int t = 0; t < 5; ++t) ldo1(ks, t);
      WAITV(3);
      flag_store(sws, tag, W_FS2 + (bid * 4 + w) * 128);
      lds_inc(S_OP);
      STAMP(9);
      f4 ha[5] = {f4{blo(xr0.x), bhi(xr0.x), blo(xr0.y), bhi(xr0.y)}, f4{blo(xr0.z), bhi(xr0.z), blo(xr0.w), bhi(xr0.w)}, f4{blo(xr1.x), bhi(xr1.x), blo(xr1.y), bhi(xr1.y)},
                  f4{blo(xr1.z), bhi(xr1.z), blo(xr1.w), bhi(xr1.w)}, f4{blo(xr2.x), bhi(xr2.x), blo(xr2.y), bhi(xr2.y)}};
#pragma unroll
      for (int t = 0; t < 5; ++t) OPQ(ha[t]);
      __builtin_amdgcn_s_waitcnt(0xC07F);
      __builtin_amdgcn_s_sleep(PD2);
      SB();
      poll_flags(ws, W_FS2 + ((team * 16 + lane) * 4 + th * 2 + (w >> 1)) * 128, lane < 16, tag);
      STAMP(10);
      v4u of[20];
      constexpr int PD = 12;
      auto ldof = [&](int ks) LAM { of[ks] = ld16c(ws, lane * 16 + (ks & 3) * 1024, oso + (ks >> 2) * 4096); };
#pragma unroll
      for (int ks = 0; ks < PD; ++ks) ldof(ks);
      SB();
#pragma unroll
      for (int ks = 0; ks < 20; ++ks) {
#pragma unroll
        for (int t = 0; t < 5; ++t) {
          ha[t] = mfma16(wf[ks % OD][t], as8(of[ks]), ha[t]);
          if (t == 0 && ks + PD < 20) ldof(ks + PD);
          if (t == 1 && ks + PD == 19) lds_inc(S_OL);
          if (ks + OD < 20) ldo1(ks + OD, t);
          SB();
        }
      }
      STAMP(11);
      v4u hc[2];
#pragma unroll
      for (int m = 0; m < 2; ++m)
        hc[m] = v4u{pk(ha[2 * m][0], ha[2 * m][1]), pk(ha[2 * m][2], ha[2 * m][3]), pk(ha[2 * m + 1][0], ha[2 * m + 1][1]), pk(ha[2 * m + 1][2], ha[2 * m + 1][3])};
      const v2u ht = {pk(ha[4][0], ha[4][1]), pk(ha[4][2], ha[4][3])};
      const u32 hb = W_H + (team * 4 + th * 2 + (w >> 1)) * 40960 + hq * 5120;
      const u32 t32 = (w & 1) * 16 + i16;
      st16c(hc[0], ws, (q >> 1) * 1024 + (t32 + 32 * (q & 1)) * 16, hb);
      st16c(hc[1], ws, (q >> 1) * 1024 + (t32 + 32 * (q & 1)) * 16, hb + 2048);
      st8c(ht, ws, (t32 + 32 * (q >> 1)) * 16 + 8 * (q & 1), hb + 4096);
      float a[2] = {0.f, 0.f}, c[2] = {0.f, 0.f};
#pragma unroll
      for (int m = 0; m < 2; ++m) {
#pragma unroll
        for (int j = 0; j < 4; ++j) { a[m] = dot2(hc[m][j], 0x3F803F80u, a[m]); c[m] = dot2(hc[m][j], hc[m][j], c[m]); }
        a[m] = dot2(ht[m], 0x3F803F80u, a[m]); c[m] = dot2(ht[m], ht[m], c[m]);
      }
      float a2 = a[0] + a[1], c2 = c[0] + c[1];
      red2(a2, c2);
      st8c(v2u{fu(a2), fu(c2)}, ws, q == 0 ? tk * 8 : OOB, W_STAT + (team * 8 + hq) * 1024);
      lst<v4u>(L_HST + w * 2560 + lane * 16, hc[0]);
      lst<v4u>(L_HST + w * 2560 + 1024 + lane * 16, hc[1]);
      lst<v2u>(L_HST + w * 2560 + 2048 + lane * 8, ht);
    }
    WAITV(0);
    flag_store(sws, tag, W_FS3 + (bid * 4 + w) * 128);
    lds_inc(S_WOC);
    STAMP(12);
    __builtin_amdgcn_s_sleep(PD2);
    const u32 lane3 = lane_id();
#define lane lane3
    poll_flags(ws, W_FS3 + ((team * 16 + (w >> 1) * 8 + (lane >> 1)) * 4 + 2 * (w & 1) + (lane & 1)) * 128, lane < 16, tag);
    STAMP(13);
    v4u wl[3][5];
    u32 wlv;
    auto ldwl = [&](int k) LAM {
      const int i = 11 + k / 5;
      if (k == 0) wlv = lane_id() * 16;
      wl[k / 5][k % 5] = ld16c(ws, ((i >= 12 && w == 3) ? OOB : wlv) + (k % 5) * 1024, W_WD + (hq * 54 + 2 * w + (i >> 1) * 8 + (i & 1)) * 5120);
    };
    {
      const u32 hh = lane >> 5, t32 = lane & 31;
      v2u sv[4];
#pragma unroll
      for (int n = 0; n < 4; ++n) sv[n] = ld8c(ws, (hh * 4 + n) * 1024 + t32 * 8, W_STAT + team * 8192 + w * 256);
      v4u hf[40];
      constexpr int HD = 12;
      const u32 hso = W_H + (team * 4 + w) * 40960;
      auto ldh = [&](int s) LAM { hf[s] = ld16c(ws, lane * 16 + (s & 3) * 1024, hso + (s >> 2) * 4096); };
#pragma unroll
      for (int s = 0; s < HD; ++s) ldh(s);
      SB();
      lds_wait(L_LAND, 4);
      u32 lb[3] = {L_HS + lane * 16, L_HS + 3 * HSLOT + lane * 16, L_HS + 6 * HSLOT + lane * 16};
      OPQ(lb[0]); OPQ(lb[1]); OPQ(lb[2]);
      auto ldw = [&](int n, int s) LAM -> s8 {
        const int hs = (2 * n + s / 20 + 5) % 7;
        return lld<s8>(lb[hs / 3] + (hs % 3) * HSLOT + (s % 20) * 1024);
      };
      auto ldcs = [&](int n) LAM -> f16v {
        const u32 csb = L_CS3 + (lane_id() & 32);
        const f4 g0 = lld<f4>(csb + n * 128), g1 = lld<f4>(csb + n * 128 + 16), u0 = lld<f4>(csb + n * 128 + 64), u1 = lld<f4>(csb + n * 128 + 80);
        return __builtin_shufflevector(__builtin_shufflevector(g0, g1, 0, 1, 2, 3, 4, 5, 6, 7), __builtin_shufflevector(u0, u1, 0, 1, 2, 3, 4, 5, 6, 7), 0, 1, 2, 3, 4,
                                       5, 6, 7, 8, 9, 10, 11, 12, 13, 14, 15);
      };
      f16v ac[3];
      ac[0] = ldcs(0); ac[1] = ldcs(1); ac[2] = ldcs(2);
      constexpr int RD = RDEP;
      s8 rg[RD];
#pragma unroll
      for (int f = 0; f < RD; ++f) rg[f] = ldw(f & 1, f >> 1);
      SB();
      float nmean, cK1, cK2, cR;
      {
        const float a = (uf(sv[0].x) + uf(sv[1].x)) + (uf(sv[2].x) + uf(sv[3].x)), c = (uf(sv[0].y) + uf(sv[1].y)) + (uf(sv[2].y) + uf(sv[3].y));
        auto r = __builtin_amdgcn_permlane32_swap(fu(a), fu(a), false, false);
        auto r2 = __builtin_amdgcn_permlane32_swap(fu(c), fu(c), false, false);
        nmean = -(uf(r[0]) + uf(r[1])) * (1.0f / DM);
        cR = __builtin_fmaxf(__builtin_fmaf(-nmean, nmean, (uf(r2[0]) + uf(r2[1])) * (1.0f / DM)), 0.f) + 1e-5f;
        const float rstd = __builtin_amdgcn_rsqf(cR), k1 = -2.0f * 0.7978845608028654f * 1.4426950408889634f;
        cK1 = k1 * rstd; cK2 = cK1 * 0.044715f * rstd * rstd;
      }
#pragma unroll
      for (int j = 0; j < 16; ++j) { ac[0][j] *= nmean; ac[1][j] *= nmean; }
      SB();
      u32 lv;
#pragma unroll
      for (int f = 0; f < 80; ++f) {
        ac[f & 1] = mfma32(rg[f % RD], as8(hf[f >> 1]), ac[f & 1]);
        const int fn = f + RD;
        rg[f % RD] = fn < 80 ? ldw(fn & 1, fn >> 1) : ldw(2, fn - 80);
        if (f >= 8 && f < 24) ac[2][f - 8] *= nmean;
        if ((f & 1) == 0 && (f >> 1) + HD < 40) ldh((f >> 1) + HD);
        if (f == 2 * (39 - HD) + 1) lds_inc(S_HL);
        if (f == 80 - RD) lds_inc(L_CONS + 4);
        SB();
      }
      OPQ(cK1); OPQ(cK2); OPQ(cR);
      STAMP(14);
      const u32 gvo = (t32 >> 4) * 54 * 1024 + ((t32 & 15) + 16 * hh) * 16 + (rank & 1) * 512;
      const u32 gso = W_G + (team * 8 + 2 * w) * 54 * 1024 + (rank >> 1) * 1024;
      const u32 fso = W_FS4 + (bid * 4 + w) * 1024;
      float tv, au, pu, pr, ov[2];
      v4u gp;
      auto gstep = [&](const f16v& a, int e) LAM {
        const int j = e / 3, m = e % 3;
        if (m == 0 && j < 8) { tv = a[j] * a[j]; tv = __builtin_fmaf(tv, cK2, cK1); tv = tv * a[j]; au = a[j] * a[8 + j]; }
        if (m == 1) tv = __builtin_amdgcn_exp2f(tv);
        if ((m == 1 && j > 0) || e == 24) { ov[(j - 1) & 1] = pu * pr; if (((j - 1) & 1) == 1) gp[(j - 1) >> 1] = pk(ov[0], ov[1]); }
        if (m == 2) { pr = __builtin_amdgcn_rcpf(__builtin_fmaf(tv, cR, cR)); pu = au; }
      };
      auto pass = [&](auto nc) LAM {
        constexpr int n = decltype(nc)::v;
        sfor<0, 40>([&](auto sc) LAM {
          constexpr int s = decltype(sc)::v, g = (n - 2) * 40 + s, sn = s + RD;
          constexpr int G[6] = {1, 31, 61, 91, 122, 161}, I[4] = {26, 56, 86, 150};
          ac[n % 3] = mfma32(rg[s % RD], as8(hf[s]), ac[n % 3]);
          if constexpr (sn < 40) rg[s % RD] = ldw(n, sn);
          else if constexpr (n < 6) rg[s % RD] = ldw(n + 1, sn - 40);
          sfor<0, 6>([&](auto uc) LAM {
            constexpr int u = decltype(uc)::v;
            if constexpr (g >= G[u] && g <= G[u] + 24 && (!(ABL & 1) || g - G[u] < 6 || g - G[u] == 24)) gstep(ac[u % 3], g - G[u]);
            if constexpr (g == G[u] + 24) {
              if constexpr (u >= 2) { if (!(ABL & 2)) WAITV(0); flag_store(sws, tag, fso + u * 128); }
              st16c(gp, ws, gvo, gso + u * 8192);
            }
          });
          sfor<0, 4>([&](auto kc) LAM {
            constexpr int k = decltype(kc)::v;
            if constexpr (g == I[k] - 3) ac[k % 3] = ldcs(3 + k);
            if constexpr (g > I[k] && g <= I[k] + 4) {
#pragma unroll
              for (int j = 0; j < 4; ++j) ac[k % 3][4 * (g - I[k] - 1) + j] *= nmean;
            }
          });
          if constexpr (WLP && n == 6 && s >= 25) ldwl(s - 25);
          if constexpr (s == 40 - RD) lds_inc(L_CONS + 4 * n);
          if constexpr (s == 8) lv = *(volatile LAS u32*)(L_LAND + 4 * (n - 1));
          if constexpr (s == 14 && !(ABL & 4)) lds_check(lv, L_LAND + 4 * (n - 1), 4);
          SB();
        });
      };
      pass(IC<2>{}); STAMP(26); pass(IC<3>{}); STAMP(27); pass(IC<4>{}); STAMP(28); pass(IC<5>{}); STAMP(29);
      pass(IC<6>{});
      STAMP(15);
#pragma unroll
      for (int e = 0; e < 25; ++e) gstep(ac[0], e);
      st16c(gp, ws, u7 ? gvo : OOB, gso + 6 * 8192);
      if (WLP) WAITV(16); else WAITV(1);
      flag_store(sws, tag, fso + 6 * 128);
    }
#undef lane
    STAMP(30);
    {
      const u32 ln = lane_id(), vl = ln * 16;
      const u32 fv = W_FS4 + ((team * 16 + 4 * w + (ln >> 4)) * 4 + 2 * th + ((ln >> 3) & 1)) * 1024;
      const u32 gso = W_G + (team * 8 + th * 4) * 54 * 1024 + 2 * w * 1024;
      const u32 vz = w < 3 ? vl : OOB;
      u32 gsr[4];
#pragma unroll
      for (int r = 0; r < 4; ++r) gsr[r] = sgpr(gso + ((r + w + 1) & 3) * (54 * 1024));
      v4u wf[6][5], gf[6][4];
      auto ldg = [&](int i) LAM {
#pragma unroll
        for (int r = 0; r < 4; ++r) gf[i - 8][r] = ld16c(ws, i >= 12 ? vz : vl, gsr[r] + ((i >> 1) * 8 + (i & 1)) * 1024);
        SB();
      };
      v2u fl = ld8c(ws, fv, 5 * 128);
      if (!WLP) sfor<0, 5>([&](auto kc) LAM { ldwl(decltype(kc)::v); });
      if (WLP) WAITV(2); else WAITV(7);
      flag_store(sws, tag, W_FS4 + (bid * 4 + w) * 1024 + 7 * 128);
      STAMP(16);
      if (!wall(fl.x == tag.x && fl.y == tag.y)) poll_level(ws, fv, tag, 5);
      asm volatile("" ::: "memory");
      ldg(8); ldg(9);
      fl = ld8c(ws, fv, 6 * 128);
      if (!WLP) sfor<5, 15>([&](auto kc) LAM { ldwl(decltype(kc)::v); });
#pragma unroll
      for (int i = 11; i < 14; ++i)
#pragma unroll
        for (int t = 0; t < 5; ++t) wf[i - 8][t] = wl[i - 11][t];
      u32 sb = L_HS + 2 * HSLOT + w * 5120 + vl;
      OPQ(sb);
      lds_wait(S_SP);
#pragma unroll
      for (int i = 8; i < 10; ++i)
#pragma unroll
        for (int t = 0; t < 5; ++t) wf[i - 8][t] = lld<v4u>(sb + (i - 8) * HSLOT + t * 1024);
      if (!wall(fl.x == tag.x && fl.y == tag.y)) poll_level(ws, fv, tag, 6);
      asm volatile("" ::: "memory");
      STAMP(31);
      ldg(10);
      f4 da[4][5];
      auto step = [&](int i) LAM {
#pragma unroll
        for (int m = 0; m < 20; ++m) da[m / 5][m % 5] = mfma16(as8(wf[i - 8][m % 5]), as8(gf[i - 8][m / 5]), i == 8 ? z4 : da[m / 5][m % 5]);
        SB();
      };
      step(8);
      ldg(11);
      fl = ld8c(ws, fv, 7 * 128);
#pragma unroll
      for (int t = 0; t < 5; ++t) wf[2][t] = lld<v4u>(sb + 2 * HSLOT + t * 1024);
      SB();
      step(9);
      if (w < 3 && !wall(fl.x == tag.x && fl.y == tag.y)) poll_level(ws, fv, tag, 7);
      asm volatile("" ::: "memory");
      STAMP(1);
      ldg(12); ldg(13);
      step(10); step(11);
      STAMP(6);
      step(12);
#pragma unroll
      for (int m = 0; m < 21; ++m) {
        if (m < 20) da[m / 5][m % 5] = mfma16(as8(wf[5][m % 5]), as8(gf[5][m / 5]), da[m / 5][m % 5]);
        if (m >= 6) lst<f4>(sb + ((m - 6) / 5) * HSLOT + ((m - 6) % 5) * 1024, da[(m - 6) / 5][(m - 6) % 5]);
        SB();
      }
      u32 rb[4];
#pragma unroll
      for (int r = 0; r < 3; ++r) { rb[r] = L_HS + (2 + r) * HSLOT + ((w - r - 1) & 3) * 5120 + vl; OPQ(rb[r]); }
      rb[3] = L_HS + ((w + 5) % 7) * HSLOT + vl;
      OPQ(rb[3]);
      const rsrc_t ro = mk_rsrc(out, SEQ * DM * 2);
      const u32 oso = (team * 128 + th * 64 + w * 16) * 1280 + hq * 160;
      u32 ovo = (ln & 15) * 1280 + (ln >> 4) * 16, ov2 = (ln & 15) * 1280 + 128 + (ln >> 4) * 8;
      OPQ(ovo); OPQ(ov2);
      bar();
      STAMP(17);
      f4 r[5][4], rr[5];
#pragma unroll
      for (int n = 0; n < 5; ++n)
#pragma unroll
        for (int j = 0; j < 4; ++j) r[n][j] = lld<f4>(rb[j] + n * 1024);
      SB();
#pragma unroll
      for (int n = 0; n < 5; ++n) rr[n] = ((da[3][n] + r[n][0]) + (r[n][1] + r[n][2])) + r[n][3];
#pragma unroll
      for (int m = 0; m < 2; ++m)
        st16p(v4u{pk(rr[2 * m][0], rr[2 * m][1]), pk(rr[2 * m][2], rr[2 * m][3]), pk(rr[2 * m + 1][0], rr[2 * m + 1][1]), pk(rr[2 * m + 1][2], rr[2 * m + 1][3])}, ro,
              ovo + 64 * m, oso);
      __builtin_amdgcn_raw_buffer_store_b64(v2u{pk(rr[4][0], rr[4][1]), pk(rr[4][2], rr[4][3])}, ro, ov2, oso, 0);
      if (b == 0 && w == 0) flag_store(sws, tag, W_EPOCH);
    }
    STAMP(18);
    DUMP();
  } else {
    const u32 j = wv;
    __builtin_amdgcn_s_setprio(HPRIO);
    const u32 lane = lane_id();
    const u32 q = lane >> 4, i16 = lane & 15, tq = (lane >> 2) & 3, tp = lane & 3, vl = lane * 16;
    const u32 p1 = j * 256 + b;
    const bool isq = b < 150;
    const u32 pc5 = (b * 52429u) >> 18, pcb1 = isq ? pc5 : pc5 - 30, pks = b < 200 ? (b - pc5 * 5) * 4 + j : 20;
    u32 pv0, ps0, psn, pl0 = L_A + j * 4096;
    {
      const u32 jr = lane >> 3;
      const bool ex = pks >= 16;
      const u32 ra = 20 * (jr & 1) + 16 + (jr >> 1), rb = 20 * (jr >> 2) + (jr & 3);
      const u32 m1 = isq ? 3840 : 1280, m2 = isq ? 8 : ex ? 40 : 4, m3 = isq ? 32 : ex ? 160 : 40, m4 = (!isq && ex) ? 2560 : 0;
      pv0 = (isq ? jr : ex ? ra : rb) * m1 + (lane & 7) * 16;
      psn = m2 * m1;
      ps0 = (m3 * pks - m4) * m1 + pcb1 * 128;
    }
    OPS(ps0); OPS(psn); OPS(pl0); OPQ(pv0);
    if (lane < 32) lst<u32>(L_SYNC + lane * 4, 0u);
    GET_ARGS();
    {
      const v4u sp = mk_srd((const void*)(isq ? ka[1] : ka[3]), pks >= 20 ? 0u : isq ? DM * 3 * DM * 2 : DM * DM * 2);
#pragma unroll
      for (int n = 0; n < 4; ++n) { if (NT & 1) dma16n(pv0, sp, ps0 + n * psn, pl0 + n * 1024); else dma16(pv0, sp, ps0 + n * psn, pl0 + n * 1024); }
    }
    bar();
    u64 ep;
    asm volatile("s_load_dwordx2 %0, %1, 0x0" : "=s"(ep) : "s"(wsp));
    if (j == 3) {
      const v4u sp = mk_srd((const void*)ka[2], 32 * 400 * 2);
      dma16(vl, sp, rank * 1600, L_WPHI); dma16(vl, sp, rank * 1600 + 1024, L_WPHI + 1024);
    }
    u32 selv = i16 == q ? 0x3F803F80u : 0u;
    OPQ(selv);
    const s8 sel = as8(v4u{selv, selv, selv, selv});
    const u32 pi4 = ((tp & 1) << 1) | (tp >> 1);
    {
      u32 dv, cv;
      if (isq) {
        const u32 sec = (pcb1 * 26) >> 8, cn = (pcb1 - sec * 10) * 64 + lane, head = (cn * 3277) >> 16, d = cn - head * 20, hh = head & 1;
        const u32 par = pks & 1;
        const u32 tb = sec == 0 ? par * 2048 : sec * 3072 - 3072, xb = sec == 0 ? 4096 - 256 + par * 128 : 2048 - 256 - 128 + sec * 128;
        const u32 ds = W_W1 + (sec == 0 ? 102400 + (pks >> 1) * 5120 : pks * 5120);
        dv = (head >> 1) * 153600 + d * 16 + (d < 16 ? tb + hh * 1024 : xb + hh * 64) + ds;
        cv = i16 < 4 ? W_CS1 + pks * 7680 + pcb1 * 256 + i16 * 64 + q * 16 : OOB;
      } else {
        const u32 sgi = 4 * pcb1 + q, cc = (sgi * 52) >> 8, pos = sgi - cc * 5;
        const u32 o1 = ((pos & 2) + (i16 >> 3)) * 1024 + (8 * (pos & 1) + (i16 & 7)) * 16, o2 = 4096 + swap23(i16) * 16;
        dv = pks >= 20 ? OOB : cc * 102400 + (pos < 4 ? o1 : o2) + W_WO + pks * 5120;
        cv = OOB;
      }
      u32 rb = L_A + j * 4096 + tq * 128 + q * 32 + (isq ? tp : pi4) * 8;
      OPQ(dv); OPQ(rb); OPQ(cv);
      WAITV(0);
      v4u f[4];
#pragma unroll
      for (int n = 0; n < 4; ++n) f[n] = __builtin_bit_cast(v4u, cat(trd(rb + n * 1024), trd(rb + n * 1024 + 512)));
      f4 cs = z4;
#pragma unroll
      for (int n = 0; n < 4; ++n) { st16c(f[n], ws, dv + n * 256, 0); cs = mfma16(as8(f[n]), sel, cs); }
      st16c(__builtin_bit_cast(v4u, cs), ws, cv, 0);
      lds_inc(S_ST);
      SB();
    }
    STAMP(1);
    GET_EPOCH();
    WAITV(0);
    lds_inc(S_PST);
    if (j == 0) { lds_wait<0>(S_PST); flag_store(sws, tag, W_FA + b * FAS); }
    STAMP(9);
    {
      v4u sent = {SENT, SENT, SENT, SENT};
      OPQ(sent);
      st16c(sent, ws, vl, W_PART + (rank * 16 + team) * 8192 + j * 2048);
      st16c(sent, ws, vl, W_PART + (rank * 16 + team) * 8192 + j * 2048 + 1024);
      st16c(sent, ws, lane < 4 ? vl : OOB, W_RED + rank * 4096 + team * 256 + j * 64);
    }
    if (j == 3) {
      v2u pl[2][4];
      const int src = ((lane & ~3u) | tq) * 4;
#pragma unroll
      for (int h = 0; h < 2; ++h) {
        const u32 pb = L_WPHI + h * 800;
        pl[h][0] = __builtin_bit_cast(v2u, trd(pb + (4 * q + tq) * 40 + 8 * tp)); pl[h][1] = __builtin_bit_cast(v2u, trd(pb + (16 + tq) * 40 + 8 * tp));
        pl[h][2] = __builtin_bit_cast(v2u, trd(pb + (4 * q + tq) * 40 + 32)); pl[h][3] = __builtin_bit_cast(v2u, trd(pb + (16 + tq) * 40 + 32));
      }
      asm volatile("" ::: "memory");
#pragma unroll
      for (int h = 0; h < 2; ++h) {
        const u32 qh = q == (u32)h ? ~0u : 0u, rh = tp == (u32)h ? ~0u : 0u;
        lst<v4u>(L_WPHI + h * 1024 + vl, v4u{pl[h][0].x, pl[h][0].y, pl[h][1].x & qh, pl[h][1].y & qh});
        lst<v4u>(L_PH2 + h * 1024 + vl, v4u{(u32)__builtin_amdgcn_ds_bpermute(src, pl[h][2].x) & rh, (u32)__builtin_amdgcn_ds_bpermute(src, pl[h][2].y) & rh,
                                            (u32)__builtin_amdgcn_ds_bpermute(src, pl[h][3].x) & rh & qh, (u32)__builtin_amdgcn_ds_bpermute(src, pl[h][3].y) & rh & qh});
      }
    }
    u32 rdv[2], rdn[2], rcv[2], rvo[2], rs0[2], rsn[2];
    v4u rsp[2];
#pragma unroll
    for (int e = 0; e < 2; ++e) {
      const u32 piece = p1 + e * 1024;
      const bool isg = piece < 1080;
      const u32 p2 = isg ? piece : piece - 1080;
      const u32 pk_ = (p2 * (isg ? 1214u : 6554u)) >> 16, pcb = p2 - pk_ * (isg ? 54 : 10);
      const u32 mat = pcb >= 27 ? 1 : 0, rs = isg ? 3456 : 1280, u0 = 4 * (pcb - mat * 27);
      rsp[e] = mk_srd((const void*)(!isg ? ka[6] : mat ? ka[5] : ka[4]), piece < 1620 ? DM * FF * 2 : 0u);
      rs0[e] = pk_ * 32 * rs + u0 * 32; rsn[e] = 8 * rs; rvo[e] = (lane >> 3) * rs + (lane & 7) * 16;
      const u32 sgi = u0 + q, cc = (sgi * 52) >> 8, pos = sgi - cc * 5;
      const u32 o1 = ((pos & 2) + (i16 >> 3)) * 1024 + (8 * (pos & 1) + (i16 & 7)) * 16, o2 = 4096 + swap23(i16) * 16;
      const u32 dg = W_WGU + sgi * 40960 + i16 * 16 + pk_ * 2048 + mat * 256, dd = W_WD + cc * (54 * 5120) + (pos < 4 ? o1 : o2) + pk_ * 5120;
      rdv[e] = isg ? dg : piece < 1620 ? dd : OOB;
      rdn[e] = isg ? 512 : 256;
      rcv[e] = (isg && i16 < 4) ? W_CSP + (pk_ * 108 + u0 + i16) * 128 + mat * 64 + (((q & 1) << 1) | (q >> 1)) * 16 : OOB;
      OPQ(rdv[e]); OPQ(rcv[e]); OPQ(rvo[e]); OPS(rs0[e]); OPS(rsn[e]); OPS(rdn[e]);
      rsp[e].x = sgpr(rsp[e].x); rsp[e].y = sgpr(rsp[e].y); rsp[e].z = sgpr(rsp[e].z);
    }
    u32 fav, fcv = (j == 3 && lane < 16) ? (lane * 16 + rank) * 128 : OOB;
    {
      const u32 g = (lane * 52) >> 8, i5 = lane - g * 5;
      const u32 cq = (g >> 1) * 10 + ((40 * rank + (g & 1) * 39) >> 6), co = 30 + ((80 * hq + (g & 1) * 79) >> 6);
      fav = lane >= 40 ? OOB : ((g < 6 ? cq : co) * 5 + i5) * FAS;
      OPQ(fav); OPQ(fcv);
    }
    u32 c1v;
    { const u32 t = (j & 1) * 64 + lane, sec = (t * 1639) >> 16; c1v = t < 120 ? (sec * 600 + t) * 4 + (j >> 1) * (10 * 7680) : OOB; OPQ(c1v); }
    v4u rf[10];
    auto rst = [&](int k) LAM { if (k % 5 < 4) st16c(rf[k], ws, rdv[k / 5], (k % 5) * rdn[k / 5]); else st16c(rf[k], ws, rcv[k / 5], 0); };
    STAMP(23);
    if (j == 0) { __builtin_amdgcn_s_sleep(FAD); poll_mask<FAR>(ws, fav, W_FA, tag, ld8c(ws, fav, W_FA)); }
    WAITV(0);
    lds_inc(S_FA);
    lds_wait<1>(S_FA);
    STAMP(24);
    {
      u32 w1s = W_W1 + rank * 153600 + j * 5120, w1l = L_A + j * 5120;
      OPS(w1s); OPS(w1l);
      auto dd = [&](int m) LAM { dma5c(vl, sws, w1s + m * 20480, w1l + m * 20480); };
      u32 cv[10];
      dd(0); dd(1);
      WAITV(5); lds_inc(S_L1);
      dd(2); dd(3);
      WAITV(10); lds_inc(S_L1 + 4);
      dd(4);
      WAITV(10); lds_inc(S_L1 + 8);
#pragma unroll
      for (int k = 0; k < 10; ++k) cv[k] = ld4c(ws, c1v, W_CS1 + k * 7680 + rank * 160);
      v4u rt[2][4];
#pragma unroll
      for (int e = 0; e < 2; ++e) {
        const rsrc_t sp = mk_rsrc((const void*)(((u64)rsp[e].y << 32) | rsp[e].x), rsp[e].z);
#pragma unroll
        for (int n = 0; n < 4; ++n) rt[e][n] = __builtin_amdgcn_raw_buffer_load_b128(sp, rvo[e], rs0[e] + n * rsn[e], (NT & 2) ? 2 : 0);
      }
      WAITV(23); lds_inc(S_L1 + 12);
      WAITV(18); lds_inc(S_L1 + 16);
      {
        const float c = (((uf(cv[0]) + uf(cv[1])) + (uf(cv[2]) + uf(cv[3]))) + ((uf(cv[4]) + uf(cv[5])) + (uf(cv[6]) + uf(cv[7])))) + (uf(cv[8]) + uf(cv[9]));
        lst<float>(L_CS1 + j * 256 + lane * 4, c);
      }
      lds_inc(S_CS);
      const u32 rb = L_KV + j * 1024 + tq * 128 + q * 32 + pi4 * 8;
#pragma unroll
      for (int e = 0; e < 2; ++e) {
        f4 cs = z4;
#pragma unroll
        for (int n = 0; n < 4; ++n) {
          lst<v4u>(L_KV + j * 1024 + vl, rt[e][n]);
          asm volatile("" ::: "memory");
          rf[5 * e + n] = __builtin_bit_cast(v4u, cat(trd(rb), trd(rb + 512)));
          asm volatile("" ::: "memory");
        }
#pragma unroll
        for (int n = 0; n < 4; ++n) cs = mfma16(as8(rf[5 * e + n]), sel, cs);
        rf[5 * e + 4] = __builtin_bit_cast(v4u, cs);
        OPQ(rf[5 * e + 4]);
      }
      lds_wait(S_XD);
      rst(0); dd(5); rst(1); rst(2); dd(6); rst(3); rst(4);
      if (j < 2) dd(7);
      rst(5);
    }
    STAMP(5);
    STAMP(25);
    u32 wos = W_WO + hq * 102400;
    OPS(wos);
    v4u wf[11][5], gf[8][4];
    const u32 wso = W_WD + (hq * 54 + 2 * j) * 5120;
    const u32 gso = W_G + (team * 8 + th * 4) * 54 * 1024 + 2 * j * 1024;
    auto ldw1 = [&](int i, int t) LAM { wf[i][t] = ld16c(ws, vl + t * 1024, wso + ((i >> 1) * 8 + (i & 1)) * 5120); };
    auto ldg1 = [&](int i, int r) LAM { gf[i][r] = ld16c(ws, vl, gso + r * 54 * 1024 + ((i >> 1) * 8 + (i & 1)) * 1024); };
    STAMP(2);
    lds_wait<2>(S_KVD);
#pragma unroll
    for (int n = 0; n < 3; ++n) { rst(6 + n); dma5c(vl, sws, wos + (8 + 4 * n + j) * 5120, L_WO + (8 + 4 * n + j) * 5120); }
    rst(9);
    const v2u fc = ld8c(ws, fcv, W_FC);
    WAITV(21);
    STAMP(3);
    kv_job<true>(ws, j + 4, W_PART + (rank * 16 + team) * 8192);
    STAMP(4);
    WAITV(1);
    bar_nw();
    if (j == 1) flag_store(sws, tag, W_FB + bid * 32);
    dma5c(vl, sws, wos + j * 5120, L_WO + j * 5120);
    dma5c(vl, sws, wos + (4 + j) * 5120, L_WO + (4 + j) * 5120);
    if (j == 3) {
      const u32 pvo = (lane >> 5) * 8 * 8192 + (lane & 31) * 16, pso = W_PART + rank * 16 * 8192 + team * 512;
      v4u pv[8];
      __builtin_amdgcn_s_setprio(3);
      poll_mask<2>(ws, fcv, W_FC, tag, fc);
      __builtin_amdgcn_s_sleep(HOPD);
#pragma unroll 1
      for (u32 s = 0; s < SPIN_CAP; ++s) {
#pragma unroll
        for (int n = 0; n < 8; ++n) pv[n] = ld16c(ws, pvo, pso + n * 8192);
        SB();
        u32 mx = 0;
#pragma unroll
        for (int n = 0; n < 8; ++n) mx = umax4(mx, pv[n]);
        if (wall(mx != SENT)) break;
        asm volatile("" ::: "memory");
      }
      float sm[4];
#pragma unroll
      for (int k = 0; k < 4; ++k) {
        const float a = ((uf(pv[0][k]) + uf(pv[1][k])) + (uf(pv[2][k]) + uf(pv[3][k]))) + ((uf(pv[4][k]) + uf(pv[5][k])) + (uf(pv[6][k]) + uf(pv[7][k])));
        auto r = __builtin_amdgcn_permlane32_swap(fu(a), fu(a), false, false);
        sm[k] = uf(r[0]) + uf(r[1]);
      }
      st8c(v2u{san(pk(sm[0], sm[1])), san(pk(sm[2], sm[3]))}, ws, lane < 32 ? lane * 8 : OOB, W_RED + rank * 4096 + team * 256);
      STAMP(6);
      __builtin_amdgcn_s_sleep(HOPE);
      v4u kv[4];
#pragma unroll 1
      for (u32 s = 0; s < SPIN_CAP; ++s) {
#pragma unroll
        for (int n = 0; n < 4; ++n) kv[n] = ld16c(ws, vl, W_RED + rank * 4096 + n * 1024);
        SB();
        u32 mx = 0;
#pragma unroll
        for (int n = 0; n < 4; ++n) mx = umax4(mx, kv[n]);
        if (wall(mx != SENT)) break;
        asm volatile("" ::: "memory");
      }
#pragma unroll
      for (int n = 0; n < 4; ++n) lst<v4u>(L_KV + n * 1024 + vl, kv[n]);
      __builtin_amdgcn_s_setprio(HPRIO);
    }
    WAITV(0);
    lds_inc(S_KV);
    STAMP(7);
    if (j == 2) { __builtin_amdgcn_s_setprio(3); __builtin_amdgcn_s_sleep(FBD); poll256(ws, W_FB, 32, lane, tag); lds_inc(S_FB); __builtin_amdgcn_s_setprio(HPRIO); }
    lds_wait(S_FB, 1);
    STAMP(22);
    if (j == 3) lds_wait(S_OP);
    sfor<0, 5 * NW1>([&](auto ic) LAM { ldw1(decltype(ic)::v / 5, decltype(ic)::v % 5); });
    SB();
    auto stage_u = [&](u32 hs) LAM {
      const u32 so = W_WGU + (16 * (hs >> 1) + rank) * 40960 + ((hs & 1) * 4 + j) * 5120, dst = L_HS + ((hs + 5) % 7) * HSLOT + j * 5120;
      if (hs >= 7) waitv<PACE>();
      if (!(ABL & 8) || hs < 7) dma5c(vl, sws, so, dst);
    };
    lds_wait(S_OP);
    stage_u(0); stage_u(1);
    {
      const u32 t = j * 64 + lane, u = 16 * (t >> 5) + rank;
      if (u < 108) {
        u32 cv[20];
#pragma unroll
        for (int k = 0; k < 20; ++k) cv[k] = ld4c(ws, u * 128 + (t & 31) * 4, W_CSP + k * 108 * 128);
        SB();
        float c = 0.f;
#pragma unroll
        for (int k = 0; k < 20; ++k) c += uf(cv[k]);
        lst<float>(L_CS3 + t * 4, c);
      }
    }
    u32 sdb[3] = {L_HS + j * 5120 + vl, L_HS + 3 * HSLOT + j * 5120 + vl, L_HS + 6 * HSLOT + j * 5120 + vl};
    OPQ(sdb[0]); OPQ(sdb[1]); OPQ(sdb[2]);
    auto spill = [&](int i) LAM {
#pragma unroll
      for (int t = 0; t < 5; ++t) lst<v4u>(sdb[((i + 1) % 7) / 3] + (((i + 1) % 7) % 3) * HSLOT + t * 1024, wf[i][t]);
      SB();
    };
    auto ldw = [&](auto lo, auto hi) LAM {
      sfor<decltype(lo)::v * 5, decltype(hi)::v * 5>([&](auto ic) LAM { if (decltype(ic)::v % 5 == 0) waitv<PACE>(); ldw1(decltype(ic)::v / 5, decltype(ic)::v % 5); });
      SB();
    };
    auto ldg = [&](int i) LAM {
      waitv<PACE>();
#pragma unroll
      for (int r = 0; r < 4; ++r) ldg1(i, r);
      SB();
    };
    STAMP(10);
    lds_wait(S_OL);
    ldw(IC<NW1>{}, IC<NW2>{});
    lds_wait<2>(S_WOC);
    STAMP(11);
    auto landed = [&](int k) LAM { lds_inc(L_LAND + 4 * k); };
    stage_u(2); stage_u(3); stage_u(4);
    WAITV(0); landed(0);
    STAMP(13);
    stage_u(5); stage_u(6);
    WAITV(0); landed(1);
    lds_wait<2>(S_HL);
    ldw(IC<NW2>{}, IC<NW3>{});
    lds_wait<1>(L_CONS + 4);
    stage_u(7); stage_u(8); stage_u(9);
    WAITV(5); landed(2);
    stage_u(10);
    ldw(IC<NW3>{}, IC<9>{});
    waitv<5 * (9 - NW3)>(); landed(3);
    lds_wait<2>(L_CONS + 8);
    stage_u(11);
    if (u7) stage_u(12);
    ldw(IC<9>{}, IC<11>{});
    WAITV(10); landed(4);
    const u32 fvo = W_FS4 + ((team * 16 + 4 * j + (lane >> 4)) * 4 + 2 * th + ((lane >> 3) & 1)) * 1024;
    lds_wait<2>(L_CONS + 12);
    if (u7) stage_u(13);
    spill(4);
    WAITV(0); landed(5);
    STAMP(26);
    lds_wait<2>(L_CONS + 16);
    STAMP(27);
    v2u fl = ld8c(ws, fvo, 2 * 128);
    spill(5); spill(6);
    if (!wall(fl.x == tag.x && fl.y == tag.y)) poll_level(ws, fvo, tag, 2);
    asm volatile("" ::: "memory");
    ldg(0); ldg(1); ldg(2);
    lds_wait<2>(L_CONS + 20);
    STAMP(28);
    fl = ld8c(ws, fvo, 3 * 128);
    spill(7); spill(8); ldg(3);
    if (!wall(fl.x == tag.x && fl.y == tag.y)) poll_level(ws, fvo, tag, 3);
    asm volatile("" ::: "memory");
    STAMP(29);
    __builtin_amdgcn_s_setprio(P4PRIO);
    lds_wait<1>(L_CONS + 24);
    STAMP(16);
    fl = ld8c(ws, fvo, 4 * 128);
    spill(9); spill(10);
    lds_inc(S_SP);
    f4 da[4][5];
    sfor<0, 8>([&](auto ic) LAM {
      constexpr int i = decltype(ic)::v;
#pragma unroll
      for (int m = 0; m < 20; ++m) {
        const int r = m / 5, t = m % 5;
        da[r][t] = mfma16(as8(wf[i][t]), as8(gf[i][r]), i == 0 ? z4 : da[r][t]);
        if constexpr (i <= 3) { if (t == 4) ldg1(i + 4, r); }
        if constexpr (i >= 3 && i <= 6) { if (m >= 12 && m < 17) wf[i + 1][m - 12] = lld<v4u>(sdb[((i + 2) % 7) / 3] + (((i + 2) % 7) % 3) * HSLOT + (m - 12) * 1024); }
        if constexpr (i == 7) { if (m >= 6) lst<f4>(sdb[(((m - 6) / 5 + 5) % 7) / 3] + ((((m - 6) / 5 + 5) % 7) % 3) * HSLOT + ((m - 6) % 5) * 1024, da[(m - 6) / 5][(m - 6) % 5]); }
        SB();
      }
      if constexpr (i == 1) {
        if (!wall(fl.x == tag.x && fl.y == tag.y)) poll_level(ws, fvo, tag, 4);
        asm volatile("" ::: "memory");
      }
      SB();
    });
    STAMP(18);
#pragma unroll
    for (int m = 14; m < 20; ++m) lst<f4>(sdb[((m / 5 + 5) % 7) / 3] + (((m / 5 + 5) % 7) % 3) * HSLOT + (m % 5) * 1024, da[m / 5][m % 5]);
    lds_inc(S_HP);
    __builtin_amdgcn_s_setprio(0);
    lds_wait(S_HP);
    {
      const u32 pb = L_HS + ((j + 5) % 7) * HSLOT + vl;
      f4 r[5][4];
#pragma unroll
      for (int n = 0; n < 5; ++n)
#pragma unroll
        for (int k = 0; k < 4; ++k) r[n][k] = lld<f4>(pb + k * 5120 + n * 1024);
      const v4u h0 = lld<v4u>(L_HST + j * 2560 + vl), h1 = lld<v4u>(L_HST + j * 2560 + 1024 + vl);
      const v2u h2 = lld<v2u>(L_HST + j * 2560 + 2048 + lane * 8);
      const u32 hw[10] = {h0.x, h0.y, h0.z, h0.w, h1.x, h1.y, h1.z, h1.w, h2.x, h2.y};
#pragma unroll
      for (int n = 0; n < 5; ++n)
        lst<f4>(pb + n * 1024, ((r[n][0] + r[n][1]) + (r[n][2] + r[n][3])) + f4{blo(hw[2 * n]), bhi(hw[2 * n]), blo(hw[2 * n + 1]), bhi(hw[2 * n + 1])});
    }
    bar();
    DUMP();
  }
}

extern "C" void kernel_launch(void* const* d_in, const int* in_sizes, int n_in, void* d_out, int out_size, void* d_ws, size_t ws_size,
                              hipStream_t stream) {
  (void)hipFuncSetAttribute((const void*)fused_block, hipFuncAttributeMaxDynamicSharedMemorySize, L_BYTES);
  fused_block<<<256, 512, L_BYTES, stream>>>((const u16*)d_in[0], (const u16*)d_in[1], (const u16*)d_in[2], (const u16*)d_in[3],
                                             (const u16*)d_in[5], (const u16*)d_in[6], (const u16*)d_in[7], (u16*)d_out, (char*)d_ws);
}
```

```cpp
#include <hip/hip_runtime.h>
#include <hip/hip_bf16.h>

typedef unsigned short u16;
typedef unsigned int u32;
typedef unsigned long long u64;
typedef u32 v4u __attribute__((ext_vector_type(4)));
typedef u32 v2u __attribute__((ext_vector_type(2)));
typedef float f4 __attribute__((ext_vector_type(4)));
typedef float f2 __attribute__((ext_vector_type(2)));
typedef float f16v __attribute__((ext_vector_type(16)));
typedef short s4 __attribute__((ext_vector_type(4)));
typedef short s8 __attribute__((ext_vector_type(8)));
typedef __bf16 bf2 __attribute__((ext_vector_type(2)));
#define LAS __attribute__((address_space(3)))
#define CAS __attribute__((address_space(4)))
#if defined(__HIP_DEVICE_COMPILE__)
#define NOPK __attribute__((target("no-packed-fp32-ops")))
#else
#define NOPK
#endif
#define DEV __device__ __forceinline__ NOPK
#define SB() __builtin_amdgcn_sched_barrier(0)
#define OPQ(x) asm volatile("" : "+v"(x))
typedef __amdgpu_buffer_rsrc_t rsrc_t;

#define DBG 0
#define ROT ((rank + 3 * (team & 1)) % 10)
#define P1ST 0
#define HOPA0 28
#define HOPA1 56
#define HOPB 1
#define FAS 128
#define NRM1 3
#define NRM2 8
#define HOPA 2
#define NWPRE 2
#define HOPD 12
#define HOPE 10
#define FAD 10
#define FAR 4
#define PD2 12
#define KDEP 4
#define FBD 0
#define LVS 8
#define WD6 8
#define NW2 6
#define NW0 4
#define NW1 4
#define XD3 1
#define HPRIO 0
#define RDEP 4
#define CODEPF 1
#define CODE_BYTES 45056
#define HROT 0
#define OROT 0
#define P4PRIO 3
#define ABL 0
#define NW3 7
#define WLP 0
#define PACE 63
#define XT 1
#define RT 0
#define RLATE 0
#define RSP 2
#define NT 0
#define XPS 1
#define P1X 0
#define HDEP 12
#define FTOUCH 1
#define XI 2
#define XMAP 4

constexpr int SEQ = 2048, DM = 640, FF = 1728;
constexpr u32 W_EPOCH = 0;
constexpr u32 W_FA = 4096;
constexpr u32 W_FB = W_FA + 256 * 128;
constexpr u32 W_FC = W_FB + 256 * 32;
constexpr u32 W_FS2 = W_FC + 256 * 128;
constexpr u32 W_FS3 = W_FS2 + 256 * 4 * 128;
constexpr u32 W_FS4 = W_FS3 + 256 * 4 * 128;
constexpr u32 W_RED = W_FS4 + 256 * 4 * 8 * 128;
constexpr u32 W_STAT = W_RED + 65536;
constexpr u32 W_CSP = W_STAT + 131072;
constexpr u32 W_CS1 = W_CSP + 20 * 108 * 128;
constexpr u32 W_PART = W_CS1 + 20 * 7680;
constexpr u32 W_O = W_PART + 16 * 16 * 8192;
constexpr u32 W_H = W_O + 16 * 8 * 20 * 1024;
constexpr u32 W_G = W_H + 16 * 4 * 40 * 1024;
constexpr u32 W_WGU = W_G + 16 * 8 * 54 * 1024;
constexpr u32 W_WD = W_WGU + 108 * 40 * 1024;
constexpr u32 W_WO = W_WD + 8 * 54 * 5 * 1024;
constexpr u32 W_W1 = W_WO + 8 * 20 * 5 * 1024;
constexpr u32 W_DBG = W_W1 + 16 * 153600;
constexpr u32 W_END = W_DBG + 256 * 8 * 32 * 4;
constexpr u32 L_WPHI = 0;
constexpr u32 L_CS3 = 3200;
constexpr u32 L_SYNC = 3072;
constexpr u32 L_CS1 = 2048;
constexpr u32 S_PST = L_SYNC, S_FA = L_SYNC + 4, S_XD = L_SYNC + 8, S_CS = L_SYNC + 12, S_KVD = L_SYNC + 16, S_WOC = L_SYNC + 20, S_ST = L_SYNC + 24, S_FB = L_SYNC + 28, S_OP = L_SYNC + 52, S_OL = L_SYNC + 56, S_HL = L_SYNC + 60;
constexpr u32 S_L1 = L_SYNC + 32, S_SP = L_SYNC + 64, S_HP = L_SYNC + 68, S_KV = L_SYNC + 72, L_LAND = L_SYNC + 80, L_CONS = L_SYNC + 100;
constexpr u32 L_KV = 4096;
constexpr u32 L_A = 8192;
constexpr u32 L_B = L_A + 102400;
constexpr u32 L_PH2 = L_B + 51200;
constexpr u32 L_IMG = L_A, L_WO = L_A;
constexpr u32 L_HS = L_A, HSLOT = 20480;
constexpr u32 L_HST = L_HS + 7 * HSLOT;
constexpr u32 L_BYTES = 163840;
constexpr u32 OOB = 0x80000000u;
constexpr u32 SENT = 0xFFFFFFFFu;
constexpr u32 SPIN_CAP = 1u << 18;

DEV v4u mk_srd(const void* p, u32 bytes) { return v4u{(u32)(size_t)p, (u32)((size_t)p >> 32), bytes, 0x00020000u}; }
DEV rsrc_t mk_rsrc(const void* p, u32 bytes) { return __builtin_amdgcn_make_buffer_rsrc((void*)p, 0, bytes, 0x00020000); }
DEV void dma16(u32 voff, v4u srd, u32 soff, u32 lds) {
  asm volatile("s_mov_b32 m0, %2\n\ts_nop 0\n\tbuffer_load_dwordx4 %0, %1, %3 offen lds" ::"v"(voff), "s"(srd), "s"(lds), "s"(soff) : "memory");
}
DEV void dma16c(u32 voff, v4u srd, u32 soff, u32 lds) {
  asm volatile("s_mov_b32 m0, %2\n\ts_nop 0\n\tbuffer_load_dwordx4 %0, %1, %3 offen sc1 lds" ::"v"(voff), "s"(srd), "s"(lds), "s"(soff) : "memory");
}
DEV void dma16n(u32 voff, v4u srd, u32 soff, u32 lds) {
  asm volatile("s_mov_b32 m0, %2\n\ts_nop 0\n\tbuffer_load_dwordx4 %0, %1, %3 offen nt lds" ::"v"(voff), "s"(srd), "s"(lds), "s"(soff) : "memory");
}
DEV void dma_a(u32 voff, v4u srd, u32 sb, u32 sa, u32 lb, u32 la) {
  u32 t;
  asm volatile("s_add_u32 m0, %5, %6\n\ts_add_u32 %0, %3, %4\n\tbuffer_load_dwordx4 %1, %2, %0 offen lds"
               : "=&s"(t) : "v"(voff), "s"(srd), "s"(sb), "s"(sa), "s"(lb), "s"(la) : "memory", "scc");
}
DEV void dma_ac(u32 voff, v4u srd, u32 sb, u32 sa, u32 lb, u32 la) {
  u32 t;
  asm volatile("s_add_u32 m0, %5, %6\n\ts_add_u32 %0, %3, %4\n\tbuffer_load_dwordx4 %1, %2, %0 offen sc1 lds"
               : "=&s"(t) : "v"(voff), "s"(srd), "s"(sb), "s"(sa), "s"(lb), "s"(la) : "memory", "scc");
}
DEV void dma_i(u32 voff, v4u srd, u32 sb, u32 sa, u32 lb, u32 la) {
  u32 t;
  asm volatile("s_add_u32 m0, %5, %6\n\ts_add_u32 %0, %3, %4\n\tbuffer_load_dwordx4 %1, %2, %0 offen lds"
               : "=&s"(t) : "v"(voff), "s"(srd), "s"(sb), "i"(sa), "s"(lb), "i"(la) : "memory", "scc");
}
DEV void touch(u32 voff, v4u srd, u32 soff, u32 lds) {
  asm volatile("s_mov_b32 m0, %2\n\ts_nop 0\n\tbuffer_load_dword %0, %1, %3 offen lds" ::"v"(voff), "s"(srd), "s"(lds), "s"(soff) : "memory");
}
DEV void touchc(u32 voff, v4u srd, u32 soff, u32 lds) {
  asm volatile("s_mov_b32 m0, %2\n\ts_nop 0\n\tbuffer_load_dword %0, %1, %3 offen sc1 lds" ::"v"(voff), "s"(srd), "s"(lds), "s"(soff) : "memory");
}
DEV void dma5c(u32 voff, v4u srd, u32 so, u32 lds) {
  u32 t;
  asm volatile("s_mov_b32 m0, %4\n\ts_add_u32 %0, %3, 0x1000\n\t"
               "buffer_load_dwordx4 %1, %2, %3 offen sc1 lds\n\t"
               "buffer_load_dwordx4 %1, %2, %3 offen offset:1024 sc1 lds\n\t"
               "buffer_load_dwordx4 %1, %2, %3 offen offset:2048 sc1 lds\n\t"
               "buffer_load_dwordx4 %1, %2, %3 offen offset:3072 sc1 lds\n\t"
               "s_add_u32 m0, %4, 0x1000\n\ts_nop 0\n\t"
               "buffer_load_dwordx4 %1, %2, %0 offen sc1 lds"
               : "=&s"(t) : "v"(voff), "s"(srd), "s"(so), "s"(lds) : "memory", "scc");
}
DEV u32 sgpr(u32 x) {
  u32 o;
  asm volatile("s_mov_b32 %0, %1" : "=s"(o) : "s"(__builtin_amdgcn_readfirstlane(x)));
  return o;
}
#define OPS(x) x = sgpr(x)
template <int N> DEV void waitv() { asm volatile("s_waitcnt vmcnt(%0)" ::"n"(N) : "memory"); }
#define WAITV(n) asm volatile("s_waitcnt vmcnt(" #n ")" ::: "memory")
DEV void bar() {
  asm volatile("" ::: "memory");
  __builtin_amdgcn_s_waitcnt(0xC07F);
  __builtin_amdgcn_s_barrier();
  asm volatile("" ::: "memory");
  SB();
}
DEV void bar_nw() {
  asm volatile("" ::: "memory");
  __builtin_amdgcn_s_barrier();
  asm volatile("" ::: "memory");
  SB();
}
DEV s4 trd(u32 a) { return __builtin_amdgcn_ds_read_tr16_b64_v4i16((LAS s4*)a); }
DEV s8 cat(s4 a, s4 b) { return __builtin_shufflevector(a, b, 0, 1, 2, 3, 4, 5, 6, 7); }
DEV s8 as8(v4u a) { return __builtin_bit_cast(s8, a); }
DEV s4 as4(v2u a) { return __builtin_bit_cast(s4, a); }
DEV f4 mfma16(s8 a, s8 b, f4 c) { return __builtin_amdgcn_mfma_f32_16x16x32_bf16(a, b, c, 0, 0, 0); }
DEV f4 mfma16h(s4 a, s4 b, f4 c) { return __builtin_amdgcn_mfma_f32_16x16x16bf16_1k(a, b, c, 0, 0, 0); }
DEV f16v mfma32(s8 a, s8 b, f16v c) { return __builtin_amdgcn_mfma_f32_32x32x16_bf16(a, b, c, 0, 0, 0); }
DEV u32 pk(float a, float b) { return __builtin_bit_cast(u32, __builtin_convertvector(f2{a, b}, bf2)); }
DEV float blo(u32 x) { return __builtin_bit_cast(float, x << 16); }
DEV float bhi(u32 x) { return __builtin_bit_cast(float, x & 0xFFFF0000u); }
DEV float dot2(u32 a, u32 b, float c) { return __builtin_amdgcn_fdot2_f32_bf16(__builtin_bit_cast(bf2, a), __builtin_bit_cast(bf2, b), c, false); }
DEV u32 fu(float x) { return __builtin_bit_cast(u32, x); }
DEV float uf(u32 x) { return __builtin_bit_cast(float, x); }
template <class T> DEV T lld(u32 a) { return *(LAS T*)a; }
template <class T> DEV void lst(u32 a, T v) { *(LAS T*)a = v; }
DEV v4u ld16c(rsrc_t r, u32 vo, u32 so) { return __builtin_amdgcn_raw_buffer_load_b128(r, vo, so, 16); }
DEV v2u ld8c(rsrc_t r, u32 vo, u32 so) { return __builtin_amdgcn_raw_buffer_load_b64(r, vo, so, 16); }
DEV u32 ld4c(rsrc_t r, u32 vo, u32 so) { return __builtin_amdgcn_raw_buffer_load_b32(r, vo, so, 16); }
DEV void st16c(v4u v, rsrc_t r, u32 vo, u32 so) {
  __builtin_amdgcn_raw_buffer_store_b128(v, r, vo, so, 16);
  asm volatile("s_nop 1" ::"v"(v) : "memory");
}
DEV void st16p(v4u v, rsrc_t r, u32 vo, u32 so) {
  __builtin_amdgcn_raw_buffer_store_b128(v, r, vo, so, 0);
  asm volatile("s_nop 1" ::"v"(v) : "memory");
}
DEV void st8c(v2u v, rsrc_t r, u32 vo, u32 so) { __builtin_amdgcn_raw_buffer_store_b64(v, r, vo, so, 16); }
DEV void st4c(u32 v, rsrc_t r, u32 vo, u32 so) { __builtin_amdgcn_raw_buffer_store_b32(v, r, vo, so, 16); }
DEV bool wall(bool c) { return __builtin_amdgcn_ballot_w64(!c) == 0ull; }
DEV u32 san(u32 x) { return x < 0xFFFFFFFEu ? x : 0xFFFFFFFEu; }
DEV u32 swap23(u32 k) { return (k & ~12u) | ((k & 4u) << 1) | ((k & 8u) >> 1); }

template <bool MAXOP, bool RCP> DEV void red4(float& a, float& b, float& c, float& d) {
  auto r1 = __builtin_amdgcn_permlane16_swap(fu(a), fu(b), false, false);
  auto r2 = __builtin_amdgcn_permlane16_swap(fu(c), fu(d), false, false);
  float ab = MAXOP ? __builtin_elementwise_maximum(uf(r1[0]), uf(r1[1])) : uf(r1[0]) + uf(r1[1]);
  float cd = MAXOP ? __builtin_elementwise_maximum(uf(r2[0]), uf(r2[1])) : uf(r2[0]) + uf(r2[1]);
  auto r3 = __builtin_amdgcn_permlane32_swap(fu(ab), fu(cd), false, false);
  float z = MAXOP ? __builtin_elementwise_maximum(uf(r3[0]), uf(r3[1])) : uf(r3[0]) + uf(r3[1]);
  if (RCP) z = __builtin_amdgcn_rcpf(z);
  auto g1 = __builtin_amdgcn_permlane32_swap(fu(z), fu(z), false, false);
  auto g2 = __builtin_amdgcn_permlane16_swap(g1[0], g1[0], false, false);
  auto g3 = __builtin_amdgcn_permlane16_swap(g1[1], g1[1], false, false);
  a = uf(g2[0]); b = uf(g2[1]); c = uf(g3[0]); d = uf(g3[1]);
}
DEV void red2(float& a, float& b) {
  auto r1 = __builtin_amdgcn_permlane16_swap(fu(a), fu(b), false, false);
  float ab = uf(r1[0]) + uf(r1[1]);
  auto r3 = __builtin_amdgcn_permlane32_swap(fu(ab), fu(ab), false, false);
  float z = uf(r3[0]) + uf(r3[1]);
  auto g2 = __builtin_amdgcn_permlane16_swap(fu(z), fu(z), false, false);
  a = uf(g2[0]); b = uf(g2[1]);
}

#define LAM __attribute__((always_inline)) NOPK
template <int V> struct IC { static constexpr int v = V; };
template <int A, int B, class F> DEV void sfor(F&& f) {
  if constexpr (A < B) { f(IC<A>{}); sfor<A + 1, B>(f); }
}
struct Phi { v2u pl[2], mi[2], ex; };
DEV float fmx(float a, float b) { return __builtin_elementwise_maximum(a, b); }
DEV float fmn(float a, float b) { return __builtin_elementwise_minimum(a, b); }
DEV void phi_mm(s8 b0, s8 b1, const s8 (&A)[4], f4 (&p)[3]) {
  const f4 z = {0.f, 0.f, 0.f, 0.f};
  p[2] = mfma16(A[2], b0, z);
  p[0] = mfma16(A[0], b0, z);
  p[1] = mfma16(A[1], b1, z);
  p[2] = mfma16(A[3], b1, p[2]);
}
DEV Phi phi_sm(const f4 (&pp)[3]) {
  const float p[2][5] = {{pp[0][0], pp[0][1], pp[0][2], pp[0][3], pp[2][0]}, {pp[1][0], pp[1][1], pp[1][2], pp[1][3], pp[2][1]}};
  float mx[2], nm[2];
#pragma unroll
  for (int h = 0; h < 2; ++h) {
    mx[h] = fmx(fmx(fmx(fmx(p[h][0], p[h][1]), p[h][2]), p[h][3]), p[h][4]);
    nm[h] = fmx(fmx(fmx(fmx(-p[h][0], -p[h][1]), -p[h][2]), -p[h][3]), -p[h][4]);
  }
  red4<true, false>(mx[0], nm[0], mx[1], nm[1]);
  float ep[2][5], em[2][5], sp[2], sm[2];
#pragma unroll
  for (int h = 0; h < 2; ++h) {
#pragma unroll
    for (int j = 0; j < 5; ++j) {
      ep[h][j] = __builtin_amdgcn_exp2f(p[h][j] - mx[h]);
      em[h][j] = __builtin_amdgcn_exp2f(-p[h][j] - nm[h]);
    }
    sp[h] = ((ep[h][0] + ep[h][1]) + (ep[h][2] + ep[h][3])) + ep[h][4];
    sm[h] = ((em[h][0] + em[h][1]) + (em[h][2] + em[h][3])) + em[h][4];
  }
  red4<false, true>(sp[0], sm[0], sp[1], sm[1]);
  Phi o;
#pragma unroll
  for (int h = 0; h < 2; ++h) {
    o.pl[h] = v2u{pk(ep[h][0] * sp[h], ep[h][1] * sp[h]), pk(ep[h][2] * sp[h], ep[h][3] * sp[h])};
    o.mi[h] = v2u{pk(em[h][0] * sm[h], em[h][1] * sm[h]), pk(em[h][2] * sm[h], em[h][3] * sm[h])};
    o.ex[h] = pk(ep[h][4] * sp[h], em[h][4] * sm[h]);
  }
  return o;
}
template <class HK> DEV Phi phi_smh(const f4 (&pp)[3], HK&& hk) {
  const float p[2][5] = {{pp[0][0], pp[0][1], pp[0][2], pp[0][3], pp[2][0]}, {pp[1][0], pp[1][1], pp[1][2], pp[1][3], pp[2][1]}};
  float m[4], e[4][5], sm[4];
#pragma unroll
  for (int h = 0; h < 2; ++h) {
    m[2 * h] = fmx(fmx(fmx(fmx(p[h][0], p[h][1]), p[h][2]), p[h][3]), p[h][4]);
    m[2 * h + 1] = fmx(fmx(fmx(fmx(-p[h][0], -p[h][1]), -p[h][2]), -p[h][3]), -p[h][4]);
  }
  hk(IC<0>{});
  red4<true, false>(m[0], m[1], m[2], m[3]);
  hk(IC<1>{}); hk(IC<2>{});
  sfor<0, 4>([&](auto kc) LAM {
    constexpr int k = decltype(kc)::v;
#pragma unroll
    for (int j = 0; j < 5; ++j) e[k][j] = __builtin_amdgcn_exp2f(((k & 1) ? -p[k >> 1][j] : p[k >> 1][j]) - m[k]);
    if constexpr (k > 0) sm[k - 1] = ((e[k - 1][0] + e[k - 1][1]) + (e[k - 1][2] + e[k - 1][3])) + e[k - 1][4];
    hk(IC<3 + k>{});
    if constexpr (k & 1) hk(IC<7 + (k >> 1)>{});
  });
  sm[3] = ((e[3][0] + e[3][1]) + (e[3][2] + e[3][3])) + e[3][4];
  red4<false, true>(sm[0], sm[1], sm[2], sm[3]);
  hk(IC<9>{}); hk(IC<10>{});
  Phi o;
  o.pl[0] = v2u{pk(e[0][0] * sm[0], e[0][1] * sm[0]), pk(e[0][2] * sm[0], e[0][3] * sm[0])};
  o.ex[0] = pk(e[0][4] * sm[0], e[1][4] * sm[1]);
  hk(IC<11>{});
  o.mi[0] = v2u{pk(e[1][0] * sm[1], e[1][1] * sm[1]), pk(e[1][2] * sm[1], e[1][3] * sm[1])};
  o.ex[1] = pk(e[2][4] * sm[2], e[3][4] * sm[3]);
  hk(IC<12>{});
  o.pl[1] = v2u{pk(e[2][0] * sm[2], e[2][1] * sm[2]), pk(e[2][2] * sm[2], e[2][3] * sm[2])};
  o.mi[1] = v2u{pk(e[3][0] * sm[3], e[3][1] * sm[3]), pk(e[3][2] * sm[3], e[3][3] * sm[3])};
  hk(IC<13>{});
  return o;
}
DEV v2u scpk(f4 a, float sc) { return v2u{pk(a[0] * sc, a[1] * sc), pk(a[2] * sc, a[3] * sc)}; }
DEV v2u lnpk(f4 a, f4 cs, float nmean, float sc) {
  return v2u{pk(__builtin_fmaf(nmean, cs[0], a[0]) * sc, __builtin_fmaf(nmean, cs[1], a[1]) * sc),
             pk(__builtin_fmaf(nmean, cs[2], a[2]) * sc, __builtin_fmaf(nmean, cs[3], a[3]) * sc)};
}
DEV f4 lcs(u32 a) { return lld<f4>(a); }
DEV u32 bsel(u32 m, u32 a, u32 b) { return (a & m) | (b & ~m); }
DEV void poll_flags(rsrc_t ws, u32 voff, bool active, v2u tag) {
#pragma unroll 1
  for (u32 s = 0; s < SPIN_CAP; ++s) {
    v2u v = tag;
    if (active) v = ld8c(ws, voff, 0);
    if (wall(v.x == tag.x && v.y == tag.y)) break;
    asm volatile("" ::: "memory");
  }
  asm volatile("" ::: "memory");
}
template <int SL = 1> DEV void lds_check(u32 lv, u32 a, u32 n) {
  u32 sv, cnt;
  asm volatile(
      "s_nop 0\n\tv_readfirstlane_b32 %0, %2\n\ts_cmp_ge_u32 %0, %4\n\ts_cbranch_scc1 2f\n\ts_mov_b32 %1, 0x400000\n"
      "1:\n\tv_mov_b32 %2, %3\n\tds_read_b32 %2, %2\n\ts_waitcnt lgkmcnt(0)\n\tv_readfirstlane_b32 %0, %2\n\ts_cmp_ge_u32 %0, %4\n\ts_cbranch_scc1 2f\n\t"
      "s_sleep %5\n\ts_sub_u32 %1, %1, 1\n\ts_cbranch_scc0 1b\n2:"
      : "=&s"(sv), "=&s"(cnt), "+v"(lv) : "s"(a), "s"(n), "n"(SL) : "memory", "scc");
}
template <int SL = 1> DEV void lds_wait(u32 a, u32 n = 4) { lds_check<SL>(0u, a, n); }
DEV void lds_inc(u32 a) {
  u32 one = 1;
  asm volatile("" : "+s"(a), "+s"(one));
  asm volatile("s_mov_b64 exec, 1\n\tds_add_u32 %0, %1\n\ts_mov_b64 exec, -1" ::"v"(a), "v"(one) : "memory");
}
DEV void flag_store(v4u srd, v2u tag, u32 so) {
  asm volatile("" : "+s"(tag));
  asm volatile("s_mov_b64 exec, 1\n\tbuffer_store_dwordx2 %0, off, %1, %2 sc1\n\ts_mov_b64 exec, -1" ::"v"(tag), "s"(srd), "s"(so) : "memory");
}
template <int SL = 0> DEV void poll_mask(rsrc_t ws, u32 fv, u32 base, v2u tag, v2u v) {
#pragma unroll 1
  for (u32 s = 0; s < SPIN_CAP; ++s) {
    fv = (v.x == tag.x && v.y == tag.y) ? OOB : fv;
    if (wall(fv == OOB)) break;
    __builtin_amdgcn_s_sleep(SL);
    asm volatile("" ::: "memory");
    v = ld8c(ws, fv, base);
  }
  asm volatile("" ::: "memory");
}
DEV void poll256(rsrc_t ws, u32 base, u32 stride, u32 lane, v2u tag) {
#pragma unroll 1
  for (u32 s = 0; s < SPIN_CAP; ++s) {
    v2u f[4];
    bool ok = true;
#pragma unroll
    for (int n = 0; n < 4; ++n) f[n] = ld8c(ws, lane * stride, base + n * 64 * stride);
#pragma unroll
    for (int n = 0; n < 4; ++n) ok = ok && f[n].x == tag.x && f[n].y == tag.y;
    if (wall(ok)) break;
    __builtin_amdgcn_s_sleep(8);
    asm volatile("" ::: "memory");
  }
  asm volatile("" ::: "memory");
}
DEV u32 lane_id() {
  u32 m = ~0u;
  asm volatile("" : "+s"(m));
  const u32 l = __builtin_amdgcn_mbcnt_hi(m, __builtin_amdgcn_mbcnt_lo(m, 0u));
  return l;
}
DEV u32 umax(u32 a, u32 b) { return a > b ? a : b; }
DEV u32 umax4(u32 m, v4u v) { return umax(umax(umax(umax(m, v.x), v.y), v.z), v.w); }

#define STAMP(i) do {} while (0)

DEV void poll_level(rsrc_t ws, u32 voff, v2u tag, u32 level) {
#pragma unroll 1
  for (u32 s = 0; s < SPIN_CAP; ++s) {
    const v2u v = ld8c(ws, voff, level * 128);
    voff = (v.x == tag.x && v.y == tag.y) ? OOB : voff;
    if (wall(voff == OOB)) break;
    __builtin_amdgcn_s_sleep(LVS);
    asm volatile("" ::: "memory");
  }
  asm volatile("" ::: "memory");
}

template <bool TWO> DEV void kv_job(rsrc_t ws, u32 jb, u32 so) {
  const u32 lane = lane_id();
  const u32 q = lane >> 4, tq = (lane >> 2) & 3, tp = lane & 3;
  const u32 a1c = !TWO ? jb * 32 : jb < 6 ? (jb - 4) * 32 : 128;
  const u32 b1c = !TWO ? 160 + (jb >> 1) * 32 : jb == 6 ? 160 : 224;
  const u32 a2c = jb < 6 ? a1c + 64 : a1c;
  const u32 b2c = jb == 6 ? 192 : b1c;
  const u32 rb = L_IMG + (2 * (q >> 1) + 16 * (q & 1) + 4 * tq) * 264 + 8 * tp;
  constexpr int NO = TWO ? 4 : 2;
  u32 ra[4] = {rb + a1c, rb + b1c, rb + a2c, rb + b2c};
  u32 mlo = (jb != 6 && (lane & 1)) ? ~0u : 0u, mhi = (jb == 6 || (lane & 1)) ? ~0u : 0u, vl = lane * 16;
#pragma unroll
  for (int n = 0; n < NO; ++n) OPQ(ra[n]);
  OPQ(vl);
  if (TWO) { OPQ(mlo); OPQ(mhi); }
  bar();
  s8 fr[4][NO];
#pragma unroll
  for (int ks = 0; ks < 4; ++ks)
#pragma unroll
    for (int n = 0; n < NO; ++n) fr[ks][n] = cat(trd(ra[n] + ks * 32 * 264), trd(ra[n] + ks * 32 * 264 + 264));
  SB();
  f4 d1 = {0.f, 0.f, 0.f, 0.f}, d2 = d1;
#pragma unroll
  for (int ks = 0; ks < 4; ++ks) {
    d1 = mfma16(fr[ks][0], fr[ks][1], d1);
    if (TWO) d2 = mfma16(fr[ks][NO - 2], fr[ks][NO - 1], d2);
  }
  if (TWO) d1 = f4{uf(bsel(mlo, fu(d2[0]), fu(d1[0]))), uf(bsel(mlo, fu(d2[1]), fu(d1[1]))), uf(bsel(mhi, fu(d2[2]), fu(d1[2]))), uf(bsel(mhi, fu(d2[3]), fu(d1[3])))};
  st16c(v4u{san(fu(d1[0])), san(fu(d1[1])), san(fu(d1[2])), san(fu(d1[3]))}, ws, vl, so + jb * 1024);
}

typedef u64 v8q __attribute__((ext_vector_type(8)));
__global__ __launch_bounds__(512, 2) NOPK void fused_block(const u16* x_, const u16* wqkv_, const u16* wphi_, const u16* wo_, const u16* wg_, const u16* wu_,
                                                           const u16* wd_, u16* out_, char* wsp_) {
  v8q ka;
  u64 kw;
  asm volatile("s_load_dwordx16 %0, %2, 0x0\n\ts_load_dwordx2 %1, %2, 0x40" : "=&s"(ka), "=&s"(kw) : "s"(__builtin_amdgcn_kernarg_segment_ptr()));
  u64 pc;
  asm volatile("s_getpc_b64 %0" : "=s"(pc));
  SB();
  STAMP(0);
  const u32 wv = __builtin_amdgcn_readfirstlane(threadIdx.x >> 6);
  const u32 b = blockIdx.x, xcd = b & 7, idx = b >> 3;
  const u32 team = (xcd >> 1) * 4 + (idx >> 3), rank = ((idx & 4) << 1) | ((xcd & 1) << 2) | (idx & 3);
  const u32 bid = team * 16 + rank, hq = rank & 7, th = rank >> 3;
  const bool u7 = rank < 12;
  const f4 z4 = {0.f, 0.f, 0.f, 0.f};
#define DUMP()
#define GET_ARGS()                                                                                         \
  SB();                                                                                                    \
  asm volatile("s_waitcnt lgkmcnt(0)" ::: "memory");                                                       \
  SB();                                                                                                    \
  asm volatile("" : "+s"(ka), "+s"(kw));                                                                   \
  char* const wsp = (char*)kw;                                                                             \
  const rsrc_t ws = mk_rsrc(wsp, W_END);                                                                   \
  const v4u sws = mk_srd(wsp, W_END);
#define GET_EPOCH()                                                                                        \
  asm volatile("s_waitcnt lgkmcnt(0)" ::: "memory");                                                       \
  SB();                                                                                                    \
  asm volatile("" : "+s"(ep));                                                                             \
  ep = (ep & ~15ull) + 16ull;                                                                              \
  const v2u tag = {(u32)ep, (u32)(ep >> 32)};

  if (wv >= 4) {
    const u32 w = wv - 4;
    const u32 lane = lane_id();
    const u32 q = lane >> 4, i16 = lane & 15;
    {
      const v4u sc = mk_srd((const void*)(pc & ~255ull), CODE_BYTES);
      if (w == 0) asm volatile("s_mov_b32 m0, %2\n\ts_nop 0\n\tbuffer_load_dword %0, %1, 0 offen lds" ::"v"(lane < 16 ? (lane * 32 + idx) * 128 : OOB), "s"(sc), "s"(L_CS3) : "memory");
    }
    const u32 vx0 = (lane >> 3) * 1280 + (((lane & 7) ^ (lane >> 4)) << 4), vx1 = (vx0 ^ 64) + 8 * 1280;
    u32 xs0 = (team * 128 + w * 32) * 1280, xl0 = L_B + w * 12288;
    OPS(xs0); OPS(xl0);
    GET_ARGS();
    const u16* const x = (const u16*)ka[0];
    u16* const out = (u16*)ka[7];
    const v4u sx = mk_srd(x, SEQ * DM * 2);
    bar_nw();
    u64 ep;
    auto stagex = [&](int c) LAM {
      dma_i(vx0, sx, xs0, c * 128, xl0, (c % 3) * 4096);
      dma_i(vx1, sx, xs0, c * 128, xl0, (c % 3) * 4096 + 1024);
      dma_i(vx0, sx, xs0, c * 128 + 16 * 1280, xl0, (c % 3) * 4096 + 2048);
      dma_i(vx1, sx, xs0, c * 128 + 16 * 1280, xl0, (c % 3) * 4096 + 3072);
    };
    {
      const u32 fw = bid * 4 + w;
      u32 o = W_FS4 + fw * 1024 + lane * 128;
      o = bsel(lane == 8 ? ~0u : 0u, W_FS2 + fw * 128, o);
      o = bsel(lane == 9 ? ~0u : 0u, W_FS3 + fw * 128, o);
      o = bsel(lane == 10 ? ~0u : 0u, W_FB + bid * 32, o);
      o = bsel(lane == 11 ? ~0u : 0u, W_FA + b * FAS, o);
      o |= lane > 11 ? OOB : 0u;
      touchc(o, sws, 0, L_CS3);
    }
    touch(lane < 20 ? lane * 128 : OOB, sx, team * (128 * 1280) + (rank * 4 + w) * 2560, L_CS3);
    if (XI == 0) lds_wait(S_ST);
    stagex(0);
    if (XI == 1) lds_wait(S_ST);
    stagex(1);
    if (XI == 2) lds_wait(S_ST);
    stagex(2);
    v4u xa[2][20];
    float nm1[2], rs1[2];
    {
      v4u onesv = {0x3F803F80u, 0x3F803F80u, 0x3F803F80u, 0x3F803F80u};
      OPQ(onesv);
      const s8 ones = as8(onesv);
      f4 as[2] = {z4, z4}, ag[2] = {z4, z4};
      u32 ax0 = L_B + w * 12288 + i16 * 128 + ((q ^ ((i16 >> 1) & 7)) << 4), ax1 = ax0 ^ 64;
      OPQ(ax0); OPQ(ax1);
      sfor<0, 11>([&](auto cc_) LAM {
        constexpr int c = decltype(cc_)::v;
        if constexpr (c < 10) waitv<(c < 8 ? 8 : c == 8 ? 4 : 0)>();
        __builtin_amdgcn_s_waitcnt(0xC07F);
        SB();
        if constexpr (c < 10) {
#pragma unroll
          for (int t = 0; t < 2; ++t) { xa[t][2 * c] = lld<v4u>(ax0 + (c % 3) * 4096 + t * 2048); xa[t][2 * c + 1] = lld<v4u>(ax1 + (c % 3) * 4096 + t * 2048); }
        }
        if constexpr (c == 0 && XPS) lds_wait(XPS == 1 ? S_ST : S_PST);
        if constexpr (c < 7) { __builtin_amdgcn_s_waitcnt(0xC07F); SB(); stagex(c + 3); }
        if constexpr (c == 10) lds_inc(S_XD);
        if constexpr (c >= 1) {
#pragma unroll
          for (int k = 2 * c - 2; k < 2 * c; ++k)
#pragma unroll
            for (int t = 0; t < 2; ++t) { as[t] = mfma16(ones, as8(xa[t][k]), as[t]); ag[t] = mfma16(as8(xa[t][k]), as8(xa[t][k]), ag[t]); }
        }
        SB();
      });
      asm volatile("s_load_dwordx2 %0, %1, 0x0" : "=s"(ep) : "s"(wsp));
      STAMP(23);
      float g[2];
#pragma unroll
      for (int t = 0; t < 2; ++t) {
        u32 v = 0;
#pragma unroll
        for (int r = 0; r < 4; ++r) v |= fu(ag[t][r]) & (i16 == 4 * q + r ? ~0u : 0u);
        g[t] = uf(v);
      }
      red2(g[0], g[1]);
#pragma unroll
      for (int t = 0; t < 2; ++t) {
        nm1[t] = -as[t][0] * (1.0f / DM);
        rs1[t] = __builtin_amdgcn_rsqf(__builtin_fmaxf(__builtin_fmaf(-nm1[t], nm1[t], g[t] * (1.0f / DM)), 0.f) + 1e-5f);
        OPQ(nm1[t]); OPQ(rs1[t]);
      }
    }
    GET_EPOCH();
    const float L2E = 1.4426950408889634f;
    {
      f4 acc[2][5] = {{z4, z4, z4, z4, z4}, {z4, z4, z4, z4, z4}};
      u32 bw[2] = {L_A + lane * 16, L_A + 61440 + lane * 16};
      OPQ(bw[0]); OPQ(bw[1]);
      constexpr int KD = KDEP;
      s8 kf[KD + 1];
      u32 lv;
      lds_wait(S_FA);
      if (w == 0) flag_store(sws, tag, W_FC + bid * 128);
      lds_wait<0>(S_L1);
      __builtin_amdgcn_s_setprio(2);
      STAMP(25);
      auto ldk = [&](int g) LAM { kf[g % (KD + 1)] = lld<s8>(bw[g / 60] + (g % 60) * 1024); };
      auto mm = [&](auto gc) LAM {
        constexpr int g = decltype(gc)::v, gn = g + KD, ks = g < 60 ? g / 3 : (g - 60) / 2, n = g < 60 ? g % 3 : 3 + (g & 1);
        if constexpr (gn == 20 || gn == 40 || gn == 60 || gn == 80) lds_check(lv, S_L1 + 4 * (gn / 20), 4);
        if constexpr (gn < 100) ldk(gn);
        if constexpr (gn == 44) lds_inc(S_CS);
        if constexpr (gn == 14 || gn == 34 || gn == 54 || gn == 76) lv = *(volatile LAS u32*)(S_L1 + 4 * (gn / 20 + 1));
        if constexpr (gn == 61) lv = *(volatile LAS u32*)S_CS;
        if constexpr (gn == 62) lds_inc(S_KVD);
        acc[0][n] = mfma16(kf[g % (KD + 1)], as8(xa[0][ks]), acc[0][n]);
        acc[1][n] = mfma16(kf[g % (KD + 1)], as8(xa[1][ks]), acc[1][n]);
      };
#pragma unroll
      for (int f = 0; f < KD; ++f) ldk(f);
      SB();
      sfor<0, 60>([&](auto gc) LAM { mm(gc); SB(); });
      STAMP(2);
      auto vv = [&](auto gc) LAM { SB(); mm(gc); SB(); };
      const u32 mq = q < 2 ? ~0u : 0u;
      v2u kp[2][3];
      lds_check(lv, S_CS, 8);
      sfor<0, 3>([&](auto nc) LAM {
        constexpr int n = decltype(nc)::v;
        const f4 c = lcs(n < 2 ? L_CS1 + 160 + n * 80 + q * 16 : L_CS1 + 224 + q * 80);
        vv(IC<60 + 2 * n>{});
#pragma unroll
        for (int t = 0; t < 2; ++t) {
          const float sk = rs1[t] * L2E;
          kp[t][n] = lnpk(acc[t][n], c, nm1[t], n < 2 ? sk : uf(bsel(mq, fu(sk), fu(rs1[t]))));
          OPQ(kp[t][n]);
          if (t == 0) vv(IC<61 + 2 * n>{});
        }
      });
      sfor<0, 2>([&](auto tc) LAM {
        constexpr int t = decltype(tc)::v, g0 = 63 + 17 * t;
        const s8 A[4] = {lld<s8>(L_WPHI + lane * 16), lld<s8>(L_WPHI + 1024 + lane * 16), lld<s8>(L_PH2 + lane * 16), lld<s8>(L_PH2 + 1024 + lane * 16)};
        const u32 e0 = bsel(mq, 0x3F803F80u, kp[t][2].x), e1 = bsel(mq, 0x3F803F80u, kp[t][2].y), eb = L_IMG + (w * 32 + t * 16 + i16) * 264 + 224 + (q ^ 2) * 2;
        vv(IC<g0 + 3>{});
        lst<u16>(eb, (u16)e0); lst<u16>(eb + 8, (u16)(e0 >> 16)); lst<u16>(eb + 16, (u16)e1); lst<u16>(eb + 24, (u16)(e1 >> 16));
        vv(IC<g0 + 4>{});
        f4 pp[3];
        phi_mm(as8(v4u{kp[t][0].x, kp[t][0].y, kp[t][2].x, kp[t][2].y}), as8(v4u{kp[t][1].x, kp[t][1].y, kp[t][2].x, kp[t][2].y}), A, pp);
        vv(IC<g0 + 5>{});
        const Phi ph = phi_smh(pp, [&](auto kc) LAM {
          constexpr int k = decltype(kc)::v, o = k < 5 ? k : k == 7 ? 5 : k < 7 ? k + 1 : k;
          vv(IC<g0 + 6 + o>{});
        });
        const u32 ib = L_IMG + (w * 32 + t * 16 + i16) * 264 + q * 8;
        lst<v2u>(ib + 0, ph.pl[0]); lst<v2u>(ib + 32, ph.mi[0]); lst<v2u>(ib + 64, ph.pl[1]); lst<v2u>(ib + 96, ph.mi[1]);
        lst<v2u>(ib + 128, ph.ex);
      });
      const f4 c3 = lcs(L_CS1 + 320 + q * 16), c4 = lcs(L_CS1 + 400 + q * 16);
#pragma unroll
      for (int t = 0; t < 2; ++t) {
        const u32 ib = L_IMG + (w * 32 + t * 16 + i16) * 264 + q * 8;
        lst<v2u>(ib + 160, lnpk(acc[t][3], c3, nm1[t], rs1[t]));
        lst<v2u>(ib + 192, lnpk(acc[t][4], c4, nm1[t], rs1[t]));
      }
    }
    STAMP(3);
    kv_job<false>(ws, w, W_PART + (rank * 16 + team) * 8192);
    STAMP(4);
    bar_nw();
    {
      const u32 lane = lane_id(), q = lane >> 4, i16 = lane & 15;
      f4 qa[2][3] = {{z4, z4, z4}, {z4, z4, z4}};
      u32 bq = L_B + lane * 16, bq2 = L_B + 4096 + ((lane & 7) + 16 * q) * 16;
      OPQ(bq); OPQ(bq2);
      constexpr int QD = 4;
      s8 qf[QD + 1];
      auto ldq = [&](int f) LAM {
        const int t = f % 3, ks = f / 3;
        qf[f % (QD + 1)] = lld<s8>(t == 2 ? bq2 + (ks >> 1) * 5120 + (ks & 1) * 128 : bq + (ks >> 1) * 5120 + ((ks & 1) * 2 + t) * 1024);
      };
#pragma unroll
      for (int f = 0; f < QD; ++f) ldq(f);
      SB();
#pragma unroll
      for (int f = 0; f < 60; ++f) {
        if (f + QD < 60) ldq(f + QD);
        qa[0][f % 3] = mfma16(qf[f % (QD + 1)], as8(xa[0][f / 3]), qa[0][f % 3]);
        qa[1][f % 3] = mfma16(qf[f % (QD + 1)], as8(xa[1][f / 3]), qa[1][f % 3]);
        SB();
      }
      STAMP(5);
      const f4 c0 = lcs(L_CS1 + q * 16), c1 = lcs(L_CS1 + 80 + q * 16), c2 = lcs(L_CS1 + 64 + (q & 1) * 80);
      const s8 A[4] = {lld<s8>(L_WPHI + lane * 16), lld<s8>(L_WPHI + 1024 + lane * 16), lld<s8>(L_PH2 + lane * 16), lld<s8>(L_PH2 + 1024 + lane * 16)};
      f4 pp[2][3];
      Phi pq[2];
#pragma unroll
      for (int t = 0; t < 2; ++t) {
        const float sk = rs1[t] * L2E;
        const v2u k0 = lnpk(qa[t][0], c0, nm1[t], sk), k1 = lnpk(qa[t][1], c1, nm1[t], sk), k2 = lnpk(qa[t][2], c2, nm1[t], sk);
        phi_mm(as8(v4u{k0.x, k0.y, k2.x, k2.y}), as8(v4u{k1.x, k1.y, k2.x, k2.y}), A, pp[t]);
      }
      v4u b0[2], b1[2], bx[2];
#pragma unroll
      for (int t = 0; t < 2; ++t) {
        pq[t] = phi_sm(pp[t]);
        b0[t] = v4u{pq[t].pl[0].x, pq[t].pl[0].y, pq[t].mi[0].x, pq[t].mi[0].y};
        b1[t] = v4u{pq[t].pl[1].x, pq[t].pl[1].y, pq[t].mi[1].x, pq[t].mi[1].y};
        bx[t] = v4u{pq[t].ex.x, pq[t].ex.y, 0u, 0u};
        OPQ(b0[t]); OPQ(b1[t]); OPQ(bx[t]);
      }
      u32 mo = (lane & 1) ? ~0u : 0u, ve = i16 * 16 + q * 4;
      OPQ(mo); OPQ(ve);
      STAMP(7);
      lds_wait<0>(S_KV);
      STAMP(8);
      v2u it[8];
#pragma unroll
      for (int n = 0; n < 8; ++n) it[n] = lld<v2u>(L_KV + n * 512 + lane * 8);
      const s8 ae = as8(v4u{it[4].x & ~mo, it[4].y & ~mo, it[5].x & ~mo, it[5].y & ~mo}), ao = as8(v4u{it[4].x & mo, it[4].y & mo, it[5].x & mo, it[5].y & mo});
      const s8 ax = as8(v4u{it[7].x & ~mo, it[7].y & mo, 0u, 0u});
      f4 nx[2], n0[2], n1[2];
#pragma unroll
      for (int t = 0; t < 2; ++t) {
        nx[t] = mfma16(ae, as8(b0[t]), z4);
        nx[t] = mfma16(ao, as8(b1[t]), nx[t]);
        nx[t] = mfma16(ax, as8(bx[t]), nx[t]);
      }
#pragma unroll
      for (int t = 0; t < 2; ++t) {
        n0[t] = mfma16(as8(v4u{it[0].x, it[0].y, it[1].x, it[1].y}), as8(b0[t]), z4);
        n1[t] = mfma16(as8(v4u{it[2].x, it[2].y, it[3].x, it[3].y}), as8(b1[t]), z4);
        n0[t] = mfma16(as8(v4u{it[6].x, 0u, 0u, 0u}), as8(bx[t]), n0[t]);
        n1[t] = mfma16(as8(v4u{0u, it[6].y, 0u, 0u}), as8(bx[t]), n1[t]);
      }
#pragma unroll
      for (int t = 0; t < 2; ++t) {
        const float r0 = __builtin_amdgcn_rcpf(nx[t][2] + 1e-6f), r1 = __builtin_amdgcn_rcpf(nx[t][3] + 1e-6f);
        const u32 ob = W_O + (team * 8 + 2 * w + t) * 20480;
        st4c(pk(nx[t][0] * r0, nx[t][1] * r1), ws, ve, ob + 16384 + rank * 256);
        st16c(v4u{pk(n0[t][0] * r0, n0[t][1] * r0), pk(n0[t][2] * r0, n0[t][3] * r0), pk(n1[t][0] * r1, n1[t][1] * r1), pk(n1[t][2] * r1, n1[t][3] * r1)}, ws,
              lane * 16, ob + rank * 1024);
      }
    }
    {
      const u32 lane = lane_id(), q = lane >> 4, i16 = lane & 15;
      const u32 tk = th * 64 + w * 16 + i16;
      const u32 xo = tk * 1280 + q * 16, xso = team * 128 * 1280 + hq * 160;
      const rsrc_t rx = mk_rsrc(x, SEQ * DM * 2);
      const v4u xr0 = __builtin_amdgcn_raw_buffer_load_b128(rx, xo, xso, 0), xr1 = __builtin_amdgcn_raw_buffer_load_b128(rx, xo + 64, xso, 0);
      const v2u xr2 = __builtin_amdgcn_raw_buffer_load_b64(rx, tk * 1280 + 128 + q * 8, xso, 0);
      constexpr int OD = 6;
      s8 wf[OD][5];
      u32 bo[2] = {L_WO + lane * 16, L_WO + 51200 + lane * 16};
      OPQ(bo[0]); OPQ(bo[1]);
      const u32 oso = W_O + (team * 8 + th * 4 + w) * 20480;
      auto ldo1 = [&](int ks, int t) LAM { wf[ks % OD][t] = lld<s8>(bo[ks / 10] + (ks % 10) * 5120 + t * 1024); };
#pragma unroll
      for (int ks = 0; ks < OD; ++ks)
#pragma unroll
        for (int t = 0; t < 5; ++t) ldo1(ks, t);
      WAITV(3);
      flag_store(sws, tag, W_FS2 + (bid * 4 + w) * 128);
      lds_inc(S_OP);
      STAMP(9);
      f4 ha[5] = {f4{blo(xr0.x), bhi(xr0.x), blo(xr0.y), bhi(xr0.y)}, f4{blo(xr0.z), bhi(xr0.z), blo(xr0.w), bhi(xr0.w)}, f4{blo(xr1.x), bhi(xr1.x), blo(xr1.y), bhi(xr1.y)},
                  f4{blo(xr1.z), bhi(xr1.z), blo(xr1.w), bhi(xr1.w)}, f4{blo(xr2.x), bhi(xr2.x), blo(xr2.y), bhi(xr2.y)}};
#pragma unroll
      for (int t = 0; t < 5; ++t) OPQ(ha[t]);
      __builtin_amdgcn_s_waitcnt(0xC07F);
      __builtin_amdgcn_s_sleep(PD2);
      SB();
      poll_flags(ws, W_FS2 + ((team * 16 + lane) * 4 + th * 2 + (w >> 1)) * 128, lane < 16, tag);
      STAMP(10);
      v4u of[20];
      constexpr int PD = 12;
      auto ldof = [&](int ks) LAM { of[ks] = ld16c(ws, lane * 16 + (ks & 3) * 1024, oso + (ks >> 2) * 4096); };
#pragma unroll
      for (int ks = 0; ks < PD; ++ks) ldof(ks);
      SB();
#pragma unroll
      for (int ks = 0; ks < 20; ++ks) {
#pragma unroll
        for (int t = 0; t < 5; ++t) {
          ha[t] = mfma16(wf[ks % OD][t], as8(of[ks]), ha[t]);
          if (t == 0 && ks + PD < 20) ldof(ks + PD);
          if (t == 1 && ks + PD == 19) lds_inc(S_OL);
          if (ks + OD < 20) ldo1(ks + OD, t);
          SB();
        }
      }
      STAMP(11);
      v4u hc[2];
#pragma unroll
      for (int m = 0; m < 2; ++m)
        hc[m] = v4u{pk(ha[2 * m][0], ha[2 * m][1]), pk(ha[2 * m][2], ha[2 * m][3]), pk(ha[2 * m + 1][0], ha[2 * m + 1][1]), pk(ha[2 * m + 1][2], ha[2 * m + 1][3])};
      const v2u ht = {pk(ha[4][0], ha[4][1]), pk(ha[4][2], ha[4][3])};
      const u32 hb = W_H + (team * 4 + th * 2 + (w >> 1)) * 40960 + hq * 5120;
      const u32 t32 = (w & 1) * 16 + i16;
      st16c(hc[0], ws, (q >> 1) * 1024 + (t32 + 32 * (q & 1)) * 16, hb);
      st16c(hc[1], ws, (q >> 1) * 1024 + (t32 + 32 * (q & 1)) * 16, hb + 2048);
      st8c(ht, ws, (t32 + 32 * (q >> 1)) * 16 + 8 * (q & 1), hb + 4096);
      float a[2] = {0.f, 0.f}, c[2] = {0.f, 0.f};
#pragma unroll
      for (int m = 0; m < 2; ++m) {
#pragma unroll
        for (int j = 0; j < 4; ++j) { a[m] = dot2(hc[m][j], 0x3F803F80u, a[m]); c[m] = dot2(hc[m][j], hc[m][j], c[m]); }
        a[m] = dot2(ht[m], 0x3F803F80u, a[m]); c[m] = dot2(ht[m], ht[m], c[m]);
      }
      float a2 = a[0] + a[1], c2 = c[0] + c[1];
      red2(a2, c2);
      st8c(v2u{fu(a2), fu(c2)}, ws, q == 0 ? tk * 8 : OOB, W_STAT + (team * 8 + hq) * 1024);
      lst<v4u>(L_HST + w * 2560 + lane * 16, hc[0]);
      lst<v4u>(L_HST + w * 2560 + 1024 + lane * 16, hc[1]);
      lst<v2u>(L_HST + w * 2560 + 2048 + lane * 8, ht);
    }
    WAITV(0);
    flag_store(sws, tag, W_FS3 + (bid * 4 + w) * 128);
    lds_inc(S_WOC);
    STAMP(12);
    __builtin_amdgcn_s_sleep(PD2);
    const u32 lane3 = lane_id();
#define lane lane3
    poll_flags(ws, W_FS3 + ((team * 16 + (w >> 1) * 8 + (lane >> 1)) * 4 + 2 * (w & 1) + (lane & 1)) * 128, lane < 16, tag);
    STAMP(13);
    v4u wl[3][5];
    u32 wlv;
    auto ldwl = [&](int k) LAM {
      const int i = 11 + k / 5;
      if (k == 0) wlv = lane_id() * 16;
      wl[k / 5][k % 5] = ld16c(ws, ((i >= 12 && w == 3) ? OOB : wlv) + (k % 5) * 1024, W_WD + (hq * 54 + 2 * w + (i >> 1) * 8 + (i & 1)) * 5120);
    };
    {
      const u32 hh = lane >> 5, t32 = lane & 31;
      v2u sv[4];
#pragma unroll
      for (int n = 0; n < 4; ++n) sv[n] = ld8c(ws, (hh * 4 + n) * 1024 + t32 * 8, W_STAT + team * 8192 + w * 256);
      v4u hf[40];
      constexpr int HD = HDEP;
      const u32 hso = W_H + (team * 4 + w) * 40960;
      auto ldh = [&](int s) LAM { hf[s] = ld16c(ws, lane * 16 + (s & 3) * 1024, hso + (s >> 2) * 4096); };
#pragma unroll
      for (int s = 0; s < HD; ++s) ldh(s);
      SB();
      lds_wait(L_LAND, 4);
      u32 lb[3] = {L_HS + lane * 16, L_HS + 3 * HSLOT + lane * 16, L_HS + 6 * HSLOT + lane * 16};
      OPQ(lb[0]); OPQ(lb[1]); OPQ(lb[2]);
      auto ldw = [&](int n, int s) LAM -> s8 {
        const int hs = (2 * n + s / 20 + 5) % 7;
        return lld<s8>(lb[hs / 3] + (hs % 3) * HSLOT + (s % 20) * 1024);
      };
      auto ldcs = [&](int n) LAM -> f16v {
        const u32 csb = L_CS3 + (lane_id() & 32);
        const f4 g0 = lld<f4>(csb + n * 128), g1 = lld<f4>(csb + n * 128 + 16), u0 = lld<f4>(csb + n * 128 + 64), u1 = lld<f4>(csb + n * 128 + 80);
        return __builtin_shufflevector(__builtin_shufflevector(g0, g1, 0, 1, 2, 3, 4, 5, 6, 7), __builtin_shufflevector(u0, u1, 0, 1, 2, 3, 4, 5, 6, 7), 0, 1, 2, 3, 4,
                                       5, 6, 7, 8, 9, 10, 11, 12, 13, 14, 15);
      };
      constexpr int NX = P1X, NA = 80 + NX, RD = RDEP;
      struct Sl { int n, s; };
      constexpr auto slot = [](int f) constexpr -> Sl {
        if (f >= NA) { const int r = f - NA + NX; return Sl{2 + r / 40, r % 40}; }
        int c = 0, x = 0;
        for (int s = 0; s < 40; ++s) {
          if (c == f) return Sl{0, s};
          if (c + 1 == f) return Sl{1, s};
          c += 2;
          if ((s + 1) * NX / 40 > x) { if (c == f) return Sl{2, x}; ++c; ++x; }
        }
        return Sl{0, 0};
      };
      f16v ac[3];
      ac[0] = ldcs(0); ac[1] = ldcs(1); ac[2] = ldcs(2);
      s8 rg[RD];
      sfor<0, RD>([&](auto fc) LAM { constexpr Sl t = slot(decltype(fc)::v); rg[decltype(fc)::v] = ldw(t.n, t.s); });
      SB();
      float nmean, cK1, cK2, cR;
      {
        const float a = (uf(sv[0].x) + uf(sv[1].x)) + (uf(sv[2].x) + uf(sv[3].x)), c = (uf(sv[0].y) + uf(sv[1].y)) + (uf(sv[2].y) + uf(sv[3].y));
        auto r = __builtin_amdgcn_permlane32_swap(fu(a), fu(a), false, false);
        auto r2 = __builtin_amdgcn_permlane32_swap(fu(c), fu(c), false, false);
        nmean = -(uf(r[0]) + uf(r[1])) * (1.0f / DM);
        cR = __builtin_fmaxf(__builtin_fmaf(-nmean, nmean, (uf(r2[0]) + uf(r2[1])) * (1.0f / DM)), 0.f) + 1e-5f;
        const float rstd = __builtin_amdgcn_rsqf(cR), k1 = -2.0f * 0.7978845608028654f * 1.4426950408889634f;
        cK1 = k1 * rstd; cK2 = cK1 * 0.044715f * rstd * rstd;
      }
#pragma unroll
      for (int j = 0; j < 16; ++j) { ac[0][j] *= nmean; ac[1][j] *= nmean; ac[2][j] *= nmean; }
      OPQ(cK1); OPQ(cK2); OPQ(cR);
      SB();
      u32 lv;
      u32 gvo = (t32 >> 4) * 54 * 1024 + ((t32 & 15) + 16 * hh) * 16 + (rank & 1) * 512;
      OPQ(gvo);
      const u32 gso = W_G + (team * 8 + 2 * w) * 54 * 1024 + (rank >> 1) * 1024;
      const u32 fso = W_FS4 + (bid * 4 + w) * 1024;
      float tv, au, pr, ov[2];
      v4u gp;
      auto gstep = [&](const f16v& a, int e) LAM {
        const int j = e / 3, m = e % 3;
        if (m == 0 && j < 8) { tv = a[j] * a[j]; tv = __builtin_fmaf(tv, cK2, cK1); tv = tv * a[j]; }
        if (m == 1) tv = __builtin_amdgcn_exp2f(tv);
        if ((m == 1 && j > 0) || e == 24) { ov[(j - 1) & 1] = au * pr; if (((j - 1) & 1) == 1) gp[(j - 1) >> 1] = pk(ov[0], ov[1]); }
        if (m == 2) { pr = __builtin_amdgcn_rcpf(__builtin_fmaf(tv, cR, cR)); au = a[j] * a[8 + j]; }
      };
      auto item = [&](auto vc) LAM {
        constexpr int v = decltype(vc)::v, u = v / 32, e = v % 32;
        if constexpr (e <= 24) gstep(ac[u % 3], e);
        if constexpr (e == 24 && u < 4) ac[u % 3] = ldcs(u + 3);
        if constexpr (e == 25) st16c(gp, ws, gvo, gso + u * 8192);
        if constexpr (e == 26 && u >= 2) { WAITV(1); flag_store(sws, tag, fso + u * 128); }
        if constexpr (e >= 28 && u < 4) {
#pragma unroll
          for (int j = 0; j < 4; ++j) ac[u % 3][4 * (e - 28) + j] *= nmean;
        }
      };
      constexpr auto first = [](int g) constexpr -> int {
        const int g3 = 40 - NX;
        if (g <= 0) return 0;
        if (g3 < 33 && g <= g3) return (g - 1) * 32 / (g3 - 1);
        int v = g3 < 33 ? 32 : 0, t = g3 < 33 ? g3 : 1;
        while (t < g) {
          const int u = v / 32, rdy = u < 2 ? 0 : g3 + (u - 2) * 40 + 1;
          if (v < 192 && t >= rdy) { ++v; while (v < 192 && (v % 32 == 27 || (v % 32 == 26 && v < 64) || (v % 32 > 27 && v >= 128))) ++v; }
          ++t;
        }
        return v;
      };
      sfor<0, 280>([&](auto fc) LAM {
        constexpr int f = decltype(fc)::v, g = f - NA;
        constexpr Sl t = slot(f), tn = slot(f + RD);
        ac[t.n % 3] = mfma32(rg[f % RD], as8(hf[t.s]), ac[t.n % 3]);
        if constexpr (f + RD < 280) rg[f % RD] = ldw(tn.n, tn.s);
        if constexpr (f < NA && t.n == 0 && t.s + HD < 40) ldh(t.s + HD);
        if constexpr (f < NA && t.n == 1 && t.s + HD == 39) lds_inc(S_HL);
        if constexpr (g >= 0) sfor<first(g), first(g + 1)>(item);
        if constexpr (f + RD == NA) { lds_inc(L_CONS + 4); STAMP(14); }
        if constexpr (f + RD > NA && (f + RD == 280 || tn.s == 0)) lds_inc(L_CONS + 4 * slot(f + RD - 1).n);
        if constexpr (f + RD + 8 < 280 && slot(f + RD + 8).n >= 2 && slot(f + RD + 8).s == 20) lv = *(volatile LAS u32*)(L_LAND + 4 * (slot(f + RD + 8).n - 1));
        if constexpr (f + RD + 1 < 280 && slot(f + RD + 1).n >= 2 && slot(f + RD + 1).s == 20) lds_check(lv, L_LAND + 4 * (slot(f + RD + 1).n - 1), 4);
        SB();
      });
      STAMP(15);
#pragma unroll
      for (int e = 0; e < 25; ++e) gstep(ac[0], e);
      st16c(gp, ws, u7 ? gvo : OOB, gso + 6 * 8192);
      if (WLP) WAITV(16); else WAITV(1);
      flag_store(sws, tag, fso + 6 * 128);
    }
#undef lane
    STAMP(30);
    {
      const u32 ln = lane_id(), vl = ln * 16;
      const u32 fv = W_FS4 + ((team * 16 + 4 * w + (ln >> 4)) * 4 + 2 * th + ((ln >> 3) & 1)) * 1024;
      const u32 gso = W_G + (team * 8 + th * 4) * 54 * 1024 + 2 * w * 1024;
      const u32 vz = w < 3 ? vl : OOB;
      u32 gsr[4];
#pragma unroll
      for (int r = 0; r < 4; ++r) gsr[r] = sgpr(gso + ((r + w + 1) & 3) * (54 * 1024));
      v4u wf[6][5], gf[6][4];
      auto ldg = [&](int i) LAM {
#pragma unroll
        for (int r = 0; r < 4; ++r) gf[i - 8][r] = ld16c(ws, i >= 12 ? vz : vl, gsr[r] + ((i >> 1) * 8 + (i & 1)) * 1024);
        SB();
      };
      v2u fl = ld8c(ws, fv, 5 * 128);
      if (!WLP) sfor<0, 5>([&](auto kc) LAM { ldwl(decltype(kc)::v); });
      if (WLP) WAITV(2); else WAITV(7);
      flag_store(sws, tag, W_FS4 + (bid * 4 + w) * 1024 + 7 * 128);
      STAMP(16);
      if (!wall(fl.x == tag.x && fl.y == tag.y)) poll_level(ws, fv, tag, 5);
      asm volatile("" ::: "memory");
      ldg(8); ldg(9);
      fl = ld8c(ws, fv, 6 * 128);
      if (!WLP) sfor<5, 15>([&](auto kc) LAM { ldwl(decltype(kc)::v); });
#pragma unroll
      for (int i = 11; i < 14; ++i)
#pragma unroll
        for (int t = 0; t < 5; ++t) wf[i - 8][t] = wl[i - 11][t];
      u32 sb = L_HS + 2 * HSLOT + w * 5120 + vl;
      OPQ(sb);
      lds_wait(S_SP);
#pragma unroll
      for (int i = 8; i < 10; ++i)
#pragma unroll
        for (int t = 0; t < 5; ++t) wf[i - 8][t] = lld<v4u>(sb + (i - 8) * HSLOT + t * 1024);
      if (!wall(fl.x == tag.x && fl.y == tag.y)) poll_level(ws, fv, tag, 6);
      asm volatile("" ::: "memory");
      STAMP(31);
      ldg(10);
      f4 da[4][5];
      auto step = [&](int i) LAM {
#pragma unroll
        for (int m = 0; m < 20; ++m) da[m / 5][m % 5] = mfma16(as8(wf[i - 8][m % 5]), as8(gf[i - 8][m / 5]), i == 8 ? z4 : da[m / 5][m % 5]);
        SB();
      };
      step(8);
      ldg(11);
      fl = ld8c(ws, fv, 7 * 128);
#pragma unroll
      for (int t = 0; t < 5; ++t) wf[2][t] = lld<v4u>(sb + 2 * HSLOT + t * 1024);
      SB();
      step(9);
      if (w < 3 && !wall(fl.x == tag.x && fl.y == tag.y)) poll_level(ws, fv, tag, 7);
      asm volatile("" ::: "memory");
      STAMP(1);
      ldg(12); ldg(13);
      step(10); step(11);
      STAMP(6);
      step(12);
#pragma unroll
      for (int m = 0; m < 21; ++m) {
        if (m < 20) da[m / 5][m % 5] = mfma16(as8(wf[5][m % 5]), as8(gf[5][m / 5]), da[m / 5][m % 5]);
        if (m >= 6) lst<f4>(sb + ((m - 6) / 5) * HSLOT + ((m - 6) % 5) * 1024, da[(m - 6) / 5][(m - 6) % 5]);
        SB();
      }
      u32 rb[4];
#pragma unroll
      for (int r = 0; r < 3; ++r) { rb[r] = L_HS + (2 + r) * HSLOT + ((w - r - 1) & 3) * 5120 + vl; OPQ(rb[r]); }
      rb[3] = L_HS + ((w + 5) % 7) * HSLOT + vl;
      OPQ(rb[3]);
      const rsrc_t ro = mk_rsrc(out, SEQ * DM * 2);
      const u32 oso = (team * 128 + th * 64 + w * 16) * 1280 + hq * 160;
      u32 ovo = (ln & 15) * 1280 + (ln >> 4) * 16, ov2 = (ln & 15) * 1280 + 128 + (ln >> 4) * 8;
      OPQ(ovo); OPQ(ov2);
      bar();
      STAMP(17);
      f4 r[5][4], rr[5];
#pragma unroll
      for (int n = 0; n < 5; ++n)
#pragma unroll
        for (int j = 0; j < 4; ++j) r[n][j] = lld<f4>(rb[j] + n * 1024);
      SB();
#pragma unroll
      for (int n = 0; n < 5; ++n) rr[n] = ((da[3][n] + r[n][0]) + (r[n][1] + r[n][2])) + r[n][3];
#pragma unroll
      for (int m = 0; m < 2; ++m)
        st16p(v4u{pk(rr[2 * m][0], rr[2 * m][1]), pk(rr[2 * m][2], rr[2 * m][3]), pk(rr[2 * m + 1][0], rr[2 * m + 1][1]), pk(rr[2 * m + 1][2], rr[2 * m + 1][3])}, ro,
              ovo + 64 * m, oso);
      __builtin_amdgcn_raw_buffer_store_b64(v2u{pk(rr[4][0], rr[4][1]), pk(rr[4][2], rr[4][3])}, ro, ov2, oso, 0);
      if (b == 0 && w == 0) flag_store(sws, tag, W_EPOCH);
    }
    STAMP(18);
    DUMP();
  } else {
    const u32 j = wv;
    __builtin_amdgcn_s_setprio(HPRIO);
    const u32 lane = lane_id();
    const u32 q = lane >> 4, i16 = lane & 15, tq = (lane >> 2) & 3, tp = lane & 3, vl = lane * 16;
    const u32 p1 = j * 256 + b;
    const bool isq = b < 150;
    const u32 pc5 = (b * 52429u) >> 18, pcb1 = isq ? pc5 : pc5 - 30, pks = b < 200 ? (b - pc5 * 5) * 4 + j : 20;
    u32 pv0, ps0, psn, pl0 = L_A + j * 4096;
    {
      const u32 jr = lane >> 3;
      const bool ex = pks >= 16;
      const u32 ra = 20 * (jr & 1) + 16 + (jr >> 1), rb = 20 * (jr >> 2) + (jr & 3);
      const u32 m1 = isq ? 3840 : 1280, m2 = isq ? 8 : ex ? 40 : 4, m3 = isq ? 32 : ex ? 160 : 40, m4 = (!isq && ex) ? 2560 : 0;
      pv0 = (isq ? jr : ex ? ra : rb) * m1 + (lane & 7) * 16;
      psn = m2 * m1;
      ps0 = (m3 * pks - m4) * m1 + pcb1 * 128;
    }
    OPS(ps0); OPS(psn); OPS(pl0); OPQ(pv0);
    if (lane < 32) lst<u32>(L_SYNC + lane * 4, 0u);
    GET_ARGS();
    {
      const v4u sp = mk_srd((const void*)(isq ? ka[1] : ka[3]), pks >= 20 ? 0u : isq ? DM * 3 * DM * 2 : DM * DM * 2);
#pragma unroll
      for (int n = 0; n < 4; ++n) { if (NT & 1) dma16n(pv0, sp, ps0 + n * psn, pl0 + n * 1024); else dma16(pv0, sp, ps0 + n * psn, pl0 + n * 1024); }
    }
    bar();
    u64 ep;
    if (j == 3) {
      const v4u sp = mk_srd((const void*)ka[2], 32 * 400 * 2);
      dma16(vl, sp, rank * 1600, L_WPHI); dma16(vl, sp, rank * 1600 + 1024, L_WPHI + 1024);
    }
    u32 selv = i16 == q ? 0x3F803F80u : 0u;
    OPQ(selv);
    const s8 sel = as8(v4u{selv, selv, selv, selv});
    const u32 pi4 = ((tp & 1) << 1) | (tp >> 1);
    {
      u32 dv, cv;
      if (isq) {
        const u32 sec = (pcb1 * 26) >> 8, cn = (pcb1 - sec * 10) * 64 + lane, head = (cn * 3277) >> 16, d = cn - head * 20, hh = head & 1;
        const u32 par = pks & 1;
        const u32 tb = sec == 0 ? par * 2048 : sec == 1 ? 0 : 61440 - pks * 1024, xb = sec == 0 ? 4096 - 256 + par * 128 : 2048 - 256 - 128 + sec * 128;
        const u32 ds = W_W1 + (sec == 0 ? 102400 + (pks >> 1) * 5120 : pks * 3072);
        dv = (head >> 1) * 153600 + d * 16 + (d < 16 ? tb + hh * 1024 : xb + hh * 64) + ds;
        cv = i16 < 4 ? W_CS1 + pks * 7680 + pcb1 * 256 + i16 * 64 + q * 16 : OOB;
      } else {
        const u32 sgi = 4 * pcb1 + q, cc = (sgi * 52) >> 8, pos = sgi - cc * 5;
        const u32 o1 = ((pos & 2) + (i16 >> 3)) * 1024 + (8 * (pos & 1) + (i16 & 7)) * 16, o2 = 4096 + swap23(i16) * 16;
        dv = pks >= 20 ? OOB : cc * 102400 + (pos < 4 ? o1 : o2) + W_WO + pks * 5120;
        cv = OOB;
      }
      u32 rb = L_A + j * 4096 + tq * 128 + q * 32 + (isq ? tp : pi4) * 8;
      OPQ(dv); OPQ(rb); OPQ(cv);
      WAITV(0);
      v4u f[4];
#pragma unroll
      for (int n = 0; n < 4; ++n) f[n] = __builtin_bit_cast(v4u, cat(trd(rb + n * 1024), trd(rb + n * 1024 + 512)));
      f4 cs = z4;
#pragma unroll
      for (int n = 0; n < 4; ++n) { st16c(f[n], ws, dv + n * 256, 0); cs = mfma16(as8(f[n]), sel, cs); }
      st16c(__builtin_bit_cast(v4u, cs), ws, cv, 0);
      lds_inc(S_ST);
      asm volatile("s_load_dwordx2 %0, %1, 0x0" : "=s"(ep) : "s"(wsp));
      SB();
    }
    STAMP(1);
    GET_EPOCH();
    WAITV(0);
    lds_inc(S_PST);
    if (j == 0) { lds_wait<0>(S_PST); flag_store(sws, tag, W_FA + b * FAS); }
    STAMP(9);
    {
      v4u sent = {SENT, SENT, SENT, SENT};
      OPQ(sent);
      st16c(sent, ws, vl, W_PART + (rank * 16 + team) * 8192 + j * 2048);
      st16c(sent, ws, vl, W_PART + (rank * 16 + team) * 8192 + j * 2048 + 1024);
      st16c(sent, ws, lane < 4 ? vl : OOB, W_RED + rank * 4096 + team * 256 + j * 64);
    }
    if (j == 3) {
      v2u pl[2][4];
      const int src = ((lane & ~3u) | tq) * 4;
#pragma unroll
      for (int h = 0; h < 2; ++h) {
        const u32 pb = L_WPHI + h * 800;
        pl[h][0] = __builtin_bit_cast(v2u, trd(pb + (4 * q + tq) * 40 + 8 * tp)); pl[h][1] = __builtin_bit_cast(v2u, trd(pb + (16 + tq) * 40 + 8 * tp));
        pl[h][2] = __builtin_bit_cast(v2u, trd(pb + (4 * q + tq) * 40 + 32)); pl[h][3] = __builtin_bit_cast(v2u, trd(pb + (16 + tq) * 40 + 32));
      }
      asm volatile("" ::: "memory");
#pragma unroll
      for (int h = 0; h < 2; ++h) {
        const u32 qh = q == (u32)h ? ~0u : 0u, rh = tp == (u32)h ? ~0u : 0u;
        lst<v4u>(L_WPHI + h * 1024 + vl, v4u{pl[h][0].x, pl[h][0].y, pl[h][1].x & qh, pl[h][1].y & qh});
        lst<v4u>(L_PH2 + h * 1024 + vl, v4u{(u32)__builtin_amdgcn_ds_bpermute(src, pl[h][2].x) & rh, (u32)__builtin_amdgcn_ds_bpermute(src, pl[h][2].y) & rh,
                                            (u32)__builtin_amdgcn_ds_bpermute(src, pl[h][3].x) & rh & qh, (u32)__builtin_amdgcn_ds_bpermute(src, pl[h][3].y) & rh & qh});
      }
    }
    u32 rdv[2], rdn[2], rcv[2], rvo[2], rs0[2], rsn[2];
    v4u rsp[2];
#pragma unroll
    for (int e = 0; e < 2; ++e) {
      const u32 piece = p1 + e * 1024;
      const bool isg = piece < 1080;
      const u32 p2 = isg ? piece : piece - 1080;
      const u32 pk_ = (p2 * (isg ? 1214u : 6554u)) >> 16, pcb = p2 - pk_ * (isg ? 54 : 10);
      const u32 mat = pcb >= 27 ? 1 : 0, rs = isg ? 3456 : 1280, u0 = 4 * (pcb - mat * 27);
      rsp[e] = mk_srd((const void*)(!isg ? ka[6] : mat ? ka[5] : ka[4]), piece < 1620 ? DM * FF * 2 : 0u);
      rs0[e] = pk_ * 32 * rs + u0 * 32; rsn[e] = 8 * rs; rvo[e] = (lane >> 3) * rs + (lane & 7) * 16;
      const u32 sgi = u0 + q, cc = (sgi * 52) >> 8, pos = sgi - cc * 5;
      const u32 o1 = ((pos & 2) + (i16 >> 3)) * 1024 + (8 * (pos & 1) + (i16 & 7)) * 16, o2 = 4096 + swap23(i16) * 16;
      const u32 dg = W_WGU + sgi * 40960 + i16 * 16 + pk_ * 2048 + mat * 256, dd = W_WD + cc * (54 * 5120) + (pos < 4 ? o1 : o2) + pk_ * 5120;
      rdv[e] = isg ? dg : piece < 1620 ? dd : OOB;
      rdn[e] = isg ? 512 : 256;
      rcv[e] = (isg && i16 < 4) ? W_CSP + (pk_ * 108 + u0 + i16) * 128 + mat * 64 + (((q & 1) << 1) | (q >> 1)) * 16 : OOB;
      OPQ(rdv[e]); OPQ(rcv[e]); OPQ(rvo[e]); OPS(rs0[e]); OPS(rsn[e]); OPS(rdn[e]);
      rsp[e].x = sgpr(rsp[e].x); rsp[e].y = sgpr(rsp[e].y); rsp[e].z = sgpr(rsp[e].z);
    }
    u32 fav, fcv = (j == 3 && lane < 16) ? (lane * 16 + rank) * 128 : OOB;
    {
      const u32 g = (lane * 52) >> 8, i5 = lane - g * 5;
      const u32 cq = (g >> 1) * 10 + ((40 * rank + (g & 1) * 39) >> 6), co = 30 + ((80 * hq + (g & 1) * 79) >> 6);
      fav = lane >= 40 ? OOB : ((g < 6 ? cq : co) * 5 + i5) * FAS;
      OPQ(fav); OPQ(fcv);
    }
    u32 c1v;
    { const u32 t = j * 30 + (lane & 31), sec = (t * 1639) >> 16; c1v = (lane & 31) < 30 ? (sec * 600 + t) * 4 + (lane >> 5) * (10 * 7680) : OOB; OPQ(c1v); }
    v4u rf[10];
    auto rst = [&](int k) LAM { if (k % 5 < 4) st16c(rf[k], ws, rdv[k / 5], (k % 5) * rdn[k / 5]); else st16c(rf[k], ws, rcv[k / 5], 0); };
    STAMP(23);
    if (j == 0) { __builtin_amdgcn_s_sleep(FAD); poll_mask<FAR>(ws, fav, W_FA, tag, ld8c(ws, fav, W_FA)); }
    WAITV(0);
    lds_inc(S_FA);
    lds_wait<1>(S_FA);
    STAMP(24);
    {
      u32 w1s = W_W1 + rank * 153600 + j * 5120, w1l = L_A + j * 5120;
      OPS(w1s); OPS(w1l);
      auto dd = [&](int m) LAM { dma5c(vl, sws, w1s + m * 20480, w1l + m * 20480); };
      u32 cv[10];
      dd(0); dd(1);
      WAITV(5); lds_inc(S_L1);
      dd(2);
#pragma unroll
      for (int k = 0; k < 10; ++k) asm volatile("buffer_load_dword %0, %1, %2, %3 offen sc1" : "=v"(cv[k]) : "v"(c1v), "s"(sws), "s"(W_CS1 + k * 7680 + rank * 160) : "memory");
      WAITV(15); lds_inc(S_L1 + 4);
      dd(3);
      WAITV(15); lds_inc(S_L1 + 8);
      dd(4);
      WAITV(10);
      SB();
      asm volatile("; cv landed" : "+v"(cv[0]), "+v"(cv[1]), "+v"(cv[2]), "+v"(cv[3]), "+v"(cv[4]), "+v"(cv[5]), "+v"(cv[6]), "+v"(cv[7]), "+v"(cv[8]), "+v"(cv[9]));
      {
        const float c = (((uf(cv[0]) + uf(cv[1])) + (uf(cv[2]) + uf(cv[3]))) + ((uf(cv[4]) + uf(cv[5])) + (uf(cv[6]) + uf(cv[7])))) + (uf(cv[8]) + uf(cv[9]));
        auto r = __builtin_amdgcn_permlane32_swap(fu(c), fu(c), false, false);
        if (lane < 30) lst<float>(L_CS1 + j * 120 + lane * 4, uf(r[0]) + uf(r[1]));
      }
      lds_inc(S_CS);
      WAITV(5); lds_inc(S_L1 + 12);
      v4u rt[2][4];
#pragma unroll
      for (int e = 0; e < 2; ++e) {
        const rsrc_t sp = mk_rsrc((const void*)(((u64)rsp[e].y << 32) | rsp[e].x), rsp[e].z);
#pragma unroll
        for (int n = 0; n < 4; ++n) rt[e][n] = __builtin_amdgcn_raw_buffer_load_b128(sp, rvo[e], rs0[e] + n * rsn[e], (NT & 2) ? 2 : 0);
      }
      WAITV(8); lds_inc(S_L1 + 16);
      const u32 rb = L_KV + j * 1024 + tq * 128 + q * 32 + pi4 * 8;
#pragma unroll
      for (int e = 0; e < 2; ++e) {
        f4 cs = z4;
#pragma unroll
        for (int n = 0; n < 4; ++n) {
          lst<v4u>(L_KV + j * 1024 + vl, rt[e][n]);
          asm volatile("" ::: "memory");
          rf[5 * e + n] = __builtin_bit_cast(v4u, cat(trd(rb), trd(rb + 512)));
          asm volatile("" ::: "memory");
        }
#pragma unroll
        for (int n = 0; n < 4; ++n) cs = mfma16(as8(rf[5 * e + n]), sel, cs);
        rf[5 * e + 4] = __builtin_bit_cast(v4u, cs);
        OPQ(rf[5 * e + 4]);
      }
      lds_wait(S_XD);
      rst(0); dd(5); rst(1); rst(2); dd(6); rst(3); rst(4);
      if (j < 2) dd(7);
      rst(5);
    }
    STAMP(5);
    STAMP(25);
    u32 wos = W_WO + hq * 102400;
    OPS(wos);
    v4u wf[11][5], gf[8][4];
    const u32 wso = W_WD + (hq * 54 + 2 * j) * 5120;
    const u32 gso = W_G + (team * 8 + th * 4) * 54 * 1024 + 2 * j * 1024;
    auto ldw1 = [&](int i, int t) LAM { wf[i][t] = ld16c(ws, vl + t * 1024, wso + ((i >> 1) * 8 + (i & 1)) * 5120); };
    auto ldg1 = [&](int i, int r) LAM { gf[i][r] = ld16c(ws, vl, gso + r * 54 * 1024 + ((i >> 1) * 8 + (i & 1)) * 1024); };
    STAMP(2);
    lds_wait<2>(S_KVD);
    rst(6);
    dma5c(vl, sws, wos + (8 + j) * 5120, L_WO + (8 + j) * 5120);
    rst(7); rst(8); rst(9);
    const v2u fc = ld8c(ws, fcv, W_FC);
    WAITV(1);
    lds_inc(S_ST);
    if (j == 1) { lds_wait(S_ST, 8); flag_store(sws, tag, W_FB + bid * 32); }
    STAMP(3);
    kv_job<true>(ws, j + 4, W_PART + (rank * 16 + team) * 8192);
    STAMP(4);
    bar_nw();
    if (j < 3) {
#pragma unroll 1
      for (u32 k = j; k < 16; k += 3) { const u32 ks = k < 8 ? k : k + 4; dma5c(vl, sws, wos + ks * 5120, L_WO + ks * 5120); }
    }
    if (j == 3) {
      const u32 pvo = (lane >> 5) * 8 * 8192 + (lane & 31) * 16, pso = W_PART + rank * 16 * 8192 + team * 512;
      v4u pv[8];
      __builtin_amdgcn_s_setprio(3);
      poll_mask<2>(ws, fcv, W_FC, tag, fc);
      __builtin_amdgcn_s_sleep(HOPD);
#pragma unroll 1
      for (u32 s = 0; s < SPIN_CAP; ++s) {
#pragma unroll
        for (int n = 0; n < 8; ++n) pv[n] = ld16c(ws, pvo, pso + n * 8192);
        SB();
        u32 mx = 0;
#pragma unroll
        for (int n = 0; n < 8; ++n) mx = umax4(mx, pv[n]);
        if (wall(mx != SENT)) break;
        asm volatile("" ::: "memory");
      }
      float sm[4];
#pragma unroll
      for (int k = 0; k < 4; ++k) {
        const float a = ((uf(pv[0][k]) + uf(pv[1][k])) + (uf(pv[2][k]) + uf(pv[3][k]))) + ((uf(pv[4][k]) + uf(pv[5][k])) + (uf(pv[6][k]) + uf(pv[7][k])));
        auto r = __builtin_amdgcn_permlane32_swap(fu(a), fu(a), false, false);
        sm[k] = uf(r[0]) + uf(r[1]);
      }
      st8c(v2u{san(pk(sm[0], sm[1])), san(pk(sm[2], sm[3]))}, ws, lane < 32 ? lane * 8 : OOB, W_RED + rank * 4096 + team * 256);
      STAMP(6);
      __builtin_amdgcn_s_sleep(HOPE);
      v4u kv[4];
#pragma unroll 1
      for (u32 s = 0; s < SPIN_CAP; ++s) {
#pragma unroll
        for (int n = 0; n < 4; ++n) kv[n] = ld16c(ws, vl, W_RED + rank * 4096 + n * 1024);
        SB();
        u32 mx = 0;
#pragma unroll
        for (int n = 0; n < 4; ++n) mx = umax4(mx, kv[n]);
        if (wall(mx != SENT)) break;
        asm volatile("" ::: "memory");
      }
#pragma unroll
      for (int n = 0; n < 4; ++n) lst<v4u>(L_KV + n * 1024 + vl, kv[n]);
      __builtin_amdgcn_s_setprio(HPRIO);
    }
    WAITV(0);
    lds_inc(S_KV);
    STAMP(7);
    if (j == 2) { __builtin_amdgcn_s_setprio(3); __builtin_amdgcn_s_sleep(FBD); poll256(ws, W_FB, 32, lane, tag); lds_inc(S_FB); __builtin_amdgcn_s_setprio(HPRIO); }
    lds_wait(S_FB, 1);
    STAMP(22);
    if (j == 3) lds_wait(S_OP);
    sfor<0, 5 * NW1>([&](auto ic) LAM { ldw1(decltype(ic)::v / 5, decltype(ic)::v % 5); });
    SB();
    auto stage_u = [&](u32 hs) LAM {
      const u32 so = W_WGU + (16 * (hs >> 1) + rank) * 40960 + ((hs & 1) * 4 + j) * 5120, dst = L_HS + ((hs + 5) % 7) * HSLOT + j * 5120;
      if (hs >= 7) waitv<PACE>();
      if (!(ABL & 8) || hs < 7) dma5c(vl, sws, so, dst);
    };
    lds_wait(S_OP);
    stage_u(0); stage_u(1);
    {
      const u32 t = j * 64 + lane, u = 16 * (t >> 5) + rank;
      if (u < 108) {
        u32 cv[20];
#pragma unroll
        for (int k = 0; k < 20; ++k) cv[k] = ld4c(ws, u * 128 + (t & 31) * 4, W_CSP + k * 108 * 128);
        SB();
        float c = 0.f;
#pragma unroll
        for (int k = 0; k < 20; ++k) c += uf(cv[k]);
        lst<float>(L_CS3 + t * 4, c);
      }
    }
    u32 sdb[3] = {L_HS + j * 5120 + vl, L_HS + 3 * HSLOT + j * 5120 + vl, L_HS + 6 * HSLOT + j * 5120 + vl};
    OPQ(sdb[0]); OPQ(sdb[1]); OPQ(sdb[2]);
    auto spill = [&](int i) LAM {
#pragma unroll
      for (int t = 0; t < 5; ++t) lst<v4u>(sdb[((i + 1) % 7) / 3] + (((i + 1) % 7) % 3) * HSLOT + t * 1024, wf[i][t]);
      SB();
    };
    auto ldw = [&](auto lo, auto hi) LAM {
      sfor<decltype(lo)::v * 5, decltype(hi)::v * 5>([&](auto ic) LAM { if (decltype(ic)::v % 5 == 0) waitv<PACE>(); ldw1(decltype(ic)::v / 5, decltype(ic)::v % 5); });
      SB();
    };
    auto ldg = [&](int i) LAM {
      waitv<PACE>();
#pragma unroll
      for (int r = 0; r < 4; ++r) ldg1(i, r);
      SB();
    };
    STAMP(10);
    lds_wait(S_OL);
    ldw(IC<NW1>{}, IC<NW2>{});
    lds_wait<2>(S_WOC);
    STAMP(11);
    auto landed = [&](int k) LAM { lds_inc(L_LAND + 4 * k); };
    stage_u(2); stage_u(3); stage_u(4);
    WAITV(0); landed(0);
    STAMP(13);
    stage_u(5); stage_u(6);
    WAITV(0); landed(1);
    lds_wait<2>(S_HL);
    ldw(IC<NW2>{}, IC<NW3>{});
    lds_wait<1>(L_CONS + 4);
    stage_u(7); stage_u(8); stage_u(9);
    WAITV(5); landed(2);
    stage_u(10);
    ldw(IC<NW3>{}, IC<9>{});
    waitv<5 * (9 - NW3)>(); landed(3);
    lds_wait<2>(L_CONS + 8);
    stage_u(11);
    if (u7) stage_u(12);
    ldw(IC<9>{}, IC<11>{});
    WAITV(10); landed(4);
    const u32 fvo = W_FS4 + ((team * 16 + 4 * j + (lane >> 4)) * 4 + 2 * th + ((lane >> 3) & 1)) * 1024;
    lds_wait<2>(L_CONS + 12);
    if (u7) stage_u(13);
    spill(4);
    WAITV(0); landed(5);
    STAMP(26);
    lds_wait<2>(L_CONS + 16);
    STAMP(27);
    v2u fl = ld8c(ws, fvo, 2 * 128);
    spill(5); spill(6);
    if (!wall(fl.x == tag.x && fl.y == tag.y)) poll_level(ws, fvo, tag, 2);
    asm volatile("" ::: "memory");
    ldg(0); ldg(1); ldg(2);
    lds_wait<2>(L_CONS + 20);
    STAMP(28);
    fl = ld8c(ws, fvo, 3 * 128);
    spill(7); spill(8); ldg(3);
    if (!wall(fl.x == tag.x && fl.y == tag.y)) poll_level(ws, fvo, tag, 3);
    asm volatile("" ::: "memory");
    STAMP(29);
    __builtin_amdgcn_s_setprio(P4PRIO);
    lds_wait<1>(L_CONS + 24);
    STAMP(16);
    fl = ld8c(ws, fvo, 4 * 128);
    spill(9); spill(10);
    lds_inc(S_SP);
    f4 da[4][5];
    sfor<0, 8>([&](auto ic) LAM {
      constexpr int i = decltype(ic)::v;
#pragma unroll
      for (int m = 0; m < 20; ++m) {
        const int r = m / 5, t = m % 5;
        da[r][t] = mfma16(as8(wf[i][t]), as8(gf[i][r]), i == 0 ? z4 : da[r][t]);
        if constexpr (i <= 3) { if (t == 4) ldg1(i + 4, r); }
        if constexpr (i >= 3 && i <= 6) { if (m >= 12 && m < 17) wf[i + 1][m - 12] = lld<v4u>(sdb[((i + 2) % 7) / 3] + (((i + 2) % 7) % 3) * HSLOT + (m - 12) * 1024); }
        if constexpr (i == 7) { if (m >= 6) lst<f4>(sdb[(((m - 6) / 5 + 5) % 7) / 3] + ((((m - 6) / 5 + 5) % 7) % 3) * HSLOT + ((m - 6) % 5) * 1024, da[(m - 6) / 5][(m - 6) % 5]); }
        SB();
      }
      if constexpr (i == 1) {
        if (!wall(fl.x == tag.x && fl.y == tag.y)) poll_level(ws, fvo, tag, 4);
        asm volatile("" ::: "memory");
      }
      SB();
    });
    STAMP(18);
#pragma unroll
    for (int m = 14; m < 20; ++m) lst<f4>(sdb[((m / 5 + 5) % 7) / 3] + (((m / 5 + 5) % 7) % 3) * HSLOT + (m % 5) * 1024, da[m / 5][m % 5]);
    lds_inc(S_HP);
    __builtin_amdgcn_s_setprio(0);
    lds_wait(S_HP);
    {
      const u32 pb = L_HS + ((j + 5) % 7) * HSLOT + vl;
      f4 r[5][4];
#pragma unroll
      for (int n = 0; n < 5; ++n)
#pragma unroll
        for (int k = 0; k < 4; ++k) r[n][k] = lld<f4>(pb + k * 5120 + n * 1024);
      const v4u h0 = lld<v4u>(L_HST + j * 2560 + vl), h1 = lld<v4u>(L_HST + j * 2560 + 1024 + vl);
      const v2u h2 = lld<v2u>(L_HST + j * 2560 + 2048 + lane * 8);
      const u32 hw[10] = {h0.x, h0.y, h0.z, h0.w, h1.x, h1.y, h1.z, h1.w, h2.x, h2.y};
#pragma unroll
      for (int n = 0; n < 5; ++n)
        lst<f4>(pb + n * 1024, ((r[n][0] + r[n][1]) + (r[n][2] + r[n][3])) + f4{blo(hw[2 * n]), bhi(hw[2 * n]), blo(hw[2 * n + 1]), bhi(hw[2 * n + 1])});
    }
    bar();
    DUMP();
  }
}

extern "C" void kernel_launch(void* const* d_in, const int* in_sizes, int n_in, void* d_out, int out_size, void* d_ws, size_t ws_size,
                              hipStream_t stream) {
  (void)hipFuncSetAttribute((const void*)fused_block, hipFuncAttributeMaxDynamicSharedMemorySize, L_BYTES);
  fused_block<<<256, 512, L_BYTES, stream>>>((const u16*)d_in[0], (const u16*)d_in[1], (const u16*)d_in[2], (const u16*)d_in[3],
                                             (const u16*)d_in[5], (const u16*)d_in[6], (const u16*)d_in[7], (u16*)d_out, (char*)d_ws);
}
```
